```python
import torch

def _softmax(x, axis=-1):
    return torch.softmax(x, dim=axis)
def _gelu(x):
    return torch.nn.functional.gelu(x, approximate="tanh")
def _silu(x):
    return torch.nn.functional.silu(x)
def _sigmoid(x):
    return torch.sigmoid(x)
def _layernorm(x, axis=-1, eps=1e-5):
    m = x.mean(dim=axis, keepdim=True)
    v = x.var(dim=axis, keepdim=True, unbiased=False)
    return (x - m) / torch.sqrt(v + eps)
def _scatter_add(tgt, idx, src, axis=0):
    return tgt.clone().index_add_(axis, idx, src)
def _segment_sum(src, seg, n):
    out = torch.zeros((n,) + src.shape[1:], dtype=src.dtype)
    return out.index_add_(0, seg, src)
def _pad(x, pw):
    flat = tuple(v for pair in reversed(pw) for v in pair)
    return torch.nn.functional.pad(x, flat)

kernel_name = 'recurrent_scan__linear_kernel_hedgehog__dense_activation_squared_relu__prompt'

def setup_inputs():

    _w = (lambda a: (lambda t: t.to(torch.bfloat16) if t.is_floating_point() else t)(torch.as_tensor(a)))
    def _A_setup():
        B, S, D, H = 1, 2048, 512, 32
        Dh = D // H
        return {
            "x": _w((lambda *s: torch.randn(*s, dtype=torch.bfloat16) * 0.05)(B, S, D)),
            "wqkv": _w((lambda *s: torch.randn(*s, dtype=torch.bfloat16) * 0.2)(D, 3 * D)),
            "w_phi": _w((lambda *s: torch.randn(*s, dtype=torch.bfloat16) * 0.2)(H, Dh, Dh) * (Dh ** -0.5)),
            "wo": _w((lambda *s: torch.randn(*s, dtype=torch.bfloat16) * 0.05)(D, D)),
        }
    _A_in = _A_setup()

    def _B_setup():
        B, S, D, F = 1, 2048, 512, 1792
        return {
            "x": _w((lambda *s: torch.randn(*s, dtype=torch.bfloat16) * 0.05)(B, S, D)),
            "w1": _w((lambda *s: torch.randn(*s, dtype=torch.bfloat16) * 0.05)(D, F)),
            "b1": _w((lambda *s: torch.randn(*s, dtype=torch.bfloat16) * 0.2)(F)),
            "w2": _w((lambda *s: torch.randn(*s, dtype=torch.bfloat16) * 0.05)(F, D)),
            "b2": _w((lambda *s: torch.randn(*s, dtype=torch.bfloat16) * 0.8)(D)),
        }
    _B_in = _B_setup()

    return {
        "A_x": _A_in["x"],
        "A_wqkv": _A_in["wqkv"],
        "A_w_phi": _A_in["w_phi"],
        "A_wo": _A_in["wo"],
        "B_x": _B_in["x"],
        "B_w1": _B_in["w1"],
        "B_b1": _B_in["b1"],
        "B_w2": _B_in["w2"],
        "B_b2": _B_in["b2"],
    }

def reference(A_x, A_wqkv, A_w_phi, A_wo, B_x, B_w1, B_b1, B_w2, B_b2):
    def _A_ref(x, wqkv, w_phi, wo):
        B, S, D = x.shape
        H = 32; Dh = D // H
        qkv = (lambda x, *d: x.permute(*d))((x @ wqkv).reshape(B, S, 3, H, Dh), 2, 0, 3, 1, 4)
        q, k, v = qkv[0], qkv[1], qkv[2]
        pq = torch.einsum("bhsd,hdf->bhsf", q, w_phi)
        pk = torch.einsum("bhsd,hdf->bhsf", k, w_phi)
        phi_q = torch.concatenate([_softmax(pq, axis=-1), _softmax(-pq, axis=-1)], axis=-1)
        phi_k = torch.concatenate([_softmax(pk, axis=-1), _softmax(-pk, axis=-1)], axis=-1)
        kv = torch.einsum("bhsf,bhsd->bhfd", phi_k, v)
        z  = torch.einsum("bhsf->bhf", phi_k)
        num = torch.einsum("bhqf,bhfd->bhqd", phi_q, kv)
        den = torch.einsum("bhqf,bhf->bhq", phi_q, z)[..., None] + 1e-6
        o = num / den
        return (lambda x, *d: x.permute(*d))(o, 0, 2, 1, 3).reshape(B, S, D) @ wo

    def _B_ref(x, w1, b1, w2, b2):
        h = torch.einsum("bsd,df->bsf", x, w1) + b1
        zero = torch.zeros_like(h)
        r = torch.where(h > zero, h, zero)
        h = r * r
        out = torch.einsum("bsf,fd->bsd", h, w2) + b2
        return out

    x = A_x
    xn = _layernorm(x, axis=-1)
    s = _A_ref(xn, A_wqkv, A_w_phi, A_wo)
    g = _silu(_B_ref(xn, B_w1, B_b1, B_w2, B_b2))
    return x + s * g
```

```cpp
#include <hip/hip_runtime.h>
#include <hip/hip_bf16.h>
typedef __hip_bfloat16 bf16;
static __device__ __forceinline__ float b2f(bf16 x){return __bfloat162float(x);}
static __device__ __forceinline__ float rb(float x){return __bfloat162float(__float2bfloat16(x));}

__global__ void ln_k(const bf16* x, bf16* xn){
  int row=blockIdx.x; int t=threadIdx.x;
  float s=0; for(int i=t;i<512;i+=64) s+=b2f(x[row*512+i]);
  for(int o=32;o;o>>=1) s+=__shfl_xor(s,o);
  float m=s/512.f; float v=0; for(int i=t;i<512;i+=64){float d=b2f(x[row*512+i])-m; v+=d*d;}
  for(int o=32;o;o>>=1) v+=__shfl_xor(v,o);
  v/=512.f; float r=1.f/sqrtf(v+1e-5f);
  for(int i=t;i<512;i+=64) xn[row*512+i]=__float2bfloat16((b2f(x[row*512+i])-m)*r);
}
__global__ void gemm_k(const bf16* A,const bf16* B,float* C,int M,int N,int K){
  int n=blockIdx.x*64+threadIdx.x; int m=blockIdx.y;
  float s=0; for(int k=0;k<K;k++) s+=b2f(A[(size_t)m*K+k])*b2f(B[(size_t)k*N+n]);
  C[(size_t)m*N+n]=s;
}
__global__ void phi_k(const float* qkv,const bf16* wphi,float* phiq,float* phik){
  int idx=blockIdx.x*blockDim.x+threadIdx.x; int which=idx&1; int h=(idx>>1)&31; int t=idx>>6;
  float q[16]; for(int d=0;d<16;d++) q[d]=rb(qkv[(size_t)t*1536+which*512+h*16+d]);
  float p[16]; float mx=-1e30f,mn=1e30f;
  for(int f=0;f<16;f++){float s=0; for(int d=0;d<16;d++) s+=q[d]*b2f(wphi[h*256+d*16+f]); p[f]=s; mx=fmaxf(mx,s); mn=fminf(mn,s);}
  float s1=0,s2=0; for(int f=0;f<16;f++){s1+=expf(p[f]-mx); s2+=expf(mn-p[f]);}
  float* o=(which?phik:phiq)+((size_t)t*32+h)*32;
  for(int f=0;f<16;f++){o[f]=expf(p[f]-mx)/s1; o[16+f]=expf(mn-p[f])/s2;}
}
__global__ void kv_k(const float* qkv,const float* phik,float* kv,float* z){
  int h=blockIdx.x; int f=threadIdx.x/17, d=threadIdx.x%17;
  float s=0; for(int t=0;t<2048;t++){float v= d<16? rb(qkv[(size_t)t*1536+1024+h*16+d]):1.f; s+=phik[((size_t)t*32+h)*32+f]*v;}
  if(d<16) kv[(h*32+f)*16+d]=s; else z[h*32+f]=s;
}
__global__ void o_k(const float* phiq,const float* kv,const float* z,bf16* o){
  int idx=blockIdx.x*blockDim.x+threadIdx.x; int d=idx&15; int h=(idx>>4)&31; int t=idx>>9;
  float n=0,den=0; for(int f=0;f<32;f++){float p=phiq[((size_t)t*32+h)*32+f]; n+=p*kv[(h*32+f)*16+d]; den+=p*z[h*32+f];}
  o[(size_t)t*512+h*16+d]=__float2bfloat16(n/(den+1e-6f));
}
__global__ void h_k(const float* hpre,const bf16* b1,bf16* h){
  int idx=blockIdx.x*blockDim.x+threadIdx.x; int f=idx%1792;
  float v=hpre[idx]+b2f(b1[f]); v=v>0?v:0; h[idx]=__float2bfloat16(v*v);
}
__global__ void fin_k(const bf16* x,const float* s,const float* out,const bf16* b2,bf16* res){
  int idx=blockIdx.x*blockDim.x+threadIdx.x; int c=idx&511;
  float a=out[idx]+b2f(b2[c]); float g=a/(1.f+expf(-a));
  res[idx]=__float2bfloat16(b2f(x[idx])+s[idx]*g);
}
extern "C" void kernel_launch(void* const* d_in, const int* in_sizes, int n_in, void* d_out, int out_size, void* d_ws, size_t ws_size, hipStream_t stream){
  const bf16* x=(const bf16*)d_in[0]; const bf16* wqkv=(const bf16*)d_in[1]; const bf16* wphi=(const bf16*)d_in[2]; const bf16* wo=(const bf16*)d_in[3];
  const bf16* w1=(const bf16*)d_in[5]; const bf16* b1=(const bf16*)d_in[6]; const bf16* w2=(const bf16*)d_in[7]; const bf16* b2=(const bf16*)d_in[8];
  char* ws=(char*)d_ws; size_t off=0; auto al=[&](size_t n){void* p=ws+off; off+=(n+255)/256*256; return p;};
  bf16* xn=(bf16*)al(2048*512*2); float* qkv=(float*)al(2048*1536*4); float* phiq=(float*)al(2048*1024*4); float* phik=(float*)al(2048*1024*4);
  float* kv=(float*)al(32*32*16*4); float* z=(float*)al(32*32*4); bf16* o=(bf16*)al(2048*512*2); float* hpre=(float*)al(2048*1792*4); bf16* h=(bf16*)al(2048*1792*2);
  float* s=(float*)al(2048*512*4); float* out=(float*)al(2048*512*4);
  ln_k<<<2048,64,0,stream>>>(x,xn);
  gemm_k<<<dim3(1536/64,2048),64,0,stream>>>(xn,wqkv,qkv,2048,1536,512);
  phi_k<<<2048*64/256,256,0,stream>>>(qkv,wphi,phiq,phik);
  kv_k<<<32,32*17,0,stream>>>(qkv,phik,kv,z);
  o_k<<<2048*512/256,256,0,stream>>>(phiq,kv,z,o);
  gemm_k<<<dim3(512/64,2048),64,0,stream>>>(o,wo,s,2048,512,512);
  gemm_k<<<dim3(1792/64,2048),64,0,stream>>>(xn,w1,hpre,2048,1792,512);
  h_k<<<2048*1792/256,256,0,stream>>>(hpre,b1,h);
  gemm_k<<<dim3(512/64,2048),64,0,stream>>>(h,w2,out,2048,512,1792);
  fin_k<<<2048*512/256,256,0,stream>>>(x,s,out,b2,(bf16*)d_out);
}
```

```cpp
#include <hip/hip_runtime.h>
#include <hip/hip_bf16.h>
#include <stdint.h>

typedef __attribute__((ext_vector_type(8))) __bf16 bf16x8;
typedef __attribute__((ext_vector_type(4))) __bf16 bf16x4;
typedef __attribute__((ext_vector_type(2))) __bf16 bf16x2;
typedef __attribute__((ext_vector_type(16))) float f32x16;
typedef __attribute__((ext_vector_type(8))) float f32x8;
typedef __attribute__((ext_vector_type(4))) float f32x4;
typedef __attribute__((ext_vector_type(2))) float f32x2;
typedef __attribute__((ext_vector_type(4))) unsigned u32x4;
typedef __attribute__((ext_vector_type(2))) unsigned u32x2;
#define LAS __attribute__((address_space(3)))
#define GAS __attribute__((address_space(1)))
#define DI __device__ __forceinline__
#define MFMA32(a, b, c) __builtin_amdgcn_mfma_f32_32x32x16_bf16((a), (b), (c), 0, 0, 0)

constexpr int SEQ = 2048, DM = 512, NH = 32, FF = 1792;
constexpr float LOG2E = 1.4426950408889634f;

constexpr size_t WS_H    = 0;
constexpr size_t WS_PHI  = WS_H + (size_t)64 * 112 * 1024;
constexpr size_t WS_WT   = WS_PHI + (size_t)64 * 64 * 1024;
constexpr size_t WS_KVP  = WS_WT + (size_t)16 * 144 * 1024;
constexpr size_t WS_KVR  = WS_KVP + (size_t)16 * 32 * 2176;
constexpr size_t WS_END  = WS_KVR + (size_t)32 * 1088;

constexpr int SW = 7, SX = 3;
constexpr int L_W = 0;
constexpr int L_X = 7 * 14336;
constexpr int L_PT = L_X + SX * 16384;
constexpr int L_CSP = L_PT + 9 * 1024;
constexpr int L_RSP = L_CSP + 4 * 224 * 4;
constexpr int L_B1 = L_RSP + 2 * 128 * 4;
constexpr int L_TOTAL = L_B1 + 448;
constexpr int L_WPHI = 7 * 12288;
constexpr int L_CS = 0;
constexpr int L_BF = 1024;
constexpr int L_M = 2048;
constexpr int L_R = 2560;
constexpr int L_RED = 4096;

DI void dma16(unsigned voff, const void* sbase, unsigned lds_dst) {
    unsigned keep;
    asm volatile("s_mov_b32 %0, m0\n\ts_mov_b32 m0, %3\n\ts_nop 0\n\tglobal_load_lds_dwordx4 %1, %2\n\ts_mov_b32 m0, %0"
                 : "=&s"(keep) : "v"(voff), "s"(sbase), "s"(lds_dst) : "memory");
}
DI void dma16_masked(unsigned voff, const void* sbase, unsigned lds_dst, unsigned long long mask) {
    unsigned keep;
    asm volatile("s_mov_b32 %0, m0\n\ts_mov_b32 m0, %3\n\ts_mov_b64 exec, %4\n\tglobal_load_lds_dwordx4 %1, %2\n\ts_mov_b64 exec, -1\n\ts_mov_b32 m0, %0"
                 : "=&s"(keep) : "v"(voff), "s"(sbase), "s"(lds_dst), "s"(mask) : "memory");
}
DI void wait_lgkm0() { __builtin_amdgcn_s_waitcnt(0xC07F); asm volatile("" ::: "memory"); }
template <int N> DI void wait_vm_lgkm0() { asm volatile("s_waitcnt vmcnt(%0)" ::"i"(N) : "memory"); wait_lgkm0(); }

DI bf16x4 tr_read(LAS char* p) { return __builtin_amdgcn_ds_read_tr16_b64_v4bf16((LAS bf16x4*)p); }
DI bf16x8 cat44(bf16x4 a, bf16x4 b) { return __builtin_shufflevector(a, b, 0, 1, 2, 3, 4, 5, 6, 7); }
DI float dot2(unsigned a, unsigned b, float c) {
    return __builtin_amdgcn_fdot2_f32_bf16(__builtin_bit_cast(bf16x2, a), __builtin_bit_cast(bf16x2, b), c, false);
}
DI unsigned pk2(float a, float b) { f32x2 v = {a, b}; return __builtin_bit_cast(unsigned, __builtin_convertvector(v, bf16x2)); }
template <int S> DI bf16x8 pack8(const f32x16& x) {
    u32x4 p = {pk2(x[8 * S], x[8 * S + 1]), pk2(x[8 * S + 2], x[8 * S + 3]), pk2(x[8 * S + 4], x[8 * S + 5]), pk2(x[8 * S + 6], x[8 * S + 7])};
    return __builtin_bit_cast(bf16x8, p);
}
DI float u2f(unsigned u) { return __builtin_bit_cast(float, u); }
DI unsigned f2u(float f) { return __builtin_bit_cast(unsigned, f); }

constexpr int NCH = 16;
constexpr int pos_of(bool isx, int idx) {
    int pos = 0;
    for (int c = 0; c < SW; ++c) {
        if ((c & 1) == 0 && (c >> 1) < SX) { if (isx && idx == (c >> 1)) return pos; ++pos; }
        if (!isx && idx == c) return pos;
        ++pos;
    }
    for (int c = 0; c < NCH; ++c) {
        if ((c & 1) && ((c - 1) / 2 + SX) < NCH / 2) { if (isx && idx == (c - 1) / 2 + SX) return pos; ++pos; }
        if (c + SW < NCH) { if (!isx && idx == c + SW) return pos; ++pos; }
    }
    return 1000;
}
constexpr int issued_before(int c) {
    int pos = 0;
    for (int k = 0; k < SW; ++k) { if ((k & 1) == 0 && (k >> 1) < SX) ++pos; ++pos; }
    for (int k = 0; k < c; ++k) {
        if ((k & 1) && ((k - 1) / 2 + SX) < NCH / 2) ++pos;
        if (k + SW < NCH) ++pos;
    }
    return pos;
}
constexpr int EXTRA_AT = 9;
constexpr int vm_at(int c) {
    if (c + 1 >= NCH) return 63;
    int a = pos_of(false, c + 1), b = pos_of(true, (c + 1) >> 1);
    int last = a > b ? a : b;
    int n = 2 * (issued_before(c) - 1 - last);
    if (c > EXTRA_AT) n += 1;
    return n;
}

struct K1Args {
    const __bf16* x; const __bf16* wqkv; const __bf16* wphi; const __bf16* wo;
    const __bf16* w1; const __bf16* b1; const __bf16* w2;
    char* ws;
};

template <bool QKV, int WN>
DI void k1_body(const K1Args& a, LAS char* lds, int mt, int nt, int wm, int lane, int wave, int bid) {
    constexpr int NT = QKV ? 6 : 7;
    constexpr int NTW = QKV ? 3 : (WN == 0 ? 4 : 3);
    constexpr int WSLOT = NT * 2048;
    constexpr int LPP = NT * 8;
    constexpr int LDW = QKV ? 1536 : 1792;
    const unsigned ldsb = (unsigned)(size_t)lds;
    const int hh = lane >> 5, r32 = lane & 31, blk = (lane >> 4) & 1, q4 = (lane & 15) >> 2, p4 = lane & 3;

    unsigned wvo[2], xvo[2];
#pragma unroll
    for (int i = 0; i < 2; ++i) {
        int pc = 2 * wave + i;
        int u = pc * LPP + (lane < LPP ? lane : 0);
        int st = u >> 5, rr = (u >> 2) & 7, c16 = u & 3;
        int k8 = st / NT, c = st - k8 * NT;
        int colb = QKV ? ((c >> 1) * 512 + 64 * nt + 32 * (c & 1)) : (224 * nt + 32 * c);
        wvo[i] = (unsigned)(((k8 * 8 + rr) * LDW + colb + c16 * 8) * 2);
        int row = 8 * pc + (lane >> 3);
        int sc = (lane & 7) ^ ((row >> 1) & 7);
        xvo[i] = (unsigned)((row * DM + sc * 8) * 2);
    }
    const char* wbase = (const char*)(QKV ? a.wqkv : a.w1);
    const char* xbase = (const char*)(a.x + (size_t)mt * 128 * DM);
    const unsigned long long wmask = (1ull << LPP) - 1ull;
    auto issue_w = [&](int c) {
#pragma unroll
        for (int i = 0; i < 2; ++i)
            dma16_masked(wvo[i], wbase + (size_t)c * 32 * LDW * 2, ldsb + L_W + (c % SW) * WSLOT + (2 * wave + i) * LPP * 16, wmask);
    };
    auto issue_x = [&](int j) {
#pragma unroll
        for (int i = 0; i < 2; ++i)
            dma16(xvo[i], xbase + j * 128, ldsb + L_X + (j % SX) * 16384 + (2 * wave + i) * 1024);
    };
#pragma unroll
    for (int c = 0; c < SW; ++c) {
        if ((c & 1) == 0 && (c >> 1) < SX) issue_x(c >> 1);
        issue_w(c);
    }

    LAS char* wrd = lds + L_W + hh * (NT * 512) + q4 * 64 + blk * 32 + p4 * 8;
    LAS char* crd = lds + L_W + wm * (NT * 512) + (4 * hh + q4) * 64 + blk * 32 + p4 * 8;
    LAS char* xrd[4];
    {
        int row = 32 * wm + r32;
#pragma unroll
        for (int k = 0; k < 4; ++k) xrd[k] = lds + L_X + row * 128 + (((2 * k + hh) ^ ((row >> 1) & 7)) * 16);
    }
    auto tile_of = [&](int t) { return QKV ? (2 * t + WN) : (WN * 4 + t); };

    f32x16 acc[NTW];
#pragma unroll
    for (int t = 0; t < NTW; ++t)
#pragma unroll
        for (int i = 0; i < 16; ++i) acc[t][i] = 0.f;
    float csum[NTW];
#pragma unroll
    for (int t = 0; t < NTW; ++t) csum[t] = 0.f;
    float rstat = 0.f;

    bf16x8 xf[2][2], wf[2][2][NTW];
    bf16x4 cf[2][NTW];
    auto read_frags = [&](int c) {
        const int b = c & 1;
#pragma unroll
        for (int ks = 0; ks < 2; ++ks) {
            xf[b][ks] = *(LAS bf16x8*)(xrd[(c & 1) * 2 + ks] + ((c >> 1) % SX) * 16384);
#pragma unroll
            for (int t = 0; t < NTW; ++t) {
                LAS char* p = wrd + (c % SW) * WSLOT + ks * 2 * NT * 512 + tile_of(t) * 512;
                wf[b][ks][t] = cat44(tr_read(p), tr_read(p + 256));
            }
        }
#pragma unroll
        for (int t = 0; t < NTW; ++t) cf[b][t] = tr_read(crd + (c % SW) * WSLOT + tile_of(t) * 512);
    };
    auto compute = [&](int c) {
        const int b = c & 1;
#pragma unroll
        for (int ks = 0; ks < 2; ++ks) {
#pragma unroll
            for (int t = 0; t < NTW; ++t) {
                if (QKV && t == 2) acc[t] = MFMA32(xf[b][ks], wf[b][ks][t], acc[t]);
                else acc[t] = MFMA32(wf[b][ks][t], xf[b][ks], acc[t]);
            }
            u32x4 xu = __builtin_bit_cast(u32x4, xf[b][ks]);
#pragma unroll
            for (int i = 0; i < 4; ++i) rstat = dot2(xu[i], WN == 0 ? 0x3F803F80u : xu[i], rstat);
        }
#pragma unroll
        for (int t = 0; t < NTW; ++t) {
            u32x2 cu = __builtin_bit_cast(u32x2, cf[b][t]);
            csum[t] = dot2(cu[0], 0x3F803F80u, csum[t]);
            csum[t] = dot2(cu[1], 0x3F803F80u, csum[t]);
            asm volatile("" : "+v"(csum[t]));
        }
        asm volatile("" : "+v"(rstat));
    };

    wait_vm_lgkm0<vm_at(-1)>();
    __builtin_amdgcn_s_barrier();
    asm volatile("" ::: "memory");
    read_frags(0);
#pragma unroll
    for (int c = 0; c < NCH; ++c) {
        __builtin_amdgcn_sched_barrier(0);
        switch (c) {
#define WCASE(C) case C: wait_vm_lgkm0<vm_at(C)>(); break;
            WCASE(0) WCASE(1) WCASE(2) WCASE(3) WCASE(4) WCASE(5) WCASE(6) WCASE(7)
            WCASE(8) WCASE(9) WCASE(10) WCASE(11) WCASE(12) WCASE(13) WCASE(14) WCASE(15)
#undef WCASE
        }
        if (c + 1 < NCH) __builtin_amdgcn_s_barrier();
        asm volatile("" ::: "memory");
        __builtin_amdgcn_sched_barrier(0);
        if ((c & 1) && ((c - 1) / 2 + SX) < NCH / 2) issue_x((c - 1) / 2 + SX);
        if (c + SW < NCH) issue_w(c + SW);
        if (c == EXTRA_AT) {
            const int nf = (wave == 0) ? 2 : 1;
            for (int i = 0; i < nf; ++i) {
                int fi = (i == 0) ? wave : 8;
                int f = 9 * bid + fi;
                int ct = f / 144, ks = f - ct * 144;
                const __bf16* src = ks < 112 ? a.w2 + (size_t)ks * 16 * DM : a.wo + (size_t)(ks - 112) * 16 * DM;
                unsigned vo = (unsigned)(((lane >> 2) * DM + ct * 32 + (lane & 3) * 8) * 2);
                dma16(vo, src, ldsb + L_PT + fi * 1024);
            }
            if (wave == 1) {
                if (QKV) {
                    dma16(lane * 16, a.wphi + (size_t)nt * 4 * 256, ldsb + L_WPHI);
                    dma16(lane * 16 + 1024, a.wphi + (size_t)nt * 4 * 256, ldsb + L_WPHI + 1024);
                } else {
                    dma16_masked((lane < 28 ? lane : 0) * 16, a.b1 + nt * 224, ldsb + L_B1, (1ull << 28) - 1ull);
                }
            }
        }
        if (c + 1 < NCH) read_frags(c + 1);
        compute(c);
    }
    __builtin_amdgcn_sched_barrier(0);

    {
#pragma unroll
        for (int t = 0; t < NTW; ++t) {
            auto s = __builtin_amdgcn_permlane32_swap(f2u(csum[t]), f2u(csum[t]), false, false);
            float v = u2f(s[0]) + u2f(s[1]);
            if (hh == 0) *(LAS float*)(lds + L_CSP + (wm * 224 + tile_of(t) * 32 + r32) * 4) = v;
        }
        auto s = __builtin_amdgcn_permlane32_swap(f2u(rstat), f2u(rstat), false, false);
        float v = u2f(s[0]) + u2f(s[1]);
        if (hh == 0) *(LAS float*)(lds + L_RSP + (WN * 128 + 32 * wm + r32) * 4) = v;
    }
    wait_vm_lgkm0<0>();
    __builtin_amdgcn_s_barrier();
    asm volatile("" ::: "memory");
    {
        const int tid = wave * 64 + lane;
        if (tid < NT * 32) {
            LAS float* p = (LAS float*)(lds + L_CSP) + tid;
            float v = (p[0] + p[224]) + (p[448] + p[672]);
            *(LAS float*)(lds + L_CS + tid * 4) = v;
            if (!QKV) {
                unsigned short bb = *(LAS unsigned short*)(lds + L_B1 + tid * 2);
                *(LAS float*)(lds + L_BF + tid * 4) = u2f((unsigned)bb << 16);
            }
        } else if (tid >= 256 && tid < 384) {
            int t = tid - 256;
            LAS float* p = (LAS float*)(lds + L_RSP) + t;
            float mean = p[0] * (1.f / DM);
            float var = fmaxf(p[128] * (1.f / DM) - mean * mean, 0.f);
            *(LAS float*)(lds + L_M + t * 4) = mean;
            *(LAS float*)(lds + L_R + t * 4) = __builtin_amdgcn_rsqf(var + 1e-5f);
        }
        const int nf = (wave == 0) ? 2 : 1;
        for (int i = 0; i < nf; ++i) {
            int fi = (i == 0) ? wave : 8;
            LAS char* p = lds + L_PT + fi * 1024 + (4 * hh + q4) * 64 + blk * 32 + p4 * 8;
            bf16x8 v = cat44(tr_read(p), tr_read(p + 512));
            *(GAS bf16x8*)(a.ws + WS_WT + ((size_t)(9 * bid + fi) * 64 + lane) * 16) = v;
        }
        if (wave == 2 && lane < 17) { u32x2 sv = {0xFFFFFFFFu, 0xFFFFFFFFu}; *(GAS u32x2*)(a.ws + WS_KVR + ((size_t)bid * 17 + lane) * 8) = sv; }
    }
    wait_lgkm0();
    __builtin_amdgcn_s_barrier();
    asm volatile("" ::: "memory");

    const int tt = mt * 4 + wm;
    const float m_t = *(LAS float*)(lds + L_M + (32 * wm + r32) * 4);
    const float r_t = *(LAS float*)(lds + L_R + (32 * wm + r32) * 4);

    if constexpr (!QKV) {
#pragma unroll
        for (int t = 0; t < NTW; ++t) {
            f32x16 hv;
#pragma unroll
            for (int g = 0; g < 4; ++g) {
                f32x4 cs = *(LAS f32x4*)(lds + L_CS + (tile_of(t) * 32 + 8 * g + 4 * hh) * 4);
                f32x4 bb = *(LAS f32x4*)(lds + L_BF + (tile_of(t) * 32 + 8 * g + 4 * hh) * 4);
#pragma unroll
                for (int i = 0; i < 4; ++i) {
                    float v = (acc[t][4 * g + i] - m_t * cs[i]) * r_t + bb[i];
                    v = fmaxf(v, 0.f);
                    hv[4 * g + i] = v * v;
                }
            }
            char* dst = a.ws + WS_H + ((size_t)(tt * 112 + nt * 14 + tile_of(t) * 2) * 64 + lane) * 16;
            *(GAS bf16x8*)(dst) = pack8<0>(hv);
            *(GAS bf16x8*)(dst + 1024) = pack8<1>(hv);
        }
    } else {
#pragma unroll
        for (int t = 0; t < 2; ++t)
#pragma unroll
            for (int g = 0; g < 4; ++g) {
                f32x4 cs = *(LAS f32x4*)(lds + L_CS + (tile_of(t) * 32 + 8 * g + 4 * hh) * 4);
#pragma unroll
                for (int i = 0; i < 4; ++i) acc[t][4 * g + i] -= m_t * cs[i];
            }
        bf16x8 vfa[2], vfb[2];
        {
            const float csl = *(LAS float*)(lds + L_CS + (tile_of(2) * 32 + r32) * 4);
#pragma unroll
            for (int g = 0; g < 4; ++g) {
                f32x4 mm = *(LAS f32x4*)(lds + L_M + (32 * wm + 8 * g + 4 * hh) * 4);
                f32x4 rr = *(LAS f32x4*)(lds + L_R + (32 * wm + 8 * g + 4 * hh) * 4);
#pragma unroll
                for (int i = 0; i < 4; ++i) acc[2][4 * g + i] = (acc[2][4 * g + i] - mm[i] * csl) * rr[i];
            }
            u32x4 v0 = __builtin_bit_cast(u32x4, pack8<0>(acc[2])), v1 = __builtin_bit_cast(u32x4, pack8<1>(acc[2]));
            u32x4 a0, a1, b0, b1;
#pragma unroll
            for (int i = 0; i < 4; ++i) {
                a0[i] = blk ? 0x3F803F80u : v0[i]; a1[i] = blk ? 0x3F803F80u : v1[i];
                b0[i] = blk ? v0[i] : 0x3F803F80u; b1[i] = blk ? v1[i] : 0x3F803F80u;
            }
            vfa[0] = __builtin_bit_cast(bf16x8, a0); vfa[1] = __builtin_bit_cast(bf16x8, a1);
            vfb[0] = __builtin_bit_cast(bf16x8, b0); vfb[1] = __builtin_bit_cast(bf16x8, b1);
        }
        bf16x8 idf[2];
#pragma unroll
        for (int s = 0; s < 2; ++s) {
            u32x4 u;
#pragma unroll
            for (int i = 0; i < 4; ++i) {
                int f0 = 16 * s + 8 * (i >> 1) + 4 * hh + 2 * (i & 1);
                u[i] = (r32 == f0 ? 0x3F80u : 0u) | (r32 == f0 + 1 ? 0x3F800000u : 0u);
            }
            idf[s] = __builtin_bit_cast(bf16x8, u);
        }
        const float kscale = r_t * LOG2E;
        f32x16 zero16;
#pragma unroll
        for (int i = 0; i < 16; ++i) zero16[i] = 0.f;
#pragma unroll
        for (int X = 0; X < 2; ++X) {
            bf16x8 wfr;
            {
                LAS char* p = lds + L_WPHI + (2 * WN + X) * 512 + (4 * hh + q4) * 32 + p4 * 8;
                u32x4 u = __builtin_bit_cast(u32x4, cat44(tr_read(p), tr_read(p + 256)));
                const unsigned sg = blk ? 0x80008000u : 0u;
#pragma unroll
                for (int i = 0; i < 4; ++i) u[i] ^= sg;
                wfr = __builtin_bit_cast(bf16x8, u);
            }
#pragma unroll
            for (int which = 0; which < 2; ++which) {
                bf16x8 b = X == 0 ? pack8<0>(acc[which]) : pack8<1>(acc[which]);
                f32x16 p = MFMA32(wfr, b, zero16);
                float mp = fmaxf(fmaxf(fmaxf(p[0], p[1]), fmaxf(p[2], p[3])), fmaxf(fmaxf(p[4], p[5]), fmaxf(p[6], p[7])));
                float mn = fmaxf(fmaxf(fmaxf(p[8], p[9]), fmaxf(p[10], p[11])), fmaxf(fmaxf(p[12], p[13]), fmaxf(p[14], p[15])));
                {
                    auto s = __builtin_amdgcn_permlane32_swap(f2u(mp), f2u(mn), false, false);
                    float c = fmaxf(u2f(s[0]), u2f(s[1]));
                    auto s2 = __builtin_amdgcn_permlane32_swap(f2u(c), f2u(c), false, false);
                    mp = u2f(s2[0]); mn = u2f(s2[1]);
                }
                const float bp = -mp * kscale, bn = -mn * kscale;
                float sp = 0.f, sn = 0.f;
#pragma unroll
                for (int i = 0; i < 8; ++i) {
                    p[i] = __builtin_amdgcn_exp2f(__builtin_fmaf(p[i], kscale, bp)); sp += p[i];
                    p[8 + i] = __builtin_amdgcn_exp2f(__builtin_fmaf(p[8 + i], kscale, bn)); sn += p[8 + i];
                }
                {
                    auto s = __builtin_amdgcn_permlane32_swap(f2u(sp), f2u(sn), false, false);
                    float c = __builtin_amdgcn_rcpf(u2f(s[0]) + u2f(s[1]));
                    auto s2 = __builtin_amdgcn_permlane32_swap(f2u(c), f2u(c), false, false);
                    sp = u2f(s2[0]); sn = u2f(s2[1]);
                }
#pragma unroll
                for (int i = 0; i < 8; ++i) { p[i] *= sp; p[8 + i] *= sn; }
                bf16x8 F0 = pack8<0>(p), F1 = pack8<1>(p);
                const int head = nt * 4 + 2 * WN + X;
                if (which == 0) {
                    char* dst = a.ws + WS_PHI + ((size_t)(tt * 32 + head) * 2 * 64 + lane) * 16;
                    *(GAS bf16x8*)(dst) = F0;
                    *(GAS bf16x8*)(dst + 1024) = F1;
                } else {
                    f32x16 d = MFMA32(F0, idf[0], zero16);
                    d = MFMA32(F1, idf[1], d);
                    bf16x8 G0 = pack8<0>(d), G1 = pack8<1>(d);
                    f32x16 kv = MFMA32(G0, X == 0 ? vfa[0] : vfb[0], zero16);
                    kv = MFMA32(G1, X == 0 ? vfa[1] : vfb[1], kv);
                    LAS char* dst = lds + L_RED + (((WN * 2 + X) * 4 + wm) * 32 + r32) * 144 + hh * 16;
#pragma unroll
                    for (int g = 0; g < 4; ++g) {
                        f32x4 v = {kv[4 * g], kv[4 * g + 1], kv[4 * g + 2], kv[4 * g + 3]};
                        *(LAS f32x4*)(dst + g * 32) = v;
                    }
                }
            }
        }
        wait_lgkm0();
        __builtin_amdgcn_s_barrier();
        asm volatile("" ::: "memory");
        const int tid = wave * 64 + lane;
        for (int o = tid; o < 4 * 136; o += 512) {
            int hl = o / 136, rem = o - hl * 136;
            int row = rem >> 3, fq = rem & 7;
            int X = hl & 1;
            int col = row < 16 ? (X * 16 + row) : (X ? 0 : 16);
            LAS char* src = lds + L_RED + ((hl * 4) * 32 + col) * 144 + fq * 16;
            f32x4 v = (*(LAS f32x4*)(src) + *(LAS f32x4*)(src + 4608)) + (*(LAS f32x4*)(src + 2 * 4608) + *(LAS f32x4*)(src + 3 * 4608));
            *(GAS f32x4*)(a.ws + WS_KVP + ((size_t)(mt * 32 + nt * 4 + hl) * 136 + rem) * 16) = v;
        }
    }
}

__global__ void __launch_bounds__(512, 2) k1_kernel(K1Args a) {
    extern __shared__ __attribute__((aligned(16))) char smem[];
    LAS char* lds = (LAS char*)smem;
    const int bid = blockIdx.x;
    const int wave = __builtin_amdgcn_readfirstlane(threadIdx.x >> 6), lane = threadIdx.x & 63;
    const int wm = wave & 3, wn = wave >> 2;
    const int xcd = bid & 7, loc = bid >> 3;
    const int mt = (xcd & 1) * 8 + (loc & 7);
    const int sel = loc >> 3;
    const int nt = (xcd >> 1) * 2 + (sel & 1);
    if (sel < 2) {
        if (wn == 0) k1_body<true, 0>(a, lds, mt, nt, wm, lane, wave, bid);
        else k1_body<true, 1>(a, lds, mt, nt, wm, lane, wave, bid);
    } else {
        if (wn == 0) k1_body<false, 0>(a, lds, mt, nt, wm, lane, wave, bid);
        else k1_body<false, 1>(a, lds, mt, nt, wm, lane, wave, bid);
    }
}

struct K2Args {
    const __bf16* x; const __bf16* b2; char* ws; __bf16* out;
};
constexpr int PF = 12;

__global__ void __launch_bounds__(256, 1) k2_kernel(K2Args a) {
    __shared__ __attribute__((aligned(16))) char smem[2 * 4 * 3 * 4096];
    LAS char* lds = (LAS char*)smem;
    const int bid = blockIdx.x;
    const int kq = __builtin_amdgcn_readfirstlane(threadIdx.x >> 6), lane = threadIdx.x & 63;
    const int hh = lane >> 5, r32 = lane & 31;
    const int xcd = bid & 7, loc = bid >> 3;
    const int mt = xcd * 4 + (loc & 3);
    const int nt = loc >> 2;

    if (kq == 0 && lane < 17) {
        const int head = bid >> 3, g = (bid & 7) * 17 + lane;
        const GAS f32x4* src = (const GAS f32x4*)(a.ws + WS_KVP) + (size_t)head * 136 + g;
        f32x4 v[16];
#pragma unroll
        for (int m = 0; m < 16; ++m) v[m] = src[(size_t)m * 32 * 136];
#pragma unroll
        for (int st = 8; st >= 1; st >>= 1)
#pragma unroll
            for (int m = 0; m < st; ++m) v[m] += v[m + st];
        unsigned lo = pk2(v[0][0], v[0][1]), hi = pk2(v[0][2], v[0][3]);
        if (lo == 0xFFFFFFFFu) lo = 0xFFC0FFC0u;
        if (hi == 0xFFFFFFFFu) hi = 0xFFC0FFC0u;
        const int row = g >> 3, fq = g & 7;
        const size_t off = WS_KVR + (size_t)head * 1088 + (fq >> 2) * 544 + row * 32 + (fq & 1) * 16 + ((fq >> 1) & 1) * 8;
        __hip_atomic_store((GAS unsigned long long*)(a.ws + off), (unsigned long long)lo | ((unsigned long long)hi << 32),
                           __ATOMIC_RELAXED, __HIP_MEMORY_SCOPE_AGENT);
    }

    const GAS bf16x8* hp0 = (const GAS bf16x8*)(a.ws + WS_H) + ((size_t)(2 * mt) * 112 + 28 * kq) * 64 + lane;
    const GAS bf16x8* hp1 = hp0 + 112 * 64;
    const GAS bf16x8* wp0 = (const GAS bf16x8*)(a.ws + WS_WT) + ((size_t)(2 * nt) * 144 + 28 * kq) * 64 + lane;
    const GAS bf16x8* wp1 = wp0 + 144 * 64;
    f32x16 acc[2][2];
#pragma unroll
    for (int i = 0; i < 2; ++i)
#pragma unroll
        for (int j = 0; j < 2; ++j)
#pragma unroll
            for (int e = 0; e < 16; ++e) acc[i][j][e] = 0.f;
    bf16x8 hb[PF][2], wb[PF][2];
#pragma unroll
    for (int s = 0; s < PF; ++s) { hb[s][0] = hp0[s * 64]; wb[s][0] = wp0[s * 64]; hb[s][1] = hp1[s * 64]; wb[s][1] = wp1[s * 64]; }
#pragma unroll
    for (int s = 0; s < 28; ++s) {
        const int b = s % PF;
#pragma unroll
        for (int i = 0; i < 2; ++i)
#pragma unroll
            for (int j = 0; j < 2; ++j) acc[i][j] = MFMA32(wb[b][i], hb[b][j], acc[i][j]);
        if (s + PF < 28) { hb[b][0] = hp0[(s + PF) * 64]; wb[b][0] = wp0[(s + PF) * 64]; hb[b][1] = hp1[(s + PF) * 64]; wb[b][1] = wp1[(s + PF) * 64]; }
    }
    f32x16 own_out;
#pragma unroll
    for (int qd = 0; qd < 4; ++qd) {
        if (qd == kq) own_out = acc[qd >> 1][qd & 1];
        else {
            const int slot = kq < qd ? kq : kq - 1;
            LAS char* dst = lds + (qd * 3 + slot) * 4096 + lane * 16;
#pragma unroll
            for (int g = 0; g < 4; ++g) {
                f32x4 v = {acc[qd >> 1][qd & 1][4 * g], acc[qd >> 1][qd & 1][4 * g + 1], acc[qd >> 1][qd & 1][4 * g + 2], acc[qd >> 1][qd & 1][4 * g + 3]};
                *(LAS f32x4*)(dst + g * 1024) = v;
            }
        }
    }
    const GAS bf16x8* pp0 = (const GAS bf16x8*)(a.ws + WS_PHI) + ((size_t)(2 * mt) * 32 + 8 * kq) * 128 + lane;
    const GAS bf16x8* pp1 = pp0 + 32 * 128;
    const GAS bf16x8* op0 = (const GAS bf16x8*)(a.ws + WS_WT) + ((size_t)(2 * nt) * 144 + 112 + 8 * kq) * 64 + lane;
    const GAS bf16x8* op1 = op0 + 144 * 64;
    bf16x8 ph[8][2][2], wo[8][2];
#pragma unroll
    for (int h = 0; h < 8; ++h) {
        ph[h][0][0] = pp0[h * 128]; ph[h][0][1] = pp0[h * 128 + 64];
        ph[h][1][0] = pp1[h * 128]; ph[h][1][1] = pp1[h * 128 + 64];
        wo[h][0] = op0[h * 64]; wo[h][1] = op1[h * 64];
    }
    u32x4 kvf[8][2];
    {
        const __amdgpu_buffer_rsrc_t rs = __builtin_amdgcn_make_buffer_rsrc(a.ws + WS_KVR, 0, 32 * 1088, 0x00020000);
        const unsigned vo = (unsigned)(8 * kq * 1088 + (r32 < 16 ? r32 : 16) * 32 + hh * 16);
        for (unsigned spin = 0; spin < (1u << 20); ++spin) {
            bool ok = true;
#pragma unroll
            for (int h = 0; h < 8; ++h)
#pragma unroll
                for (int s = 0; s < 2; ++s) {
                    kvf[h][s] = __builtin_amdgcn_raw_buffer_load_b128(rs, vo + h * 1088 + s * 544, 0, 16);
#pragma unroll
                    for (int i = 0; i < 4; ++i) ok &= (kvf[h][s][i] != 0xFFFFFFFFu);
                }
            if (__all(ok)) break;
            __builtin_amdgcn_s_sleep(2);
        }
    }
#pragma unroll
    for (int i = 0; i < 2; ++i)
#pragma unroll
        for (int j = 0; j < 2; ++j)
#pragma unroll
            for (int e = 0; e < 16; ++e) acc[i][j][e] = 0.f;
    f32x16 zero16;
#pragma unroll
    for (int i = 0; i < 16; ++i) zero16[i] = 0.f;
#pragma unroll
    for (int h = 0; h < 8; ++h) {
#pragma unroll
        for (int j = 0; j < 2; ++j) {
            f32x16 nd = MFMA32(__builtin_bit_cast(bf16x8, kvf[h][0]), ph[h][j][0], zero16);
            nd = MFMA32(__builtin_bit_cast(bf16x8, kvf[h][1]), ph[h][j][1], nd);
            const float inv = __builtin_amdgcn_rcpf(nd[8] + 1e-6f);
#pragma unroll
            for (int e = 0; e < 8; ++e) nd[e] *= inv;
            bf16x8 of = pack8<0>(nd);
            acc[0][j] = MFMA32(wo[h][0], of, acc[0][j]);
            acc[1][j] = MFMA32(wo[h][1], of, acc[1][j]);
        }
    }
    f32x16 own_s;
#pragma unroll
    for (int qd = 0; qd < 4; ++qd) {
        if (qd == kq) own_s = acc[qd >> 1][qd & 1];
        else {
            const int slot = kq < qd ? kq : kq - 1;
            LAS char* dst = lds + 49152 + (qd * 3 + slot) * 4096 + lane * 16;
#pragma unroll
            for (int g = 0; g < 4; ++g) {
                f32x4 v = {acc[qd >> 1][qd & 1][4 * g], acc[qd >> 1][qd & 1][4 * g + 1], acc[qd >> 1][qd & 1][4 * g + 2], acc[qd >> 1][qd & 1][4 * g + 3]};
                *(LAS f32x4*)(dst + g * 1024) = v;
            }
        }
    }
    const int token = mt * 64 + (kq & 1) * 32 + r32;
    const int col0 = nt * 64 + (kq >> 1) * 32 + 4 * hh;
    u32x2 xr[4], br[4];
#pragma unroll
    for (int g = 0; g < 4; ++g) {
        xr[g] = *(const GAS u32x2*)(a.x + (size_t)token * DM + col0 + 8 * g);
        br[g] = *(const GAS u32x2*)(a.b2 + col0 + 8 * g);
    }
    __syncthreads();
#pragma unroll
    for (int sl = 0; sl < 3; ++sl)
#pragma unroll
        for (int g = 0; g < 4; ++g) {
            f32x4 vo = *(LAS f32x4*)(lds + (kq * 3 + sl) * 4096 + g * 1024 + lane * 16);
            f32x4 vs = *(LAS f32x4*)(lds + 49152 + (kq * 3 + sl) * 4096 + g * 1024 + lane * 16);
#pragma unroll
            for (int i = 0; i < 4; ++i) { own_out[4 * g + i] += vo[i]; own_s[4 * g + i] += vs[i]; }
        }
#pragma unroll
    for (int g = 0; g < 4; ++g) {
        float res[4];
#pragma unroll
        for (int i = 0; i < 4; ++i) {
            const float xv = u2f((i & 1) ? (xr[g][i >> 1] & 0xFFFF0000u) : (xr[g][i >> 1] << 16));
            const float bv = u2f((i & 1) ? (br[g][i >> 1] & 0xFFFF0000u) : (br[g][i >> 1] << 16));
            const float o = own_out[4 * g + i] + bv;
            const float gt = o * __builtin_amdgcn_rcpf(1.f + __builtin_amdgcn_exp2f(-o * LOG2E));
            res[i] = xv + own_s[4 * g + i] * gt;
        }
        u32x2 ov = {pk2(res[0], res[1]), pk2(res[2], res[3])};
        *(GAS u32x2*)(a.out + (size_t)token * DM + col0 + 8 * g) = ov;
    }
}

extern "C" void kernel_launch(void* const* d_in, const int* in_sizes, int n_in, void* d_out, int out_size, void* d_ws,
                              size_t ws_size, hipStream_t stream) {
    K1Args a1;
    a1.x = (const __bf16*)d_in[0]; a1.wqkv = (const __bf16*)d_in[1]; a1.wphi = (const __bf16*)d_in[2]; a1.wo = (const __bf16*)d_in[3];
    a1.w1 = (const __bf16*)d_in[5]; a1.b1 = (const __bf16*)d_in[6]; a1.w2 = (const __bf16*)d_in[7];
    a1.ws = (char*)d_ws;
    K2Args a2;
    a2.x = (const __bf16*)d_in[0]; a2.b2 = (const __bf16*)d_in[8]; a2.ws = (char*)d_ws; a2.out = (__bf16*)d_out;
    (void)hipFuncSetAttribute((const void*)k1_kernel, hipFuncAttributeMaxDynamicSharedMemorySize, L_TOTAL);
    k1_kernel<<<256, 512, L_TOTAL, stream>>>(a1);
    k2_kernel<<<256, 256, 0, stream>>>(a2);
}
```

```cpp
#include <hip/hip_runtime.h>
#include <hip/hip_bf16.h>
#include <stdint.h>
#include <type_traits>

typedef __attribute__((ext_vector_type(8))) __bf16 bf16x8;
typedef __attribute__((ext_vector_type(4))) __bf16 bf16x4;
typedef __attribute__((ext_vector_type(2))) __bf16 bf16x2;
typedef __attribute__((ext_vector_type(16))) float f32x16;
typedef __attribute__((ext_vector_type(4))) float f32x4;
typedef __attribute__((ext_vector_type(2))) float f32x2;
typedef __attribute__((ext_vector_type(4))) unsigned u32x4;
typedef __attribute__((ext_vector_type(2))) unsigned u32x2;
typedef __amdgpu_buffer_rsrc_t srd_t;
#define LAS __attribute__((address_space(3)))
#define GAS __attribute__((address_space(1)))
#define DI __device__ __forceinline__
#define MFMA32(a, b, c) __builtin_amdgcn_mfma_f32_32x32x16_bf16((a), (b), (c), 0, 0, 0)
#define SCHED0() __builtin_amdgcn_sched_barrier(0)
#define CFENCE() asm volatile("" ::: "memory")
#define RLX_AGENT __ATOMIC_RELAXED, __HIP_MEMORY_SCOPE_AGENT

constexpr int SEQ = 2048, DM = 512, NH = 32, FF = 1792;
constexpr float LOG2E = 1.4426950408889634f;
constexpr int SC1 = 16;
constexpr unsigned SPIN_CAP = 1u << 16;

constexpr unsigned WS_CTRL = 0;
constexpr unsigned WS_FLAG = 256;
constexpr unsigned WS_KVR  = WS_FLAG + 256 * 64;
constexpr unsigned WS_KVP  = WS_KVR + 32 * 2176;
constexpr unsigned WS_H    = (WS_KVP + 16u * 32 * 136 * 32 + 4095) & ~4095u;
constexpr unsigned WS_O    = WS_H + 64u * 112 * 1024;
constexpr unsigned WS_END  = WS_O + 64u * 32 * 1024;

#define ST(i)

constexpr int SLOT = 45056;
constexpr int XOFF = 28672;
constexpr int L_CSP = 3 * SLOT + 4096;
constexpr int L_RSP = L_CSP + 4 * 224 * 4;
constexpr int L_RR = L_RSP + 2 * 128 * 8;
constexpr int L_AUX = L_RR + 1024;
constexpr int L_DUMMY = L_AUX + 2048;
constexpr int L_RED = 98304;
constexpr int NB = 11;
constexpr int L2_CNT = NB * 8192;
constexpr int L2_A = 98304;
constexpr int phi_addr(int p) { return p < 8 ? 81920 + p * 1024 : 138240 + (p - 8) * 1024; }
constexpr int L_TOTAL = 163840;
static_assert(L_DUMMY + 256 <= L_TOTAL && L_RED + 39168 <= L_CSP, "lds");
constexpr int slot_base(int j) { return ((j + 1) % 3) * SLOT; }

template <int LOFF, int SOFF> DI void dma16(unsigned voff, srd_t srd, unsigned lbase) {
    unsigned t;
    asm volatile("s_add_i32 m0, %3, %4\n\ts_mov_b32 %0, %5\n\tbuffer_load_dwordx4 %1, %2, %0 offen lds"
                 : "=&s"(t) : "v"(voff), "s"(srd), "s"(lbase), "i"(LOFF), "i"(SOFF) : "memory", "scc");
}
template <int LOFF> DI void touch_ptr(const void* p, unsigned lbase) {
    asm volatile("s_add_i32 m0, %1, %2\n\ts_nop 0\n\tglobal_load_lds_dword %0, off" :: "v"(p), "s"(lbase), "i"(LOFF) : "memory", "scc");
}
template <int LOFF> DI void touch_c(unsigned voff, srd_t srd, unsigned lbase) {
    asm volatile("s_add_i32 m0, %2, %3\n\ts_nop 0\n\tbuffer_load_dword %0, %1, 0 offen sc1 lds" :: "v"(voff), "s"(srd), "s"(lbase), "i"(LOFF) : "memory", "scc");
}
template <int LOFF> DI void touch(unsigned voff, srd_t srd, unsigned lbase) {
    asm volatile("s_add_i32 m0, %2, %3\n\ts_nop 0\n\tbuffer_load_dword %0, %1, 0 offen lds" :: "v"(voff), "s"(srd), "s"(lbase), "i"(LOFF) : "memory", "scc");
}
template <int OFF> DI void ld16c(u32x4& d, unsigned voff, srd_t srd) {
    asm volatile("buffer_load_dwordx4 %0, %1, %2, %3 offen offset:%4 sc1" : "=v"(d) : "v"(voff), "s"(srd), "s"(OFF & ~4095), "i"(OFF & 4095) : "memory");
}
template <int OFF> DI void ld4c(unsigned& d, unsigned voff, srd_t srd) {
    asm volatile("buffer_load_dword %0, %1, %2, %3 offen offset:%4 sc1" : "=v"(d) : "v"(voff), "s"(srd), "s"(OFF & ~4095), "i"(OFF & 4095) : "memory");
}
DI void ld16(u32x4& d, unsigned voff, srd_t srd) { asm volatile("buffer_load_dwordx4 %0, %1, %2, 0 offen" : "=v"(d) : "v"(voff), "s"(srd) : "memory"); }
template <int OFF> DI void ld8(u32x2& d, unsigned voff, srd_t srd) { asm volatile("buffer_load_dwordx2 %0, %1, %2, 0 offen offset:%3" : "=v"(d) : "v"(voff), "s"(srd), "i"(OFF) : "memory"); }
DI float fmx(float a, float b) { return __builtin_elementwise_maximum(a, b); }
DI void wait_lgkm0() { __builtin_amdgcn_s_waitcnt(0xC07F); CFENCE(); }
template <int N> DI void wait_vm() { asm volatile("s_waitcnt vmcnt(%0)" ::"i"(N) : "memory"); }
DI void barrier() { __builtin_amdgcn_s_barrier(); CFENCE(); }
#define PIN(x) asm volatile("" : "+v"(x))
DI srd_t mk_srd(const void* p, unsigned bytes) { return __builtin_amdgcn_make_buffer_rsrc((void*)p, 0, (int)bytes, 0x00020000); }

DI bf16x4 tr_read(LAS char* p) { return __builtin_amdgcn_ds_read_tr16_b64_v4bf16((LAS bf16x4*)p); }
DI bf16x8 cat44(bf16x4 a, bf16x4 b) { return __builtin_shufflevector(a, b, 0, 1, 2, 3, 4, 5, 6, 7); }
DI float dot2(unsigned a, unsigned b, float c) {
    return __builtin_amdgcn_fdot2_f32_bf16(__builtin_bit_cast(bf16x2, a), __builtin_bit_cast(bf16x2, b), c, false);
}
DI unsigned pk2(float a, float b) { f32x2 v = {a, b}; return __builtin_bit_cast(unsigned, __builtin_convertvector(v, bf16x2)); }
template <int S> DI u32x4 pack8u(const f32x16& x) {
    u32x4 p = {pk2(x[8 * S], x[8 * S + 1]), pk2(x[8 * S + 2], x[8 * S + 3]), pk2(x[8 * S + 4], x[8 * S + 5]), pk2(x[8 * S + 6], x[8 * S + 7])};
    return p;
}
template <int S> DI bf16x8 pack8(const f32x16& x) { return __builtin_bit_cast(bf16x8, pack8u<S>(x)); }
DI bf16x8 asbf(u32x4 u) { return __builtin_bit_cast(bf16x8, u); }
DI float u2f(unsigned u) { return __builtin_bit_cast(float, u); }
DI unsigned f2u(float f) { return __builtin_bit_cast(unsigned, f); }
DI unsigned hi2(unsigned lo, unsigned hi) { return __builtin_amdgcn_perm(hi, lo, 0x07060302u); }
DI void split3(float v, unsigned& s0, unsigned& s1, unsigned& s2) {
    s0 = f2u(v) & 0xFFFF0000u;
    float r = v - u2f(s0);
    s1 = f2u(r) & 0xFFFF0000u;
    s2 = f2u(r - u2f(s1));
}
DI float half_sum(float v) { auto s = __builtin_amdgcn_permlane32_swap(f2u(v), f2u(v), false, false); return u2f(s[0]) + u2f(s[1]); }
DI float wave_sum(float v) {
#define DPPADD(ctrl, rm) v += u2f((unsigned)__builtin_amdgcn_update_dpp(0, (int)f2u(v), ctrl, rm, 0xf, true))
    DPPADD(0x111, 0xf); DPPADD(0x112, 0xf); DPPADD(0x114, 0xf); DPPADD(0x118, 0xf); DPPADD(0x142, 0xa); DPPADD(0x143, 0xc);
#undef DPPADD
    return u2f((unsigned)__builtin_amdgcn_readlane((int)f2u(v), 63));
}
template <int C, int N, class F> DI void static_for(F&& f) { if constexpr (C < N) { f(std::integral_constant<int, C>{}); static_for<C + 1, N>(f); } }

#define TOUCH1 2
struct Args {
    const __bf16* x; const __bf16* wqkv; const __bf16* wphi; const __bf16* wo;
    const __bf16* w1; const __bf16* b1; const __bf16* w2; const __bf16* b2;
    char* ws; __bf16* out;
};
struct Geo { int bid, wave, lane, mt, nt, mt2, nt2, qkv; };
#define IC(v) std::integral_constant<int, (v)>{}

DI void wait16(u32x4 (&v)[16]) {
    asm volatile("s_waitcnt vmcnt(0) ; PINW %0 %1 %2 %3 %4 %5 %6 %7 %8 %9 %10 %11 %12 %13 %14 %15"
                 : "+v"(v[0]), "+v"(v[1]), "+v"(v[2]), "+v"(v[3]), "+v"(v[4]), "+v"(v[5]), "+v"(v[6]), "+v"(v[7]),
                   "+v"(v[8]), "+v"(v[9]), "+v"(v[10]), "+v"(v[11]), "+v"(v[12]), "+v"(v[13]), "+v"(v[14]), "+v"(v[15]) :: "memory");
}

template <bool QKV, int WN>
DI unsigned phase1(const Args& a, LAS char* lds, const Geo& G, u32x4 (&phi)[4][2], float& vp2, unsigned long long* stamps) {
    constexpr int NT = QKV ? 6 : 7;
    constexpr int NTW = QKV ? 3 : (WN == 0 ? 3 : 4);
    constexpr int LDW = QKV ? 1536 : 1792;
    constexpr int DW = WN == 0 ? NT : 4;
    constexpr int D2 = WN == 0 ? 6 : 4;
    const int lane = G.lane, wave = G.wave, wm = wave & 3, mt = G.mt, nt = G.nt;
    const unsigned ldsb = (unsigned)(size_t)lds;
    const int hh = lane >> 5, r32 = lane & 31, blk = (lane >> 4) & 1, q4 = (lane & 15) >> 2, p4 = lane & 3;
    const srd_t rs = mk_srd(a.ws, WS_END);
    const srd_t rx = mk_srd(a.x, SEQ * DM * 2);
    const srd_t rw = mk_srd(QKV ? a.wqkv : a.w1, DM * LDW * 2);
    const srd_t rw2 = mk_srd(a.w2, FF * DM * 2);
    ST(0);
    const char* tp = (const char*)a.x + (size_t)(G.bid * 8 + (lane & 7)) * 1024 + wave * 128;
    if (lane >= 8 && lane < 14) tp = (const char*)a.wqkv + (size_t)wave * 196608 + (G.bid * 6 + lane - 8) * 128;
    if (lane >= 16 && lane < 23) tp = (const char*)a.w1 + (size_t)wave * 229376 + (G.bid * 7 + lane - 16) * 128;
    if (wave < TOUCH1) touch_ptr<L_DUMMY>(tp, ldsb);
    auto colb_of = [&](int c) { return QKV ? ((c >> 1) * 512 + 64 * nt + 32 * (c & 1)) : (224 * nt + 32 * c); };
    unsigned dvo[DW];
#pragma unroll
    for (int i = 0; i < DW; ++i) {
        if (WN == 0) {
            int st = 2 * i + hh;
            int k8 = 2 * wm + (st >= NT ? 1 : 0), c = st >= NT ? st - NT : st;
            dvo[i] = (unsigned)(((k8 * 8 + ((lane >> 2) & 7)) * LDW + colb_of(c) + (lane & 3) * 8) * 2);
        } else {
            int row = 8 * (4 * wm + i) + (lane >> 3);
            dvo[i] = (unsigned)(((mt * 128 + row) * DM + ((lane & 7) ^ ((row >> 1) & 7)) * 8) * 2);
        }
    }
    const unsigned lb_pair = ldsb + (WN == 0 ? wm * NT * 1024 : XOFF + wm * 4096);
    auto dma_pair = [&](auto JI, auto II) {
        constexpr int j = JI.value, i = II.value;
        if constexpr (WN == 0) dma16<slot_base(j) + i * 1024, j * 64 * LDW * 2>(dvo[i], rw, lb_pair);
        else dma16<slot_base(j) + i * 1024, j * 128>(dvo[i], rx, lb_pair);
    };
    const unsigned w2vo = (unsigned)(((wm * 448 + ((lane >> 2) & 7)) * DM + G.nt2 * 64 + hh * 32 + (lane & 3) * 8) * 2);
    const unsigned lb_w2 = ldsb + wm * 2048;
    auto dma_w2 = [&](auto BI, auto II) {
        constexpr int r = 5 * BI.value + (WN == 0 ? 0 : 3) + (II.value >> 1), t = II.value & 1;
        dma16<r * 8192 + t * 1024, (r * 16 + t * 8) * DM * 2>(w2vo, rw2, lb_w2);
    };
    ST(20);
    static_for<0, DW>([&](auto I) { dma_pair(IC(0), I); });
    ST(21);
    barrier();
    unsigned epoch;
    ld4c<WS_CTRL>(epoch, 0u, rs);
    touch_ptr<L_DUMMY>(tp, ldsb);
    static_for<0, DW>([&](auto I) { dma_pair(IC(1), I); });
    if (wave == 1) {
        if (QKV) {
            const srd_t rp = mk_srd(a.wphi, NH * 256 * 2);
            dma16<L_AUX, 0>((unsigned)(nt * 2048 + lane * 16), rp, ldsb);
            dma16<L_AUX + 1024, 1024>((unsigned)(nt * 2048 + lane * 16), rp, ldsb);
        } else {
            const srd_t rb = mk_srd(a.b1, FF * 2);
            dma16<L_AUX, 0>((unsigned)(nt * 448 + lane * 16), rb, ldsb);
        }
    }
    ST(22);

    LAS char* wb[3]; LAS char* cb[3]; LAS char* xb[3][4];
#pragma unroll
    for (int s = 0; s < 3; ++s) {
        wb[s] = lds + s * SLOT + hh * (NT * 512) + q4 * 64 + blk * 32 + p4 * 8;
        cb[s] = lds + s * SLOT + wm * (NT * 512) + (4 * hh + q4) * 64 + blk * 32 + p4 * 8;
        PIN(wb[s]); PIN(cb[s]);
        const int row = 32 * wm + r32;
#pragma unroll
        for (int k = 0; k < 4; ++k) { xb[s][k] = lds + s * SLOT + XOFF + row * 128 + (((2 * k + hh) ^ ((row >> 1) & 7)) * 16); PIN(xb[s][k]); }
    }
    auto tile_of = [&](int t) { return QKV ? (2 * t + WN) : (WN * 3 + t); };

    f32x16 acc[NTW];
#pragma unroll
    for (int t = 0; t < NTW; ++t)
#pragma unroll
        for (int i = 0; i < 16; ++i) acc[t][i] = 0.f;
    bf16x8 xf[3], wf[3][NTW];
    u32x2 cf[NTW];
    float cs0[NTW], cs1[NTW], rs0 = 0.f, rs1 = 0.f, rq0 = 0.f, rq1 = 0.f;
#pragma unroll
    for (int t = 0; t < NTW; ++t) { cs0[t] = 0.f; cs1[t] = 0.f; }
    auto read_x = [&](int g) { xf[g % 3] = *(LAS bf16x8*)(xb[((g >> 2) + 1) % 3][g & 3]); };
    auto read_w = [&](int g, int t) {
        LAS char* p = wb[((g >> 2) + 1) % 3] + (g & 3) * 2 * NT * 512 + tile_of(t) * 512;
        wf[g % 3][t] = cat44(tr_read(p), tr_read(p + 256));
    };
    auto read_c = [&](int g, int t) { cf[t] = __builtin_bit_cast(u32x2, tr_read(cb[((g >> 2) + 1) % 3] + ((g >> 1) & 1) * 4 * NT * 512 + tile_of(t) * 512)); };
    auto acc_pair = [&](unsigned u, float& a0, float& a1) {
        float lo, hi;
        asm volatile("v_lshlrev_b32 %0, 16, %4\n\tv_and_b32 %1, 0xffff0000, %4\n\tv_add_f32 %2, %2, %0\n\tv_add_f32 %3, %3, %1"
                     : "=&v"(lo), "=&v"(hi), "+v"(a0), "+v"(a1) : "v"(u));
    };
    auto acc_pair_sq = [&](unsigned u, float& a0, float& a1, float& q0, float& q1) {
        float lo, hi;
        asm volatile("v_lshlrev_b32 %0, 16, %6\n\tv_and_b32 %1, 0xffff0000, %6\n\tv_add_f32 %2, %2, %0\n\tv_add_f32 %3, %3, %1\n\tv_fmac_f32 %4, %0, %0\n\tv_fmac_f32 %5, %1, %1"
                     : "=&v"(lo), "=&v"(hi), "+v"(a0), "+v"(a1), "+v"(q0), "+v"(q1) : "v"(u));
    };
    auto add_c = [&](int t, int i) { acc_pair(cf[t][i], cs0[t], cs1[t]); };
    auto add_x = [&](int g, int i) { acc_pair_sq(__builtin_bit_cast(u32x4, xf[g % 3])[i], rs0, rs1, rq0, rq1); };

    float vpe = 1.f, r_t = 1.f;
    bf16x8 fixT, fixF[NTW], idf[2], wfr[2];
    f32x4 rr[4];
    unsigned tag = 0;
    wait_vm<DW + 2>();
    ST(23);
    barrier();
    ST(1);
#pragma unroll
    for (int g = 0; g < 2; ++g) {
        read_x(g);
#pragma unroll
        for (int t = 0; t < NTW; ++t) read_w(g, t);
    }
#pragma unroll
    for (int t = 0; t < NTW; ++t) read_c(0, t);
    static_for<0, 32>([&](auto GI) {
        constexpr int g = GI.value, j = g >> 2;
        SCHED0();
        if constexpr ((g & 3) == 2) {
            constexpr int n = j == 2 ? DW + 1 : (j <= 5 ? DW : (j == 6 ? D2 : 63));
            if constexpr (j == 0) asm volatile("s_waitcnt vmcnt(%1) ; PINW %0" : "+v"(epoch) : "i"(n) : "memory");
            else wait_vm<n>();
            wait_lgkm0();
            barrier();
            ST(2 + j);
            if constexpr (j == 0) tag = (unsigned)__builtin_amdgcn_readfirstlane((int)epoch) + 1u;
            if constexpr (j == 1) {
                const int gw = G.bid * 8 + wave;
                const char* p = lane < 7 ? (const char*)a.w2 + (size_t)(gw * 7 + lane) * 128 : (const char*)a.wo + (size_t)(gw * 2 + (lane & 1)) * 128;
                touch_ptr<L_DUMMY>(p, ldsb);
            }
            SCHED0();
        }
        if constexpr (g == 31) {
            wait_lgkm0();
            barrier();
            SCHED0();
            f32x2 sa = *(LAS f32x2*)(lds + L_RSP + (32 * wm + r32) * 8), sb = *(LAS f32x2*)(lds + L_RSP + (128 + 32 * wm + r32) * 8);
            const int row2 = (G.mt2 & 1) * 64 + (wave & 1) * 32 + r32;
            f32x2 sc = *(LAS f32x2*)(lds + L_RSP + row2 * 8), sd = *(LAS f32x2*)(lds + L_RSP + (128 + row2) * 8);
            const float mean = (sa[0] + sb[0]) * (1.f / DM);
            vpe = fmx((sa[1] + sb[1]) * (1.f / DM) - mean * mean, 0.f) + 1e-5f;
            const float mean2 = (sc[0] + sd[0]) * (1.f / DM);
            vp2 = fmx((sc[1] + sd[1]) * (1.f / DM) - mean2 * mean2, 0.f) + 1e-5f;
            r_t = __builtin_amdgcn_rsqf(vpe);
            unsigned s0, s1, s2;
            split3(hh ? vpe * r_t : -mean, s0, s1, s2);
            u32x4 u;
            u[0] = hi2(s0, s1);
            u[1] = hh ? (s2 >> 16) : u[0];
            u[2] = hh ? 0u : hi2(s2, s0);
            u[3] = 0u;
            fixT = asbf(u);
            if (QKV) *(LAS float*)(lds + L_RR + (wave * 32 + r32) * 4) = r_t;
#pragma unroll
            for (int t = 0; t < NTW; ++t) {
                LAS float* p = (LAS float*)(lds + L_CSP) + tile_of(t) * 32 + r32;
                unsigned c0, c1, c2;
                split3((p[0] + p[224]) + (p[448] + p[672]), c0, c1, c2);
                unsigned bb = 0;
                if (!QKV) bb = *(LAS unsigned short*)(lds + L_AUX + (tile_of(t) * 32 + r32) * 2);
                u32x4 w;
                w[0] = hh ? (bb | (bb << 16)) : hi2(c0, c0);
                w[1] = hh ? bb : hi2(c1, c1);
                w[2] = hh ? 0u : hi2(c0, c2);
                w[3] = 0u;
                fixF[t] = asbf(w);
            }
        }
        if constexpr (QKV && g == 29) {
#pragma unroll
            for (int s = 0; s < 2; ++s) {
                u32x4 u;
#pragma unroll
                for (int i = 0; i < 4; ++i) {
                    int f0 = 16 * s + 8 * (i >> 1) + 4 * hh + 2 * (i & 1);
                    u[i] = (r32 == f0 ? 0x3F80u : 0u) | (r32 == f0 + 1 ? 0x3F800000u : 0u);
                }
                idf[s] = asbf(u);
            }
#pragma unroll
            for (int X = 0; X < 2; ++X) {
                LAS char* p = lds + L_AUX + (2 * WN + X) * 512 + (4 * hh + q4) * 32 + p4 * 8;
                u32x4 u = __builtin_bit_cast(u32x4, cat44(tr_read(p), tr_read(p + 256)));
                const unsigned sg = blk ? 0x80008000u : 0u;
#pragma unroll
                for (int i = 0; i < 4; ++i) u[i] ^= sg;
                wfr[X] = asbf(u);
            }
        }
        constexpr int jb = (g - 2) >> 2, q = (g - 2) & 3;
        static_for<0, NTW>([&](auto TI) {
            constexpr int t = TI.value;
            if (QKV && t == 2) acc[t] = MFMA32(xf[g % 3], wf[g % 3][t], acc[t]);
            else acc[t] = MFMA32(wf[g % 3][t], xf[g % 3], acc[t]);
            SCHED0();
            if constexpr ((g & 1) == 0) { add_c(t, 0); add_c(t, 1); }
            if constexpr (g < 30 && (g & 1) == 1 && t < 2) { add_x(g + 1 - WN, 2 * t); add_x(g + 1 - WN, 2 * t + 1); }
            if constexpr (g == 0 && WN == 0 && t < 2) { add_x(0, 2 * t); add_x(0, 2 * t + 1); }
            if constexpr (g == 30 && WN == 1 && t < 2) { add_x(31, 2 * t); add_x(31, 2 * t + 1); }
            if constexpr (g + 2 < 32) {
                if (t == NTW - 1) read_x(g + 2);
                read_w(g + 2, t);
                if constexpr ((g & 1) == 0) read_c(g + 2, t);
            }
            if constexpr (g < 2) {
                if constexpr (WN == 0) { dma_pair(IC(2), IC(g * 3 + t)); if constexpr (g == 1 && t == 2 && NT == 7) dma_pair(IC(2), IC(DW - 1)); }
                else if constexpr (t < 2) dma_pair(IC(2), IC(2 * g + t));
            } else {
                constexpr int i = WN == 0 ? (t < 2 ? 2 * q + t : 99) : (t == 1 ? q : 99);
                if constexpr (jb <= 4) { if constexpr (i < DW) dma_pair(IC(jb + 3), IC(i)); }
                else if constexpr (jb <= 6) { if constexpr (i < D2) dma_w2(IC(jb - 5), IC(i)); }
            }
            if constexpr (g != 29 && g != 31) SCHED0();
        });
        if constexpr (g == 30) {
#pragma unroll
            for (int t = 0; t < NTW; ++t) {
                const float v = half_sum(cs0[t] + cs1[t]);
                *(LAS float*)(lds + L_CSP + (wm * 224 + tile_of(t) * 32 + r32) * 4) = v;
            }
            auto sw = __builtin_amdgcn_permlane32_swap(f2u(rs0 + rs1), f2u(rq0 + rq1), false, false);
            *(LAS float*)(lds + L_RSP + (WN * 128 + 32 * wm + r32) * 8 + hh * 4) = u2f(sw[0]) + u2f(sw[1]);
        }
    });
    SCHED0();
#pragma unroll
    for (int t = 0; t < NTW; ++t) {
        if (QKV && t == 2) acc[t] = MFMA32(fixT, fixF[t], acc[t]);
        else acc[t] = MFMA32(fixF[t], fixT, acc[t]);
    }
    ST(10);
    const int tt = mt * 4 + wm;
    if constexpr (!QKV) {
#pragma unroll
        for (int t = 0; t < NTW; ++t) {
#pragma unroll
            for (int i = 0; i < 16; ++i) { float v = fmx(acc[t][i], 0.f); acc[t][i] = v * v; }
            const unsigned off = WS_H + (unsigned)((tt * 112 + nt * 14 + tile_of(t) * 2) * 64 + lane) * 16;
            __builtin_amdgcn_raw_buffer_store_b128(pack8u<0>(acc[t]), rs, off, 0, SC1);
            __builtin_amdgcn_raw_buffer_store_b128(pack8u<1>(acc[t]), rs, off + 1024, 0, SC1);
        }
        ST(12);
        wait_vm<0>();
        wait_lgkm0();
        barrier();
        if (wave == 0 && lane == 0)
            __hip_atomic_store((GAS unsigned*)(a.ws + WS_FLAG + (mt * 16 + 8 + nt) * 64), tag, RLX_AGENT);
    } else {
        bf16x8 vfa[2], vfb[2];
        {
#pragma unroll
            for (int gq = 0; gq < 4; ++gq) rr[gq] = *(LAS f32x4*)(lds + L_RR + (wave * 32 + 8 * gq + 4 * hh) * 4);
#pragma unroll
            for (int g = 0; g < 4; ++g)
#pragma unroll
                for (int i = 0; i < 4; ++i) acc[2][4 * g + i] *= rr[g][i];
            u32x4 v0 = pack8u<0>(acc[2]), v1 = pack8u<1>(acc[2]);
            u32x4 a0, a1, b0, b1;
#pragma unroll
            for (int i = 0; i < 4; ++i) {
                a0[i] = blk ? 0x3F803F80u : v0[i]; a1[i] = blk ? 0x3F803F80u : v1[i];
                b0[i] = blk ? v0[i] : 0x3F803F80u; b1[i] = blk ? v1[i] : 0x3F803F80u;
            }
            vfa[0] = asbf(a0); vfa[1] = asbf(a1); vfb[0] = asbf(b0); vfb[1] = asbf(b1);
        }
        const float kscale = r_t * LOG2E;
        f32x16 zero16;
#pragma unroll
        for (int i = 0; i < 16; ++i) zero16[i] = 0.f;
        auto feature_map = [&](f32x16& p, u32x4& F0, u32x4& F1) {
            float mp = fmx(fmx(fmx(fmx(p[0], p[1]), p[2]), fmx(fmx(p[3], p[4]), p[5])), fmx(p[6], p[7]));
            float mn = fmx(fmx(fmx(fmx(p[8], p[9]), p[10]), fmx(fmx(p[11], p[12]), p[13])), fmx(p[14], p[15]));
            {
                auto s = __builtin_amdgcn_permlane32_swap(f2u(mp), f2u(mn), false, false);
                float c = -kscale * fmx(u2f(s[0]), u2f(s[1]));
                auto s2 = __builtin_amdgcn_permlane32_swap(f2u(c), f2u(c), false, false);
                mp = u2f(s2[0]); mn = u2f(s2[1]);
            }
#pragma unroll
            for (int i = 0; i < 8; ++i) {
                p[i] = __builtin_amdgcn_exp2f(__builtin_fmaf(p[i], kscale, mp));
                p[8 + i] = __builtin_amdgcn_exp2f(__builtin_fmaf(p[8 + i], kscale, mn));
            }
            float sp = ((p[0] + p[1]) + (p[2] + p[3])) + ((p[4] + p[5]) + (p[6] + p[7]));
            float sn = ((p[8] + p[9]) + (p[10] + p[11])) + ((p[12] + p[13]) + (p[14] + p[15]));
            {
                auto s = __builtin_amdgcn_permlane32_swap(f2u(sp), f2u(sn), false, false);
                float c = __builtin_amdgcn_rcpf(u2f(s[0]) + u2f(s[1]));
                auto s2 = __builtin_amdgcn_permlane32_swap(f2u(c), f2u(c), false, false);
                sp = u2f(s2[0]); sn = u2f(s2[1]);
            }
#pragma unroll
            for (int i = 0; i < 8; ++i) { p[i] *= sp; p[8 + i] *= sn; }
            F0 = pack8u<0>(p); F1 = pack8u<1>(p);
        };
        {
            f32x16 p[2];
            p[0] = MFMA32(wfr[0], pack8<0>(acc[1]), zero16);
            p[1] = MFMA32(wfr[1], pack8<1>(acc[1]), zero16);
#pragma unroll
            for (int X = 0; X < 2; ++X) {
                u32x4 F0, F1;
                feature_map(p[X], F0, F1);
                f32x16 d = MFMA32(asbf(F0), idf[0], zero16);
                d = MFMA32(asbf(F1), idf[1], d);
                f32x16 kv = MFMA32(pack8<0>(d), X == 0 ? vfa[0] : vfb[0], zero16);
                kv = MFMA32(pack8<1>(d), X == 0 ? vfa[1] : vfb[1], kv);
                const int row = X == 0 ? r32 : (r32 == 15 ? 16 : r32 - 16);
                if (X == 0 ? (r32 <= 16) : (r32 >= 15)) {
                    LAS char* dst = lds + L_RED + (((WN * 2 + X) * 4 + wm) * 17 + row) * 144 + hh * 16;
#pragma unroll
                    for (int g = 0; g < 4; ++g) {
                        f32x4 v = {kv[4 * g], kv[4 * g + 1], kv[4 * g + 2], kv[4 * g + 3]};
                        *(LAS f32x4*)(dst + g * 32) = v;
                    }
                }
            }
        }
        wait_vm<0>();
        wait_lgkm0();
        ST(26);
        barrier();
        {
            const int tid = wave * 64 + lane;
            for (int o = tid; o < 4 * 136; o += 512) {
                int hl = o / 136, rem = o - hl * 136;
                LAS char* src = lds + L_RED + (hl * 4 * 17 + (rem >> 3)) * 144 + (rem & 7) * 16;
                f32x4 v = (*(LAS f32x4*)(src) + *(LAS f32x4*)(src + 2448)) + (*(LAS f32x4*)(src + 2 * 2448) + *(LAS f32x4*)(src + 3 * 2448));
                const unsigned off = WS_KVP + (unsigned)((mt * 32 + nt * 4 + hl) * 136 + rem) * 32;
                u32x4 g0 = {f2u(v[0]), tag, f2u(v[1]), tag}, g1 = {f2u(v[2]), tag, f2u(v[3]), tag};
                __builtin_amdgcn_raw_buffer_store_b128(g0, rs, off, 0, SC1);
                __builtin_amdgcn_raw_buffer_store_b128(g1, rs, off + 16, 0, SC1);
            }
        }
        ST(27);
        {
            f32x16 p[2];
            p[0] = MFMA32(wfr[0], pack8<0>(acc[0]), zero16);
            p[1] = MFMA32(wfr[1], pack8<1>(acc[0]), zero16);
#pragma unroll
            for (int X = 0; X < 2; ++X) {
                u32x4 F0, F1;
                feature_map(p[X], F0, F1);
                LAS char* dst = lds + phi_addr((2 * WN + X) * 8) + wm * 2048 + lane * 16;
                *(LAS u32x4*)(dst) = F0;
                *(LAS u32x4*)(dst + 1024) = F1;
            }
        }
        if (wave == 0 && lane == 0) *(LAS unsigned*)(lds + L2_CNT) = 0u;
        ST(12);
        wait_lgkm0();
        barrier();
        if (WN == 1) {
            LAS char* src = lds + (wm == 0 ? phi_addr(0) : phi_addr(8) + (wm - 1) * 8192) + lane * 16;
#pragma unroll
            for (int i = 0; i < 8; ++i) phi[i >> 1][i & 1] = *(LAS u32x4*)(src + i * 1024);
            wait_lgkm0();
        }
        barrier();
    }
    ST(13);
    return tag;
}

#define HPV 5
constexpr int HP = HPV;
constexpr int p2_pos(bool isw, int idx) {
    int pos = 0;
    for (int s = 0; s < HP; ++s) { pos += 2; if (!isw && idx == s) return pos; }
    for (int i = 0; i < 28; ++i) {
        if (i >= NB && i + NB <= 27) { pos += 2; if (isw && idx == i + NB) return pos; }
        if (i + HP <= 27) { pos += 2; if (!isw && idx == i + HP) return pos; }
    }
    return 0;
}
constexpr int p2_issued(int step) {
    int pos = 2 * HP;
    for (int i = 0; i < step; ++i) { if (i >= NB && i + NB <= 27) pos += 2; if (i + HP <= 27) pos += 2; }
    return pos;
}
constexpr int p2_wait(int i) {
    int need = p2_pos(false, i);
    if (i + 1 >= 2 * NB && i + 1 <= 27) { int w = p2_pos(true, i + 1); if (w > need) need = w; }
    return p2_issued(i) - need;
}
template <int SLEEP> DI void poll_words(srd_t rs, unsigned voff, unsigned tag) {
    for (unsigned spin = 0; spin <= SPIN_CAP; ++spin) {
        unsigned v;
        ld4c<0>(v, voff, rs);
        asm volatile("s_waitcnt vmcnt(0) ; PINW %0" : "+v"(v) :: "memory");
        if (__all(v == tag)) break;
        __builtin_amdgcn_s_sleep(SLEEP);
    }
}
DI void poll_flags2(srd_t rs, int f0, unsigned tag, int lane) { poll_words<4>(rs, WS_FLAG + (unsigned)(f0 + (lane & 1)) * 64, tag); }

DI void phase2(const Args& a, LAS char* lds, const Geo& G, unsigned tag, u32x4 (&phi)[4][2], float vp2, unsigned long long* stamps) {
    const int lane = G.lane, wave = G.wave, mt = G.mt2, nt = G.nt2;
    const unsigned ldsb = (unsigned)(size_t)lds;
    const int hh = lane >> 5, r32 = lane & 31, blk = (lane >> 4) & 1, q4 = (lane & 15) >> 2, p4 = lane & 3;
    const int kq = wave & 3, hf = wave >> 2;
    const srd_t rs = mk_srd(a.ws, WS_END);
    const unsigned wvo = (unsigned)((((lane >> 2) & 7) * DM + nt * 64 + hh * 32 + (lane & 3) * 8) * 2);
    const unsigned trl = (4 * hh + q4) * 64 + blk * 32 + p4 * 8;
    const unsigned l16 = lane * 16;
    const int token = mt * 64 + (kq & 1) * 32 + r32;
    const int col0 = nt * 64 + (kq >> 1) * 32 + 16 * hf + 4 * hh;
    u32x2 xr0, xr1, br0, br1;
    auto final_loads = [&]() {
        const srd_t rx = mk_srd(a.x, SEQ * DM * 2), rb = mk_srd(a.b2, DM * 2);
        ld8<0>(xr0, (unsigned)(token * DM + col0) * 2, rx); ld8<16>(xr1, (unsigned)(token * DM + col0) * 2, rx);
        ld8<0>(br0, (unsigned)col0 * 2, rb); ld8<16>(br1, (unsigned)col0 * 2, rb);
    };
    f32x16 acc[2][2];
#pragma unroll
    for (int i = 0; i < 2; ++i)
#pragma unroll
        for (int j = 0; j < 2; ++j)
#pragma unroll
            for (int e = 0; e < 16; ++e) acc[i][j][e] = 0.f;
    if (wave < 4) {
        const srd_t rw2 = mk_srd(a.w2, FF * DM * 2);
        const unsigned w2vo = wvo + kq * (448 * DM * 2);
        const unsigned lb = ldsb + kq * 2048;
        const unsigned hvo = (unsigned)((2 * mt) * 112 + 28 * kq) * 1024 + l16;
        auto issue_w = [&](auto SI) {
            constexpr int s = SI.value;
            dma16<(s % NB) * 8192, s * 16 * DM * 2>(w2vo, rw2, lb);
            dma16<(s % NB) * 8192 + 1024, (s * 16 + 8) * DM * 2>(w2vo, rw2, lb);
        };
        u32x4 hb[HP][2];
        auto issue_h = [&](auto SI) {
            constexpr int s = SI.value;
            ld16c<WS_H + s * 1024>(hb[s % HP][0], hvo, rs);
            ld16c<WS_H + (112 + s) * 1024>(hb[s % HP][1], hvo, rs);
        };
        issue_w(IC(10));
        LAS char* rdA = lds + kq * 2048 + trl;
        LAS char* rdB = rdA + 6 * 8192;
        PIN(rdB);
        bf16x8 wr[NB][2], wf[2][2];
        auto rd = [&](int s, bf16x8 (&d)[2]) {
            LAS char* p = (s % NB) < 6 ? rdA + (s % NB) * 8192 : rdB + (s % NB - 6) * 8192;
            d[0] = cat44(tr_read(p), tr_read(p + 1024));
            d[1] = cat44(tr_read(p + 512), tr_read(p + 1536));
        };
#pragma unroll
        for (int s = 0; s < 10; ++s) rd(s, wr[s]);
        wait_lgkm0();
        static_for<NB, 2 * NB - 1>([&](auto SI) { issue_w(SI); wait_vm<8>(); });
        rd(10, wr[10]);
        wait_lgkm0();
        issue_w(IC(2 * NB - 1));
        final_loads();
#pragma unroll
        for (int s = 0; s < NB; ++s) { PIN(wr[s][0]); PIN(wr[s][1]); }
        ST(24);
        poll_flags2(rs, (mt >> 1) * 16 + 8 + 2 * kq, tag, lane);
        ST(14);
        unsigned tch0, tch1;
        {
            const unsigned tvo = (unsigned)((2 * mt) * 112 + 28 * kq) * 1024 + (unsigned)((lane < 56 ? lane : 55) * 4 + (nt & 3)) * 128;
            ld4c<WS_H>(tch0, tvo, rs); ld4c<WS_H + 112 * 1024>(tch1, tvo, rs);
        }
        static_for<0, HP>([&](auto SI) { issue_h(SI); });
        static_for<0, 28>([&](auto SI) {
            constexpr int s = SI.value;
            SCHED0();
            asm volatile("s_waitcnt vmcnt(%2) ; PINW %0 %1" : "+v"(hb[s % HP][0]), "+v"(hb[s % HP][1]) : "i"(p2_wait(s)) : "memory");
            if constexpr (s == 0) { asm volatile("; PINW %0 %1" : "+v"(tch0), "+v"(tch1)); ST(15); }
            if constexpr (s + 1 >= NB && s + 1 < 28) rd(s + 1, wf[(s + 1) & 1]);
#pragma unroll
            for (int i = 0; i < 2; ++i)
#pragma unroll
                for (int j = 0; j < 2; ++j) acc[i][j] = MFMA32(s < NB ? wr[s < NB ? s : 0][i] : wf[s & 1][i], asbf(hb[s % HP][j]), acc[i][j]);
            SCHED0();
            if constexpr (s >= NB && s + NB < 28) issue_w(IC(s + NB));
            if constexpr (s + HP < 28) issue_h(IC(s + HP));
        });
        SCHED0();
        ST(16);
        wait_lgkm0();
#pragma unroll
        for (int qd = 0; qd < 4; ++qd)
#pragma unroll
            for (int g = 0; g < 4; ++g) {
                const f32x16& c = acc[qd >> 1][qd & 1];
                f32x4 v = {c[4 * g], c[4 * g + 1], c[4 * g + 2], c[4 * g + 3]};
                *(LAS f32x4*)(lds + (qd * 2 + (g >> 1)) * 8192 + kq * 2048 + (g & 1) * 1024 + l16) = v;
            }
    } else {
        LAS char* myreg = lds + L2_A + kq * 16384;
        const srd_t rwo = mk_srd(a.wo, DM * DM * 2);
        __builtin_amdgcn_s_setprio(3);
        auto load_wo = [&]() { static_for<0, 16>([&](auto P) { dma16<L2_A + P.value * 1024, P.value * 8 * DM * 2>(wvo + kq * (128 * DM * 2), rwo, ldsb + kq * 16384); }); };
        if (!G.qkv) {
            if (kq >= 2) {
                const int sl = (G.mt * 8 + G.nt) * 2 + (kq & 1);
                const int head = sl >> 3, g = (sl & 7) * 17 + (lane < 34 ? (lane >> 1) : 0);
                const unsigned vo = (unsigned)((head * 136 + g) * 32 + (lane & 1) * 16);
                u32x4 v[16];
                for (unsigned spin = 0; spin <= SPIN_CAP; ++spin) {
                    static_for<0, 16>([&](auto M) { ld16c<WS_KVP + M.value * 32 * 136 * 32>(v[M.value], vo, rs); });
                    wait16(v);
                    unsigned bad = 0;
#pragma unroll
                    for (int m = 0; m < 16; ++m) bad |= (v[m][1] ^ tag) | (v[m][3] ^ tag);
                    if (__all(bad == 0u)) break;
                    __builtin_amdgcn_s_sleep(4);
                }
                float s0[16], s1[16];
#pragma unroll
                for (int m = 0; m < 16; ++m) { s0[m] = u2f(v[m][0]); s1[m] = u2f(v[m][2]); }
#pragma unroll
                for (int st = 8; st >= 1; st >>= 1)
#pragma unroll
                    for (int m = 0; m < st; ++m) { s0[m] += s0[m + st]; s1[m] += s1[m + st]; }
                const int row = g >> 3, fq = g & 7;
                const unsigned off = WS_KVR + head * 2176 + fq * 272 + row * 16 + (lane & 1) * 8;
                if (lane < 34)
                    __hip_atomic_store((GAS unsigned long long*)(a.ws + off), (unsigned long long)pk2(s0[0], s1[0]) | ((unsigned long long)tag << 32), RLX_AGENT);
            }
            ST(24);
            load_wo();
        } else {
            const unsigned kvo = (unsigned)((4 * G.nt + kq) * 2176 + hh * 272 + (r32 < 16 ? r32 : 16) * 16);
            u32x4 raw[4];
            for (unsigned spin = 0; spin <= SPIN_CAP; ++spin) {
                static_for<0, 4>([&](auto I) { ld16c<WS_KVR + I.value * 544>(raw[I.value], kvo, rs); });
                asm volatile("s_waitcnt vmcnt(0) ; PINW %0 %1 %2 %3" : "+v"(raw[0]), "+v"(raw[1]), "+v"(raw[2]), "+v"(raw[3]) :: "memory");
                unsigned bad = 0;
#pragma unroll
                for (int i = 0; i < 4; ++i) bad |= (raw[i][1] ^ tag) | (raw[i][3] ^ tag);
                if (__all(bad == 0u)) break;
                __builtin_amdgcn_s_sleep(4);
            }
            ST(24);
            f32x16 zero16;
#pragma unroll
            for (int i = 0; i < 16; ++i) zero16[i] = 0.f;
            const u32x4 k0 = {raw[0][0], raw[0][2], raw[1][0], raw[1][2]}, k1 = {raw[2][0], raw[2][2], raw[3][0], raw[3][2]};
#pragma unroll
            for (int w = 0; w < 4; ++w) {
                f32x16 nd = MFMA32(asbf(k0), asbf(phi[w][0]), zero16);
                nd = MFMA32(asbf(k1), asbf(phi[w][1]), nd);
                const float inv = __builtin_amdgcn_rcpf(nd[8] + 1e-6f);
#pragma unroll
                for (int e = 0; e < 8; ++e) nd[e] *= inv;
                __builtin_amdgcn_raw_buffer_store_b128(pack8u<0>(nd), rs, WS_O + (unsigned)(((G.mt * 4 + w) * 32 + G.nt * 4 + kq) * 1024) + l16, 0, SC1);
            }
            wait_vm<0>();
            unsigned old = 0;
            if (lane == 0) old = __hip_atomic_fetch_add((LAS unsigned*)(lds + L2_CNT), 1u, __ATOMIC_RELAXED, __HIP_MEMORY_SCOPE_WORKGROUP);
            if (lane == 0 && old == 3u) __hip_atomic_store((GAS unsigned*)(a.ws + WS_FLAG + (G.mt * 16 + G.nt) * 64), tag, RLX_AGENT);
            ST(25);
            load_wo();
        }
        final_loads();
        poll_flags2(rs, (mt >> 1) * 16 + 2 * kq, tag, lane);
        ST(14);
        const unsigned ovo = (unsigned)((2 * mt) * 32 + 8 * kq) * 1024 + l16;
        u32x4 ob[16];
        static_for<0, 16>([&](auto I) { constexpr int i = I.value; ld16c<WS_O + (i & 1) * 32768 + (i >> 1) * 1024>(ob[i], ovo, rs); });
        bf16x8 wv[8][2];
#pragma unroll
        for (int h = 0; h < 8; ++h) {
            LAS char* p = myreg + h * 2048 + trl;
            wv[h][0] = cat44(tr_read(p), tr_read(p + 1024));
            wv[h][1] = cat44(tr_read(p + 512), tr_read(p + 1536));
        }
        wait16(ob);
        ST(15);
#pragma unroll
        for (int h = 0; h < 8; ++h)
#pragma unroll
            for (int i = 0; i < 2; ++i)
#pragma unroll
                for (int j = 0; j < 2; ++j) acc[i][j] = MFMA32(wv[h][i], asbf(ob[2 * h + j]), acc[i][j]);
        ST(16);
        wait_lgkm0();
#pragma unroll
        for (int qd = 0; qd < 4; ++qd)
#pragma unroll
            for (int g = 0; g < 4; ++g) {
                const f32x16& c = acc[qd >> 1][qd & 1];
                f32x4 v = {c[4 * g], c[4 * g + 1], c[4 * g + 2], c[4 * g + 3]};
                *(LAS f32x4*)(myreg + (qd * 4 + g) * 1024 + l16) = v;
            }
    }
    asm volatile("s_waitcnt vmcnt(0) ; PINW %0 %1 %2 %3" : "+v"(xr0), "+v"(xr1), "+v"(br0), "+v"(br1) :: "memory");
    wait_lgkm0();
    barrier();
    ST(17);
    const float r2 = __builtin_amdgcn_rcpf(vp2);
#pragma unroll
    for (int g = 0; g < 2; ++g) {
        LAS char* pf = lds + (kq * 2 + hf) * 8192 + g * 1024 + l16;
        LAS char* pa = lds + L2_A + (kq * 4 + 2 * hf + g) * 1024 + l16;
        f32x4 so = (*(LAS f32x4*)(pf) + *(LAS f32x4*)(pf + 2048)) + (*(LAS f32x4*)(pf + 4096) + *(LAS f32x4*)(pf + 6144));
        f32x4 ss = (*(LAS f32x4*)(pa) + *(LAS f32x4*)(pa + 16384)) + (*(LAS f32x4*)(pa + 2 * 16384) + *(LAS f32x4*)(pa + 3 * 16384));
        const u32x2 xr = g ? xr1 : xr0, br = g ? br1 : br0;
        float res[4];
#pragma unroll
        for (int i = 0; i < 4; ++i) {
            const float xv = u2f((i & 1) ? (xr[i >> 1] & 0xFFFF0000u) : (xr[i >> 1] << 16));
            const float bv = u2f((i & 1) ? (br[i >> 1] & 0xFFFF0000u) : (br[i >> 1] << 16));
            const float o = so[i] * r2 + bv;
            const float gt = o * __builtin_amdgcn_rcpf(1.f + __builtin_amdgcn_exp2f(-o * LOG2E));
            res[i] = xv + ss[i] * gt;
        }
        u32x2 ov = {pk2(res[0], res[1]), pk2(res[2], res[3])};
        *(GAS u32x2*)(a.out + (size_t)token * DM + col0 + 8 * g) = ov;
    }
    ST(18);
    if (G.bid == 0 && wave == 7) {
        for (unsigned spin = 0; spin <= SPIN_CAP; ++spin) {
            unsigned f0, f1, f2, f3;
            ld4c<WS_FLAG>(f0, (unsigned)lane * 64, rs); ld4c<WS_FLAG + 4096>(f1, (unsigned)lane * 64, rs);
            ld4c<WS_FLAG + 8192>(f2, (unsigned)lane * 64, rs); ld4c<WS_FLAG + 12288>(f3, (unsigned)lane * 64, rs);
            asm volatile("s_waitcnt vmcnt(0) ; PINW %0 %1 %2 %3" : "+v"(f0), "+v"(f1), "+v"(f2), "+v"(f3) :: "memory");
            if (__all(f0 == tag && f1 == tag && f2 == tag && f3 == tag)) break;
            __builtin_amdgcn_s_sleep(1);
        }
        if (lane == 0) __hip_atomic_store((GAS unsigned*)(a.ws + WS_CTRL), tag, RLX_AGENT);
    }
}

__global__ void __launch_bounds__(512, 2) fused_kernel(Args a) {
    extern __shared__ __attribute__((aligned(16))) char smem[];
    LAS char* lds = (LAS char*)smem;
    Geo G;
    G.bid = blockIdx.x;
    G.wave = __builtin_amdgcn_readfirstlane(threadIdx.x >> 6);
    G.lane = threadIdx.x & 63;
    const int xcd = G.bid & 7, loc = G.bid >> 3;
    const int sel = loc >> 2;
    G.mt = (xcd >> 1) * 4 + (loc & 3);
    G.nt = (xcd & 1) * 4 + (sel & 3);
    G.mt2 = G.mt * 2 + (xcd & 1);
    G.nt2 = sel;
    G.qkv = sel < 4;
    unsigned long long stamps[28];
    unsigned tag;
    float vp2 = 1.f;
    u32x4 phi[4][2];
#pragma unroll
    for (int i = 0; i < 8; ++i) phi[i >> 1][i & 1] = u32x4{0u, 0u, 0u, 0u};
    if (sel < 4) {
        if (G.wave < 4) tag = phase1<true, 0>(a, lds, G, phi, vp2, stamps);
        else tag = phase1<true, 1>(a, lds, G, phi, vp2, stamps);
    } else {
        if (G.wave < 4) tag = phase1<false, 0>(a, lds, G, phi, vp2, stamps);
        else tag = phase1<false, 1>(a, lds, G, phi, vp2, stamps);
    }
    phase2(a, lds, G, tag, phi, vp2, stamps);
}

extern "C" void kernel_launch(void* const* d_in, const int* in_sizes, int n_in, void* d_out, int out_size, void* d_ws,
                              size_t ws_size, hipStream_t stream) {
    Args a;
    a.x = (const __bf16*)d_in[0]; a.wqkv = (const __bf16*)d_in[1]; a.wphi = (const __bf16*)d_in[2]; a.wo = (const __bf16*)d_in[3];
    a.w1 = (const __bf16*)d_in[5]; a.b1 = (const __bf16*)d_in[6]; a.w2 = (const __bf16*)d_in[7]; a.b2 = (const __bf16*)d_in[8];
    a.ws = (char*)d_ws; a.out = (__bf16*)d_out;
    (void)hipFuncSetAttribute((const void*)fused_kernel, hipFuncAttributeMaxDynamicSharedMemorySize, L_TOTAL);
    fused_kernel<<<256, 512, L_TOTAL, stream>>>(a);
}
```

```cpp
#include <hip/hip_runtime.h>
#include <hip/hip_bf16.h>
#include <stdint.h>
#include <type_traits>

typedef __attribute__((ext_vector_type(8))) __bf16 bf16x8;
typedef __attribute__((ext_vector_type(4))) __bf16 bf16x4;
typedef __attribute__((ext_vector_type(2))) __bf16 bf16x2;
typedef __attribute__((ext_vector_type(16))) float f32x16;
typedef __attribute__((ext_vector_type(4))) float f32x4;
typedef __attribute__((ext_vector_type(2))) float f32x2;
typedef __attribute__((ext_vector_type(4))) unsigned u32x4;
typedef __attribute__((ext_vector_type(2))) unsigned u32x2;
typedef __amdgpu_buffer_rsrc_t srd_t;
#define LAS __attribute__((address_space(3)))
#define GAS __attribute__((address_space(1)))
#define DI __device__ __forceinline__
#define MFMA32(a, b, c) __builtin_amdgcn_mfma_f32_32x32x16_bf16((a), (b), (c), 0, 0, 0)
#define SCHED0() __builtin_amdgcn_sched_barrier(0)
#define CFENCE() asm volatile("" ::: "memory")
#define RLX_AGENT __ATOMIC_RELAXED, __HIP_MEMORY_SCOPE_AGENT

constexpr int SEQ = 2048, DM = 512, NH = 32, FF = 1792;
constexpr float LOG2E = 1.4426950408889634f;
constexpr int SC1 = 16;
constexpr unsigned SPIN_CAP = 1u << 16;

constexpr unsigned WS_CTRL = 0;
constexpr unsigned WS_FLAG = 256;
constexpr unsigned WS_RST  = WS_FLAG + 256 * 64;
constexpr unsigned WS_CSG  = WS_RST + 2048 * 16;
constexpr unsigned WS_KVR  = WS_CSG + 3328 * 32;
constexpr unsigned WS_KVP  = WS_KVR + 32 * 2176;
constexpr unsigned WS_H    = (WS_KVP + 16u * 32 * 136 * 32 + 4095) & ~4095u;
constexpr unsigned WS_O    = WS_H + 64u * 112 * 1024;
constexpr unsigned WS_END  = WS_O + 64u * 32 * 1024;

#define ST(i)

constexpr int L_UNIT = 135168;
constexpr int L_CSP = L_UNIT + 16384;
constexpr int L_RR = L_CSP + 2048;
constexpr int L_VP = L_RR + 1024;
constexpr int L_AUX = L_VP + 512;
constexpr int L_DUMMY = L_AUX + 2048;
constexpr int L_WQ0 = 98304, L_WQ1 = 65536;
constexpr int L_RED = 0;
constexpr int L_PHI = 98304;
constexpr int NB = 11;
constexpr int L2_CNT = NB * 8192;
constexpr int L2_A = 98304;
constexpr int L_TOTAL = 163840;
static_assert(L_DUMMY + 256 <= L_TOTAL, "lds");

template <int LOFF, int SOFF> DI void dma16(unsigned voff, srd_t srd, unsigned lbase) {
    unsigned t;
    asm volatile("s_add_i32 m0, %3, %4\n\ts_mov_b32 %0, %5\n\tbuffer_load_dwordx4 %1, %2, %0 offen lds"
                 : "=&s"(t) : "v"(voff), "s"(srd), "s"(lbase), "i"(LOFF), "i"(SOFF) : "memory", "scc");
}
template <int LOFF> DI void touch_ptr(const void* p, unsigned lbase) {
    asm volatile("s_add_i32 m0, %1, %2\n\ts_nop 0\n\tglobal_load_lds_dword %0, off" :: "v"(p), "s"(lbase), "i"(LOFF) : "memory", "scc");
}
template <int LOFF> DI void touch_c(unsigned voff, srd_t srd, unsigned lbase) {
    asm volatile("s_add_i32 m0, %2, %3\n\ts_nop 0\n\tbuffer_load_dword %0, %1, 0 offen sc1 lds" :: "v"(voff), "s"(srd), "s"(lbase), "i"(LOFF) : "memory", "scc");
}
template <int LOFF> DI void touch(unsigned voff, srd_t srd, unsigned lbase) {
    asm volatile("s_add_i32 m0, %2, %3\n\ts_nop 0\n\tbuffer_load_dword %0, %1, 0 offen lds" :: "v"(voff), "s"(srd), "s"(lbase), "i"(LOFF) : "memory", "scc");
}
template <int OFF> DI void ld16c(u32x4& d, unsigned voff, srd_t srd) {
    asm volatile("buffer_load_dwordx4 %0, %1, %2, %3 offen offset:%4 sc1" : "=v"(d) : "v"(voff), "s"(srd), "s"(OFF & ~4095), "i"(OFF & 4095) : "memory");
}
template <int OFF> DI void ld4c(unsigned& d, unsigned voff, srd_t srd) {
    asm volatile("buffer_load_dword %0, %1, %2, %3 offen offset:%4 sc1" : "=v"(d) : "v"(voff), "s"(srd), "s"(OFF & ~4095), "i"(OFF & 4095) : "memory");
}
DI void ld16(u32x4& d, unsigned voff, srd_t srd) { asm volatile("buffer_load_dwordx4 %0, %1, %2, 0 offen" : "=v"(d) : "v"(voff), "s"(srd) : "memory"); }
template <int OFF> DI void ld8(u32x2& d, unsigned voff, srd_t srd) { asm volatile("buffer_load_dwordx2 %0, %1, %2, 0 offen offset:%3" : "=v"(d) : "v"(voff), "s"(srd), "i"(OFF) : "memory"); }
DI float fmx(float a, float b) { return __builtin_elementwise_maximum(a, b); }
DI void wait_lgkm0() { __builtin_amdgcn_s_waitcnt(0xC07F); CFENCE(); }
template <int N> DI void wait_vm() { asm volatile("s_waitcnt vmcnt(%0)" ::"i"(N) : "memory"); }
DI void barrier() { __builtin_amdgcn_s_barrier(); CFENCE(); }
#define PIN(x) asm volatile("" : "+v"(x))
DI srd_t mk_srd(const void* p, unsigned bytes) { return __builtin_amdgcn_make_buffer_rsrc((void*)p, 0, (int)bytes, 0x00020000); }

DI bf16x4 tr_read(LAS char* p) { return __builtin_amdgcn_ds_read_tr16_b64_v4bf16((LAS bf16x4*)p); }
DI bf16x8 cat44(bf16x4 a, bf16x4 b) { return __builtin_shufflevector(a, b, 0, 1, 2, 3, 4, 5, 6, 7); }
DI float dot2(unsigned a, unsigned b, float c) {
    return __builtin_amdgcn_fdot2_f32_bf16(__builtin_bit_cast(bf16x2, a), __builtin_bit_cast(bf16x2, b), c, false);
}
DI unsigned pk2(float a, float b) { f32x2 v = {a, b}; return __builtin_bit_cast(unsigned, __builtin_convertvector(v, bf16x2)); }
template <int S> DI u32x4 pack8u(const f32x16& x) {
    u32x4 p = {pk2(x[8 * S], x[8 * S + 1]), pk2(x[8 * S + 2], x[8 * S + 3]), pk2(x[8 * S + 4], x[8 * S + 5]), pk2(x[8 * S + 6], x[8 * S + 7])};
    return p;
}
template <int S> DI bf16x8 pack8(const f32x16& x) { return __builtin_bit_cast(bf16x8, pack8u<S>(x)); }
DI bf16x8 asbf(u32x4 u) { return __builtin_bit_cast(bf16x8, u); }
DI float u2f(unsigned u) { return __builtin_bit_cast(float, u); }
DI unsigned f2u(float f) { return __builtin_bit_cast(unsigned, f); }
DI unsigned hi2(unsigned lo, unsigned hi) { return __builtin_amdgcn_perm(hi, lo, 0x07060302u); }
DI void split3(float v, unsigned& s0, unsigned& s1, unsigned& s2) {
    s0 = f2u(v) & 0xFFFF0000u;
    float r = v - u2f(s0);
    s1 = f2u(r) & 0xFFFF0000u;
    s2 = f2u(r - u2f(s1));
}
DI float half_sum(float v) { auto s = __builtin_amdgcn_permlane32_swap(f2u(v), f2u(v), false, false); return u2f(s[0]) + u2f(s[1]); }
DI float wave_sum(float v) {
#define DPPADD(ctrl, rm) v += u2f((unsigned)__builtin_amdgcn_update_dpp(0, (int)f2u(v), ctrl, rm, 0xf, true))
    DPPADD(0x111, 0xf); DPPADD(0x112, 0xf); DPPADD(0x114, 0xf); DPPADD(0x118, 0xf); DPPADD(0x142, 0xa); DPPADD(0x143, 0xc);
#undef DPPADD
    return u2f((unsigned)__builtin_amdgcn_readlane((int)f2u(v), 63));
}
template <int C, int N, class F> DI void static_for(F&& f) { if constexpr (C < N) { f(std::integral_constant<int, C>{}); static_for<C + 1, N>(f); } }

#define TOUCH1 2
struct Args {
    const __bf16* x; const __bf16* wqkv; const __bf16* wphi; const __bf16* wo;
    const __bf16* w1; const __bf16* b1; const __bf16* w2; const __bf16* b2;
    char* ws; __bf16* out;
};
struct Geo { int bid, wave, lane, mt, nt, i16, mt2, nt2, qkv; };
#define IC(v) std::integral_constant<int, (v)>{}

template <int N, int NG> DI void gather_wait(u32x4& gst, u32x4 (&gcs)[NG]) {
    if constexpr (NG == 3) asm volatile("s_waitcnt vmcnt(%4) ; PINW %0 %1 %2 %3" : "+v"(gst), "+v"(gcs[0]), "+v"(gcs[1]), "+v"(gcs[2]) : "i"(N) : "memory");
    else asm volatile("s_waitcnt vmcnt(%5) ; PINW %0 %1 %2 %3 %4" : "+v"(gst), "+v"(gcs[0]), "+v"(gcs[1]), "+v"(gcs[2]), "+v"(gcs[NG - 1]) : "i"(N) : "memory");
}
DI void wait16(u32x4 (&v)[16]) {
    asm volatile("s_waitcnt vmcnt(0) ; PINW %0 %1 %2 %3 %4 %5 %6 %7 %8 %9 %10 %11 %12 %13 %14 %15"
                 : "+v"(v[0]), "+v"(v[1]), "+v"(v[2]), "+v"(v[3]), "+v"(v[4]), "+v"(v[5]), "+v"(v[6]), "+v"(v[7]),
                   "+v"(v[8]), "+v"(v[9]), "+v"(v[10]), "+v"(v[11]), "+v"(v[12]), "+v"(v[13]), "+v"(v[14]), "+v"(v[15]) :: "memory");
}

template <bool QKV, int WN>
DI unsigned phase1(const Args& a, LAS char* lds, const Geo& G, u32x4 (&phi)[4][2], float& vp2, unsigned long long* stamps) {
    constexpr int NT = QKV ? 4 : 7;
    constexpr int NTW = QKV ? 2 : (WN == 0 ? 3 : 4);
    constexpr int NG = QKV ? 3 : NTW;
    constexpr int NU = QKV ? 24 : 28;
    constexpr int LDW = QKV ? 1536 : 1792;
    constexpr int SLOT = QKV ? 32768 : 45056, XOFF = NT * 4096;
    constexpr int DW = WN == 0 ? NT : 4;
    constexpr int D2 = WN == 0 ? 6 : 4;
    const int lane = G.lane, wave = G.wave, wm = wave & 3, mt = G.mt, nt = G.nt;
    const unsigned ldsb = (unsigned)(size_t)lds;
    const int hh = lane >> 5, r32 = lane & 31, blk = (lane >> 4) & 1, q4 = (lane & 15) >> 2, p4 = lane & 3;
    const srd_t rs = mk_srd(a.ws, WS_END);
    const srd_t rx = mk_srd(a.x, SEQ * DM * 2);
    const srd_t rw = mk_srd(QKV ? a.wqkv : a.w1, DM * LDW * 2);
    const srd_t rw2 = mk_srd(a.w2, FF * DM * 2);
    ST(0);
    if (wave < TOUCH1) {
        const char* tp = (const char*)a.x + (size_t)(G.bid * 8 + (lane & 7)) * 1024 + wave * 128;
        if (lane >= 8 && lane < 14) tp = (const char*)a.wqkv + (size_t)wave * 196608 + (G.bid * 6 + lane - 8) * 128;
        if (lane >= 16 && lane < 23) tp = (const char*)a.w1 + (size_t)wave * 229376 + (G.bid * 7 + lane - 16) * 128;
        touch_ptr<L_DUMMY>(tp, ldsb);
    }
    auto colb_of = [&](int c) { return QKV ? ((c >> 1) * 512 + 64 * nt + 32 * (c & 1)) : (224 * nt + 32 * c); };
    unsigned dvo[DW];
#pragma unroll
    for (int i = 0; i < DW; ++i) {
        if (WN == 0) {
            int st = 2 * i + hh;
            int k8 = 2 * wm + (st >= NT ? 1 : 0), c = st >= NT ? st - NT : st;
            dvo[i] = (unsigned)(((k8 * 8 + ((lane >> 2) & 7)) * LDW + colb_of(QKV ? c + 2 : c) + (lane & 3) * 8) * 2);
        } else {
            int row = 8 * (4 * wm + i) + (lane >> 3);
            dvo[i] = (unsigned)(((mt * 128 + row) * DM + ((lane & 7) ^ ((row >> 1) & 7)) * 8) * 2);
        }
    }
    const unsigned lb_pair = ldsb + (WN == 0 ? wm * NT * 1024 : XOFF + wm * 4096);
    auto dma_pair = [&](auto JI, auto II) {
        constexpr int j = JI.value, i = II.value, sb = ((j + 1) % 3) * SLOT;
        if constexpr (WN == 0) dma16<sb + i * 1024, j * 64 * LDW * 2>(dvo[i], rw, lb_pair);
        else dma16<sb + i * 1024, j * 128>(dvo[i], rx, lb_pair);
    };
    const unsigned w2vo = (unsigned)(((wm * 448 + ((lane >> 2) & 7)) * DM + G.nt2 * 64 + hh * 32 + (lane & 3) * 8) * 2);
    const unsigned lb_w2 = ldsb + wm * 2048;
    auto dma_w2 = [&](auto RI, auto TI) {
        constexpr int r = RI.value, t = TI.value;
        dma16<r * 8192 + t * 1024, (r * 16 + t * 8) * DM * 2>(w2vo, rw2, lb_w2);
    };
    const unsigned wqvo = (unsigned)(((wave * 32 + ((lane >> 2) & 7)) * LDW + 64 * nt + hh * 32 + (lane & 3) * 8) * 2);
    auto dma_wq = [&](auto CI, auto II) {
        constexpr int c = CI.value, i = II.value;
        dma16<(c ? L_WQ1 : L_WQ0) + i * 1024, (c * 256 + i * 8) * LDW * 2>(wqvo, rw, ldsb + wave * 4096);
    };
    ST(20);
    static_for<0, DW>([&](auto I) { dma_pair(IC(0), I); });
    ST(21);
    barrier();
    unsigned epoch; u32x4 rowv;
    const int srow = mt * 128 + G.i16 * 8 + wave;
    const int u1 = mt + 16 < NU ? mt + 16 : mt;
    ld4c<WS_CTRL>(epoch, 0u, rs);
    ld16(rowv, (unsigned)(srow * DM * 2 + lane * 16), rx);
    {
        const unsigned rowpart = (unsigned)(((mt & 3) * 128 + 16 * wave + (lane >> 2)) * LDW + (lane & 3) * 8) * 2;
        dma16<L_UNIT, 0>(rowpart + colb_of(mt >> 2) * 2, rw, ldsb + wave * 1024);
        dma16<L_UNIT + 8192, 0>(rowpart + colb_of(u1 >> 2) * 2, rw, ldsb + wave * 1024);
    }
    static_for<0, DW>([&](auto I) { dma_pair(IC(1), I); });
    if (wave == 1) {
        if (QKV) {
            const srd_t rp = mk_srd(a.wphi, NH * 256 * 2);
            dma16<L_AUX, 0>((unsigned)(nt * 2048 + lane * 16), rp, ldsb);
            dma16<L_AUX + 1024, 1024>((unsigned)(nt * 2048 + lane * 16), rp, ldsb);
        } else {
            const srd_t rb = mk_srd(a.b1, FF * 2);
            dma16<L_AUX, 0>((unsigned)(nt * 448 + lane * 16), rb, ldsb);
        }
    }
    ST(22);

    LAS char* wb[3]; LAS char* xb[3][4];
#pragma unroll
    for (int s = 0; s < 3; ++s) {
        wb[s] = lds + s * SLOT + hh * (NT * 512) + q4 * 64 + blk * 32 + p4 * 8;
        PIN(wb[s]);
        const int row = 32 * wm + r32;
#pragma unroll
        for (int k = 0; k < 4; ++k) { xb[s][k] = lds + s * SLOT + XOFF + row * 128 + (((2 * k + hh) ^ ((row >> 1) & 7)) * 16); PIN(xb[s][k]); }
    }
    auto tile_of = [&](int t) { return QKV ? (2 * t + WN) : (WN * 3 + t); };
    const int csbase = QKV ? nt * 192 : 1536 + nt * 224;

    f32x16 acc[NTW];
#pragma unroll
    for (int t = 0; t < NTW; ++t)
#pragma unroll
        for (int i = 0; i < 16; ++i) acc[t][i] = 0.f;
    constexpr int NXF = QKV ? 32 : 3;
    bf16x8 xf[NXF], wf[3][NTW];
    auto read_x = [&](int g) { xf[g % NXF] = *(LAS bf16x8*)(xb[((g >> 2) + 1) % 3][g & 3]); };
    auto read_w = [&](int g, int t) {
        LAS char* p = wb[((g >> 2) + 1) % 3] + (g & 3) * 2 * NT * 512 + tile_of(t) * 512;
        wf[g % 3][t] = cat44(tr_read(p), tr_read(p + 256));
    };
    unsigned tag = 0;
    auto stat_task = [&]() {
        tag = (unsigned)__builtin_amdgcn_readfirstlane((int)epoch) + 1u;
        LAS char* p = lds + L_UNIT + wave * 1024 + (4 * hh + q4) * 64 + blk * 32 + p4 * 8;
        u32x2 ua = __builtin_bit_cast(u32x2, tr_read(p)), ub = __builtin_bit_cast(u32x2, tr_read(p + 512));
        u32x2 uc = __builtin_bit_cast(u32x2, tr_read(p + 8192)), ud = __builtin_bit_cast(u32x2, tr_read(p + 8192 + 512));
        float e[8];
#pragma unroll
        for (int i = 0; i < 4; ++i) { e[2 * i] = u2f(rowv[i] << 16); e[2 * i + 1] = u2f(rowv[i] & 0xFFFF0000u); }
        const float mean = wave_sum(((e[0] + e[1]) + (e[2] + e[3])) + ((e[4] + e[5]) + (e[6] + e[7]))) * (1.f / DM);
        float q = 0.f;
#pragma unroll
        for (int i = 0; i < 8; ++i) { float d = e[i] - mean; q = __builtin_fmaf(d, d, q); }
        const float vv = wave_sum(q) * (1.f / DM) + 1e-5f;
        u32x4 g = {f2u(mean), tag, f2u(vv), tag};
        if (lane == 0) __builtin_amdgcn_raw_buffer_store_b128(g, rs, WS_RST + srow * 16, 0, SC1);
        const unsigned one = 0x3F803F80u;
        float s0 = dot2(ub[1], one, dot2(ub[0], one, dot2(ua[1], one, dot2(ua[0], one, 0.f))));
        float s1 = dot2(ud[1], one, dot2(ud[0], one, dot2(uc[1], one, dot2(uc[0], one, 0.f))));
        auto sw = __builtin_amdgcn_permlane32_swap(f2u(s0), f2u(s1), false, false);
        *(LAS float*)(lds + L_CSP + ((hh * 8 + wave) * 32 + r32) * 4) = u2f(sw[0]) + u2f(sw[1]);
    };
    u32x4 gst, gcs[NG];
    const unsigned gvo_st = (unsigned)(mt * 128 + 32 * wm + r32) * 16;
    const unsigned gvo_cs = (unsigned)(csbase + tile_of(0) * 32 + r32) * 32 + hh * 16;
    auto gather = [&]() {
        ld16c<WS_RST>(gst, gvo_st, rs);
        static_for<0, NG>([&](auto T) { ld16c<WS_CSG + T.value * (QKV ? 2048 : 1024)>(gcs[T.value], gvo_cs, rs); });
    };
    float vpe = 1.f, r_t = 1.f;
    bf16x8 fixT, fixF[NG];
    auto gather_check_and_fix = [&]() {
        for (unsigned spin = 0;; ++spin) {
            unsigned bad = (gst[1] ^ tag) | (gst[3] ^ tag);
#pragma unroll
            for (int t = 0; t < NG; ++t) bad |= (gcs[t][1] ^ tag) | (gcs[t][3] ^ tag);
            if (__all(bad == 0u) || spin > SPIN_CAP) break;
            __builtin_amdgcn_s_sleep(1);
            gather();
            gather_wait<0, NG>(gst, gcs);
        }
        vpe = u2f(gst[2]);
        r_t = __builtin_amdgcn_rsqf(vpe);
        unsigned s0, s1, s2;
        split3(hh ? vpe * r_t : -u2f(gst[0]), s0, s1, s2);
        u32x4 u;
        u[0] = hi2(s0, s1);
        u[1] = hh ? (s2 >> 16) : u[0];
        u[2] = hh ? 0u : hi2(s2, s0);
        u[3] = 0u;
        fixT = asbf(u);
        if (QKV) *(LAS float*)(lds + L_RR + (wave * 32 + r32) * 4) = r_t;
        *(LAS float*)(lds + L_VP + (32 * wm + r32) * 4) = vpe;
#pragma unroll
        for (int t = 0; t < NG; ++t) {
            unsigned c0, c1, c2;
            split3(half_sum(u2f(gcs[t][0]) + u2f(gcs[t][2])), c0, c1, c2);
            unsigned bb = 0;
            if (!QKV) bb = *(LAS unsigned short*)(lds + L_AUX + (tile_of(t) * 32 + r32) * 2);
            u32x4 w;
            w[0] = hh ? (bb | (bb << 16)) : hi2(c0, c0);
            w[1] = hh ? bb : hi2(c1, c1);
            w[2] = hh ? 0u : hi2(c0, c2);
            w[3] = 0u;
            fixF[t] = asbf(w);
        }
    };
    constexpr int JG = QKV ? 6 : 5;

    wait_vm<DW + 4>();
    ST(23);
    barrier();
    ST(1);
#pragma unroll
    for (int g = 0; g < 2; ++g) {
        read_x(g);
#pragma unroll
        for (int t = 0; t < NTW; ++t) read_w(g, t);
    }
    static_for<0, 32>([&](auto GI) {
        constexpr int g = GI.value, j = g >> 2;
        SCHED0();
        if constexpr ((g & 3) == 2) {
            if constexpr (j == 0 || j == 1 || j == JG + 1) {
#pragma unroll
                for (int t = 0; t < NTW; ++t) PIN(acc[t]);
            }
            if constexpr (j == 0) {
                asm volatile("s_waitcnt vmcnt(%2) ; PINW %0 %1" : "+v"(epoch), "+v"(rowv) : "i"(2 * DW) : "memory");
                stat_task();
            }
            constexpr int n = j <= 1 ? DW + 1 : (j == 2 ? DW + 2 : (j <= 5 ? DW : (QKV ? (j == 6 ? 4 : 0) : (j == 6 ? D2 : 63))));
            if constexpr (j == JG + 1) gather_wait<n, NG>(gst, gcs);
            else wait_vm<n>();
            wait_lgkm0();
            barrier();
            ST(2 + j);
            if constexpr (j == 1) {
                {
                    const int idx = 8 * wave + (lane & 7);
                    LAS float* p = (LAS float*)(lds + L_CSP) + (idx >> 5) * 256 + (idx & 31);
                    float s = ((p[0] + p[32]) + (p[64] + p[96])) + ((p[128] + p[160]) + (p[192] + p[224]));
                    u32x2 gq = {f2u(s), tag};
                    const int u = wave >= 4 ? u1 : mt;
                    if (lane < 8) __builtin_amdgcn_raw_buffer_store_b64(gq, rs, WS_CSG + (csbase + (u >> 2) * 32 + (idx & 31)) * 32 + (u & 3) * 8, 0, SC1);
                }
                const int gw = G.bid * 8 + wave;
                const char* p = lane < 7 ? (const char*)a.w2 + (size_t)(gw * 7 + lane) * 128 : (const char*)a.wo + (size_t)(gw * 2 + (lane & 1)) * 128;
                touch_ptr<L_DUMMY>(p, ldsb);
            }
            if constexpr (j == JG) gather();
            if constexpr (QKV && j == 7) static_for<0, 4>([&](auto I) { dma_wq(IC(1), I); });
            if constexpr (j == JG + 1) gather_check_and_fix();
            SCHED0();
        }
        constexpr int jb = (g - 2) >> 2, q = (g - 2) & 3;
        static_for<0, NTW>([&](auto TI) {
            constexpr int t = TI.value;
            if (QKV && t == 1) acc[t] = MFMA32(xf[g % NXF], wf[g % 3][t], acc[t]);
            else acc[t] = MFMA32(wf[g % 3][t], xf[g % NXF], acc[t]);
            if constexpr (g + 2 < 32) {
                if (t == 0) read_x(g + 2);
                read_w(g + 2, t);
            }
            if constexpr (g < 2) {
                if constexpr (DW == 7) { dma_pair(IC(2), IC(g * 3 + t)); if constexpr (g == 1 && t == 2) dma_pair(IC(2), IC(DW - 1)); }
                else if constexpr (t < 2) dma_pair(IC(2), IC(2 * g + t));
            } else {
                constexpr int i = DW == 7 ? (t < 2 ? 2 * q + t : 99) : (t == 1 ? q : 99);
                if constexpr (jb <= 4) { if constexpr (i < DW) dma_pair(IC(jb + 3), IC(i)); }
                else if constexpr (QKV) { if constexpr (jb == 5 && i < 4) dma_wq(IC(0), IC(i)); }
                else if constexpr (jb <= 6) { if constexpr (i < D2) dma_w2(IC(5 * (jb - 5) + (WN == 0 ? 0 : 3) + (i >> 1)), IC(i & 1)); }
            }
            SCHED0();
        });
    });
    ST(10);
    const int tt = mt * 4 + wm;
    const int row2 = (G.mt2 & 1) * 64 + (wave & 1) * 32 + r32;
    if constexpr (!QKV) {
#pragma unroll
        for (int t = 0; t < NTW; ++t) acc[t] = MFMA32(fixF[t], fixT, acc[t]);
        vp2 = *(LAS float*)(lds + L_VP + row2 * 4);
        if (wave == 0 && lane < 8) *(LAS unsigned*)(lds + L2_CNT + lane * 4) = 0u;
#pragma unroll
        for (int t = 0; t < NTW; ++t) {
#pragma unroll
            for (int i = 0; i < 16; ++i) { float v = fmx(acc[t][i], 0.f); acc[t][i] = v * v; }
            const unsigned off = WS_H + (unsigned)((tt * 112 + nt * 14 + tile_of(t) * 2) * 64 + lane) * 16;
            __builtin_amdgcn_raw_buffer_store_b128(pack8u<0>(acc[t]), rs, off, 0, SC1);
            __builtin_amdgcn_raw_buffer_store_b128(pack8u<1>(acc[t]), rs, off + 1024, 0, SC1);
        }
        ST(12);
        wait_vm<0>();
        wait_lgkm0();
        barrier();
        if (wave == 0 && lane == 0)
            __hip_atomic_store((GAS unsigned*)(a.ws + WS_FLAG + (mt * 16 + 8 + nt) * 64), tag, RLX_AGENT);
    } else {
        acc[0] = MFMA32(fixF[1], fixT, acc[0]);
        acc[1] = MFMA32(fixT, fixF[2], acc[1]);
        bf16x8 idf[2], wfr[2];
#pragma unroll
        for (int s = 0; s < 2; ++s) {
            u32x4 u;
#pragma unroll
            for (int i = 0; i < 4; ++i) {
                int f0 = 16 * s + 8 * (i >> 1) + 4 * hh + 2 * (i & 1);
                u[i] = (r32 == f0 ? 0x3F80u : 0u) | (r32 == f0 + 1 ? 0x3F800000u : 0u);
            }
            idf[s] = asbf(u);
        }
#pragma unroll
        for (int X = 0; X < 2; ++X) {
            LAS char* p = lds + L_AUX + (2 * WN + X) * 512 + (4 * hh + q4) * 32 + p4 * 8;
            u32x4 u = __builtin_bit_cast(u32x4, cat44(tr_read(p), tr_read(p + 256)));
            const unsigned sg = blk ? 0x80008000u : 0u;
#pragma unroll
            for (int i = 0; i < 4; ++i) u[i] ^= sg;
            wfr[X] = asbf(u);
        }
        u32x4 v0, v1;
        {
#pragma unroll
            for (int g = 0; g < 4; ++g) {
                f32x4 rr = *(LAS f32x4*)(lds + L_RR + (wave * 32 + 8 * g + 4 * hh) * 4);
#pragma unroll
                for (int i = 0; i < 4; ++i) acc[1][4 * g + i] *= rr[i];
            }
            v0 = pack8u<0>(acc[1]); v1 = pack8u<1>(acc[1]);
        }
        const float kscale = r_t * LOG2E;
        f32x16 zero16;
#pragma unroll
        for (int i = 0; i < 16; ++i) zero16[i] = 0.f;
        auto feature_map = [&](f32x16& p, u32x4& F0, u32x4& F1) {
            float mp = fmx(fmx(fmx(fmx(p[0], p[1]), p[2]), fmx(fmx(p[3], p[4]), p[5])), fmx(p[6], p[7]));
            float mn = fmx(fmx(fmx(fmx(p[8], p[9]), p[10]), fmx(fmx(p[11], p[12]), p[13])), fmx(p[14], p[15]));
            {
                auto s = __builtin_amdgcn_permlane32_swap(f2u(mp), f2u(mn), false, false);
                float c = -kscale * fmx(u2f(s[0]), u2f(s[1]));
                auto s2 = __builtin_amdgcn_permlane32_swap(f2u(c), f2u(c), false, false);
                mp = u2f(s2[0]); mn = u2f(s2[1]);
            }
#pragma unroll
            for (int i = 0; i < 8; ++i) {
                p[i] = __builtin_amdgcn_exp2f(__builtin_fmaf(p[i], kscale, mp));
                p[8 + i] = __builtin_amdgcn_exp2f(__builtin_fmaf(p[8 + i], kscale, mn));
            }
            float sp = ((p[0] + p[1]) + (p[2] + p[3])) + ((p[4] + p[5]) + (p[6] + p[7]));
            float sn = ((p[8] + p[9]) + (p[10] + p[11])) + ((p[12] + p[13]) + (p[14] + p[15]));
            {
                auto s = __builtin_amdgcn_permlane32_swap(f2u(sp), f2u(sn), false, false);
                float c = __builtin_amdgcn_rcpf(u2f(s[0]) + u2f(s[1]));
                auto s2 = __builtin_amdgcn_permlane32_swap(f2u(c), f2u(c), false, false);
                sp = u2f(s2[0]); sn = u2f(s2[1]);
            }
#pragma unroll
            for (int i = 0; i < 8; ++i) { p[i] *= sp; p[8 + i] *= sn; }
            F0 = pack8u<0>(p); F1 = pack8u<1>(p);
        };
        {
            const bf16x8 kb0 = pack8<0>(acc[0]), kb1 = pack8<1>(acc[0]);
#pragma unroll
            for (int X = 0; X < 2; ++X) {
                f32x16 p = MFMA32(wfr[X], X ? kb1 : kb0, zero16);
                u32x4 F0, F1;
                feature_map(p, F0, F1);
                f32x16 d = MFMA32(asbf(F0), idf[0], zero16);
                d = MFMA32(asbf(F1), idf[1], d);
                u32x4 b0, b1;
#pragma unroll
                for (int i = 0; i < 4; ++i) { b0[i] = (blk == X) ? v0[i] : 0x3F803F80u; b1[i] = (blk == X) ? v1[i] : 0x3F803F80u; }
                f32x16 kv = MFMA32(pack8<0>(d), asbf(b0), zero16);
                kv = MFMA32(pack8<1>(d), asbf(b1), kv);
                const int row = X == 0 ? r32 : (r32 == 15 ? 16 : r32 - 16);
                if (X == 0 ? (r32 <= 16) : (r32 >= 15)) {
                    LAS char* dst = lds + L_RED + (((WN * 2 + X) * 4 + wm) * 17 + row) * 144 + hh * 16;
#pragma unroll
                    for (int g = 0; g < 4; ++g) {
                        f32x4 v = {kv[4 * g], kv[4 * g + 1], kv[4 * g + 2], kv[4 * g + 3]};
                        *(LAS f32x4*)(dst + g * 32) = v;
                    }
                }
            }
        }
        wait_vm<0>();
        wait_lgkm0();
        ST(26);
        barrier();
        vp2 = *(LAS float*)(lds + L_VP + row2 * 4);
        {
            const int tid = wave * 64 + lane;
            for (int o = tid; o < 4 * 136; o += 512) {
                int hl = o / 136, rem = o - hl * 136;
                LAS char* src = lds + L_RED + (hl * 4 * 17 + (rem >> 3)) * 144 + (rem & 7) * 16;
                f32x4 v = (*(LAS f32x4*)(src) + *(LAS f32x4*)(src + 2448)) + (*(LAS f32x4*)(src + 2 * 2448) + *(LAS f32x4*)(src + 3 * 2448));
                const unsigned off = WS_KVP + (unsigned)((mt * 32 + nt * 4 + hl) * 136 + rem) * 32;
                u32x4 g0 = {f2u(v[0]), tag, f2u(v[1]), tag}, g1 = {f2u(v[2]), tag, f2u(v[3]), tag};
                __builtin_amdgcn_raw_buffer_store_b128(g0, rs, off, 0, SC1);
                __builtin_amdgcn_raw_buffer_store_b128(g1, rs, off + 16, 0, SC1);
            }
        }
        ST(27);
        f32x16 qa = MFMA32(fixF[0], fixT, zero16);
        {
            LAS char* q0 = lds + L_WQ0 + hh * 1024 + WN * 512 + q4 * 64 + blk * 32 + p4 * 8;
            LAS char* q1 = q0 + (L_WQ1 - L_WQ0);
            PIN(q0); PIN(q1);
            bf16x8 qf[4];
            auto read_q = [&](int g) { LAS char* p = (g < 16 ? q0 : q1) + (g & 15) * 2048; qf[g & 3] = cat44(tr_read(p), tr_read(p + 256)); };
#pragma unroll
            for (int g = 0; g < 3; ++g) read_q(g);
            static_for<0, 32>([&](auto GI) {
                constexpr int g = GI.value;
                SCHED0();
                if constexpr (g == 4) { wait_lgkm0(); barrier(); }
                if constexpr (g == 16) { wait_lgkm0(); barrier(); }
                qa = MFMA32(qf[g & 3], xf[g], qa);
                if constexpr (g + 3 < 32) read_q(g + 3);
                if constexpr (g >= 4 && g < 20 && (g & 1) == 0) dma_w2(IC(WN * 4 + ((g - 4) >> 2)), IC(((g - 4) >> 1) & 1));
            });
            SCHED0();
        }
        ST(11);
        {
            const bf16x8 qb0 = pack8<0>(qa), qb1 = pack8<1>(qa);
#pragma unroll
            for (int X = 0; X < 2; ++X) {
                f32x16 p = MFMA32(wfr[X], X ? qb1 : qb0, zero16);
                u32x4 F0, F1;
                feature_map(p, F0, F1);
                LAS char* dst = lds + L_PHI + ((2 * WN + X) * 4 + wm) * 2048 + lane * 16;
                *(LAS u32x4*)(dst) = F0;
                *(LAS u32x4*)(dst + 1024) = F1;
            }
        }
        ST(12);
        wait_vm<0>();
        wait_lgkm0();
        barrier();
        if (wave == 0 && lane < 8) *(LAS unsigned*)(lds + L2_CNT + lane * 4) = 0u;
        if (WN == 1) {
#pragma unroll
            for (int i = 0; i < 8; ++i) phi[i >> 1][i & 1] = *(LAS u32x4*)(lds + L_PHI + wm * 8192 + i * 1024 + lane * 16);
        }
        wait_lgkm0();
        barrier();
    }
    ST(13);
    return tag;
}

#define HPV 6
constexpr int HP = HPV;
constexpr int NR = 8;
constexpr int WPRE = 13;
constexpr int w_at(int i) { return i <= 5 ? WPRE + i : (i >= NR && i + NB <= 27 ? i + NB : -1); }
constexpr int p2_pos(bool isw, int idx) {
    int pos = 0;
    for (int s = 0; s < HP; ++s) { pos += 2; if (!isw && idx == s) return pos; }
    for (int i = 0; i < 28; ++i) {
        if (i + HP <= 27) { pos += 2; if (!isw && idx == i + HP) return pos; }
        if (w_at(i) >= 0) { pos += 2; if (isw && idx == w_at(i)) return pos; }
    }
    return 0;
}
constexpr int p2_issued(int step) {
    int pos = 2 * HP;
    for (int i = 0; i < step; ++i) { if (i + HP <= 27) pos += 2; if (w_at(i) >= 0) pos += 2; }
    return pos;
}
constexpr int p2_wait(int i) {
    int need = p2_pos(false, i);
    if (i + 1 >= WPRE && i + 1 <= 27) { int w = p2_pos(true, i + 1); if (w > need) need = w; }
    return p2_issued(i) - need;
}
template <int SLEEP> DI void poll_words(srd_t rs, unsigned voff, unsigned tag) {
    for (unsigned spin = 0; spin <= SPIN_CAP; ++spin) {
        unsigned v;
        ld4c<0>(v, voff, rs);
        asm volatile("s_waitcnt vmcnt(0) ; PINW %0" : "+v"(v) :: "memory");
        if (__all(v == tag)) break;
        __builtin_amdgcn_s_sleep(SLEEP);
    }
}

DI void phase2(const Args& a, LAS char* lds, const Geo& G, unsigned tag, u32x4 (&phi)[4][2], float vp2, unsigned long long* stamps) {
    const int lane = G.lane, wave = G.wave, mt = G.mt2, nt = G.nt2;
    const unsigned ldsb = (unsigned)(size_t)lds;
    const int hh = lane >> 5, r32 = lane & 31, blk = (lane >> 4) & 1, q4 = (lane & 15) >> 2, p4 = lane & 3;
    const int kq = wave & 3, hf = wave >> 2;
    const srd_t rs = mk_srd(a.ws, WS_END);
    const unsigned wvo = (unsigned)((((lane >> 2) & 7) * DM + nt * 64 + hh * 32 + (lane & 3) * 8) * 2);
    const unsigned trl = (4 * hh + q4) * 64 + blk * 32 + p4 * 8;
    const unsigned l16 = lane * 16;
    const int token = mt * 64 + (kq & 1) * 32 + r32;
    const int col0 = nt * 64 + (kq >> 1) * 32 + 16 * hf + 4 * hh;
    u32x2 xr0, xr1, br0, br1;
    auto final_loads = [&]() {
        const srd_t rx = mk_srd(a.x, SEQ * DM * 2), rb = mk_srd(a.b2, DM * 2);
        ld8<0>(xr0, (unsigned)(token * DM + col0) * 2, rx); ld8<16>(xr1, (unsigned)(token * DM + col0) * 2, rx);
        ld8<0>(br0, (unsigned)col0 * 2, rb); ld8<16>(br1, (unsigned)col0 * 2, rb);
    };
    f32x16 acc[2][2];
#pragma unroll
    for (int i = 0; i < 2; ++i)
#pragma unroll
        for (int j = 0; j < 2; ++j)
#pragma unroll
            for (int e = 0; e < 16; ++e) acc[i][j][e] = 0.f;
    if (wave < 4) {
        const srd_t rw2 = mk_srd(a.w2, FF * DM * 2);
        const unsigned w2vo = wvo + kq * (448 * DM * 2);
        const unsigned lb = ldsb + kq * 2048;
        const unsigned hvo = (unsigned)((2 * mt) * 112 + 28 * kq) * 1024 + l16;
        auto issue_w = [&](auto SI) {
            constexpr int s = SI.value;
            dma16<(s % NB) * 8192, s * 16 * DM * 2>(w2vo, rw2, lb);
            dma16<(s % NB) * 8192 + 1024, (s * 16 + 8) * DM * 2>(w2vo, rw2, lb);
        };
        u32x4 hb[HP][2];
        auto issue_h = [&](auto SI) {
            constexpr int s = SI.value;
            ld16c<WS_H + s * 1024>(hb[s % HP][0], hvo, rs);
            ld16c<WS_H + (112 + s) * 1024>(hb[s % HP][1], hvo, rs);
        };
        if (G.qkv) { issue_w(IC(8)); issue_w(IC(9)); }
        issue_w(IC(10));
        LAS char* rdA = lds + kq * 2048 + trl;
        LAS char* rdB = rdA + 6 * 8192;
        PIN(rdB);
        bf16x8 wr[NR][2], wf[2][2];
        auto rd = [&](int s, bf16x8 (&d)[2]) {
            LAS char* p = (s % NB) < 6 ? rdA + (s % NB) * 8192 : rdB + (s % NB - 6) * 8192;
            d[0] = cat44(tr_read(p), tr_read(p + 1024));
            d[1] = cat44(tr_read(p + 512), tr_read(p + 1536));
        };
#pragma unroll
        for (int s = 0; s < NR; ++s) rd(s, wr[s]);
        wait_lgkm0();
        static_for<NB, WPRE>([&](auto SI) { issue_w(SI); });
        final_loads();
#pragma unroll
        for (int s = 0; s < NR; ++s) { PIN(wr[s][0]); PIN(wr[s][1]); }
        ST(24);
        for (unsigned spin = 0; spin <= 16 * SPIN_CAP; ++spin) {
            if (__hip_atomic_load((LAS unsigned*)(lds + L2_CNT + 16), __ATOMIC_RELAXED, __HIP_MEMORY_SCOPE_WORKGROUP) != 0u) break;
            __builtin_amdgcn_s_sleep(1);
        }
        CFENCE();
        ST(14);
        unsigned tch0, tch1;
        {
            const unsigned tvo = (unsigned)((2 * mt) * 112 + 28 * kq) * 1024 + (unsigned)((lane < 56 ? lane : 55) * 4 + (nt & 3)) * 128;
            ld4c<WS_H>(tch0, tvo, rs); ld4c<WS_H + 112 * 1024>(tch1, tvo, rs);
        }
        static_for<0, HP>([&](auto SI) { issue_h(SI); });
        static_for<0, 28>([&](auto SI) {
            constexpr int s = SI.value;
            SCHED0();
            asm volatile("s_waitcnt vmcnt(%2) ; PINW %0 %1" : "+v"(hb[s % HP][0]), "+v"(hb[s % HP][1]) : "i"(p2_wait(s)) : "memory");
            if constexpr (s == 0) { asm volatile("; PINW %0 %1" : "+v"(tch0), "+v"(tch1)); ST(15); }
            if constexpr (s + 1 >= NR && s + 1 < 28) rd(s + 1, wf[(s + 1) & 1]);
#pragma unroll
            for (int i = 0; i < 2; ++i)
#pragma unroll
                for (int j = 0; j < 2; ++j) acc[i][j] = MFMA32(s < NR ? wr[s < NR ? s : 0][i] : wf[s & 1][i], asbf(hb[s % HP][j]), acc[i][j]);
            SCHED0();
            if constexpr (s + HP < 28) issue_h(IC(s + HP));
            if constexpr (w_at(s) >= 0) issue_w(IC(w_at(s) >= 0 ? w_at(s) : 0));
        });
        SCHED0();
        ST(16);
        wait_lgkm0();
#pragma unroll
        for (int qd = 0; qd < 4; ++qd)
#pragma unroll
            for (int g = 0; g < 4; ++g) {
                const f32x16& c = acc[qd >> 1][qd & 1];
                f32x4 v = {c[4 * g], c[4 * g + 1], c[4 * g + 2], c[4 * g + 3]};
                *(LAS f32x4*)(lds + (qd * 2 + (g >> 1)) * 8192 + kq * 2048 + (g & 1) * 1024 + l16) = v;
            }
    } else {
        LAS char* myreg = lds + L2_A + kq * 16384;
        const srd_t rwo = mk_srd(a.wo, DM * DM * 2);
        __builtin_amdgcn_s_setprio(3);
        auto load_wo = [&]() { static_for<0, 16>([&](auto P) { dma16<L2_A + P.value * 1024, P.value * 8 * DM * 2>(wvo + kq * (128 * DM * 2), rwo, ldsb + kq * 16384); }); };
        if (kq == 0) {
            poll_words<1>(rs, WS_FLAG + (unsigned)((mt >> 1) * 16 + 8 + (lane & 7)) * 64, tag);
            if (lane == 0) __hip_atomic_store((LAS unsigned*)(lds + L2_CNT + 16), 1u, __ATOMIC_RELAXED, __HIP_MEMORY_SCOPE_WORKGROUP);
        }
        if (!G.qkv) {
            if (kq >= 2) {
                const int sl = (G.mt * 8 + G.nt) * 2 + (kq & 1);
                const int head = sl >> 3, g = (sl & 7) * 17 + (lane < 34 ? (lane >> 1) : 0);
                const unsigned vo = (unsigned)((head * 136 + g) * 32 + (lane & 1) * 16);
                u32x4 v[16];
                for (unsigned spin = 0; spin <= SPIN_CAP; ++spin) {
                    static_for<0, 16>([&](auto M) { ld16c<WS_KVP + M.value * 32 * 136 * 32>(v[M.value], vo, rs); });
                    wait16(v);
                    unsigned bad = 0;
#pragma unroll
                    for (int m = 0; m < 16; ++m) bad |= (v[m][1] ^ tag) | (v[m][3] ^ tag);
                    if (__all(bad == 0u)) break;
                    __builtin_amdgcn_s_sleep(4);
                }
                float s0[16], s1[16];
#pragma unroll
                for (int m = 0; m < 16; ++m) { s0[m] = u2f(v[m][0]); s1[m] = u2f(v[m][2]); }
#pragma unroll
                for (int st = 8; st >= 1; st >>= 1)
#pragma unroll
                    for (int m = 0; m < st; ++m) { s0[m] += s0[m + st]; s1[m] += s1[m + st]; }
                const int row = g >> 3, fq = g & 7;
                const unsigned off = WS_KVR + head * 2176 + fq * 272 + row * 16 + (lane & 1) * 8;
                if (lane < 34)
                    __hip_atomic_store((GAS unsigned long long*)(a.ws + off), (unsigned long long)pk2(s0[0], s1[0]) | ((unsigned long long)tag << 32), RLX_AGENT);
            }
            ST(24);
            load_wo();
        } else {
            const unsigned kvo = (unsigned)((4 * G.nt + kq) * 2176 + hh * 272 + (r32 < 16 ? r32 : 16) * 16);
            u32x4 raw[4];
            for (unsigned spin = 0; spin <= SPIN_CAP; ++spin) {
                static_for<0, 4>([&](auto I) { ld16c<WS_KVR + I.value * 544>(raw[I.value], kvo, rs); });
                asm volatile("s_waitcnt vmcnt(0) ; PINW %0 %1 %2 %3" : "+v"(raw[0]), "+v"(raw[1]), "+v"(raw[2]), "+v"(raw[3]) :: "memory");
                unsigned bad = 0;
#pragma unroll
                for (int i = 0; i < 4; ++i) bad |= (raw[i][1] ^ tag) | (raw[i][3] ^ tag);
                if (__all(bad == 0u)) break;
                __builtin_amdgcn_s_sleep(4);
            }
            ST(24);
            f32x16 zero16;
#pragma unroll
            for (int i = 0; i < 16; ++i) zero16[i] = 0.f;
            const u32x4 k0 = {raw[0][0], raw[0][2], raw[1][0], raw[1][2]}, k1 = {raw[2][0], raw[2][2], raw[3][0], raw[3][2]};
#pragma unroll
            for (int w = 0; w < 4; ++w) {
                f32x16 nd = MFMA32(asbf(k0), asbf(phi[w][0]), zero16);
                nd = MFMA32(asbf(k1), asbf(phi[w][1]), nd);
                const float inv = __builtin_amdgcn_rcpf(nd[8] + 1e-6f);
#pragma unroll
                for (int e = 0; e < 8; ++e) nd[e] *= inv;
                __builtin_amdgcn_raw_buffer_store_b128(pack8u<0>(nd), rs, WS_O + (unsigned)(((G.mt * 4 + w) * 32 + G.nt * 4 + kq) * 1024) + l16, 0, SC1);
            }
            wait_vm<0>();
            unsigned old = 0;
            if (lane == 0) old = __hip_atomic_fetch_add((LAS unsigned*)(lds + L2_CNT), 1u, __ATOMIC_RELAXED, __HIP_MEMORY_SCOPE_WORKGROUP);
            if (lane == 0 && old == 3u) __hip_atomic_store((GAS unsigned*)(a.ws + WS_FLAG + (G.mt * 16 + G.nt) * 64), tag, RLX_AGENT);
            ST(25);
            load_wo();
        }
        final_loads();
        poll_words<4>(rs, WS_FLAG + (unsigned)((mt >> 1) * 16 + 2 * kq + (lane & 1)) * 64, tag);
        ST(14);
        const unsigned ovo = (unsigned)((2 * mt) * 32 + 8 * kq) * 1024 + l16;
        u32x4 ob[16];
        static_for<0, 16>([&](auto I) { constexpr int i = I.value; ld16c<WS_O + (i & 1) * 32768 + (i >> 1) * 1024>(ob[i], ovo, rs); });
        bf16x8 wv[8][2];
#pragma unroll
        for (int h = 0; h < 8; ++h) {
            LAS char* p = myreg + h * 2048 + trl;
            wv[h][0] = cat44(tr_read(p), tr_read(p + 1024));
            wv[h][1] = cat44(tr_read(p + 512), tr_read(p + 1536));
        }
        wait16(ob);
        ST(15);
#pragma unroll
        for (int h = 0; h < 8; ++h)
#pragma unroll
            for (int i = 0; i < 2; ++i)
#pragma unroll
                for (int j = 0; j < 2; ++j) acc[i][j] = MFMA32(wv[h][i], asbf(ob[2 * h + j]), acc[i][j]);
        ST(16);
        wait_lgkm0();
#pragma unroll
        for (int qd = 0; qd < 4; ++qd)
#pragma unroll
            for (int g = 0; g < 4; ++g) {
                const f32x16& c = acc[qd >> 1][qd & 1];
                f32x4 v = {c[4 * g], c[4 * g + 1], c[4 * g + 2], c[4 * g + 3]};
                *(LAS f32x4*)(myreg + (qd * 4 + g) * 1024 + l16) = v;
            }
    }
    asm volatile("s_waitcnt vmcnt(0) ; PINW %0 %1 %2 %3" : "+v"(xr0), "+v"(xr1), "+v"(br0), "+v"(br1) :: "memory");
    wait_lgkm0();
    barrier();
    ST(17);
    const float r2 = __builtin_amdgcn_rcpf(vp2);
#pragma unroll
    for (int g = 0; g < 2; ++g) {
        LAS char* pf = lds + (kq * 2 + hf) * 8192 + g * 1024 + l16;
        LAS char* pa = lds + L2_A + (kq * 4 + 2 * hf + g) * 1024 + l16;
        f32x4 so = (*(LAS f32x4*)(pf) + *(LAS f32x4*)(pf + 2048)) + (*(LAS f32x4*)(pf + 4096) + *(LAS f32x4*)(pf + 6144));
        f32x4 ss = (*(LAS f32x4*)(pa) + *(LAS f32x4*)(pa + 16384)) + (*(LAS f32x4*)(pa + 2 * 16384) + *(LAS f32x4*)(pa + 3 * 16384));
        const u32x2 xr = g ? xr1 : xr0, br = g ? br1 : br0;
        float res[4];
#pragma unroll
        for (int i = 0; i < 4; ++i) {
            const float xv = u2f((i & 1) ? (xr[i >> 1] & 0xFFFF0000u) : (xr[i >> 1] << 16));
            const float bv = u2f((i & 1) ? (br[i >> 1] & 0xFFFF0000u) : (br[i >> 1] << 16));
            const float o = so[i] * r2 + bv;
            const float gt = o * __builtin_amdgcn_rcpf(1.f + __builtin_amdgcn_exp2f(-o * LOG2E));
            res[i] = xv + ss[i] * gt;
        }
        u32x2 ov = {pk2(res[0], res[1]), pk2(res[2], res[3])};
        *(GAS u32x2*)(a.out + (size_t)token * DM + col0 + 8 * g) = ov;
    }
    ST(18);
    if (G.bid == 0 && wave == 7) {
        for (unsigned spin = 0; spin <= SPIN_CAP; ++spin) {
            unsigned f0, f1, f2, f3;
            ld4c<WS_FLAG>(f0, (unsigned)lane * 64, rs); ld4c<WS_FLAG + 4096>(f1, (unsigned)lane * 64, rs);
            ld4c<WS_FLAG + 8192>(f2, (unsigned)lane * 64, rs); ld4c<WS_FLAG + 12288>(f3, (unsigned)lane * 64, rs);
            asm volatile("s_waitcnt vmcnt(0) ; PINW %0 %1 %2 %3" : "+v"(f0), "+v"(f1), "+v"(f2), "+v"(f3) :: "memory");
            if (__all(f0 == tag && f1 == tag && f2 == tag && f3 == tag)) break;
            __builtin_amdgcn_s_sleep(1);
        }
        if (lane == 0) __hip_atomic_store((GAS unsigned*)(a.ws + WS_CTRL), tag, RLX_AGENT);
    }
}

__global__ void __launch_bounds__(512, 2) fused_kernel(Args a) {
    extern __shared__ __attribute__((aligned(16))) char smem[];
    LAS char* lds = (LAS char*)smem;
    Geo G;
    G.bid = blockIdx.x;
    G.wave = __builtin_amdgcn_readfirstlane(threadIdx.x >> 6);
    G.lane = threadIdx.x & 63;
    const int xcd = G.bid & 7, loc = G.bid >> 3;
    const int sel = loc >> 2;
    G.mt = (xcd >> 1) * 4 + (loc & 3);
    G.nt = (xcd & 1) * 4 + (sel & 3);
    G.i16 = (xcd & 1) * 8 + sel;
    G.mt2 = G.mt * 2 + (xcd & 1);
    G.nt2 = sel;
    G.qkv = sel < 4;
    unsigned long long stamps[28];
    unsigned tag;
    float vp2 = 1.f;
    u32x4 phi[4][2];
#pragma unroll
    for (int i = 0; i < 8; ++i) phi[i >> 1][i & 1] = u32x4{0u, 0u, 0u, 0u};
    if (sel < 4) {
        if (G.wave < 4) tag = phase1<true, 0>(a, lds, G, phi, vp2, stamps);
        else tag = phase1<true, 1>(a, lds, G, phi, vp2, stamps);
    } else {
        if (G.wave < 4) tag = phase1<false, 0>(a, lds, G, phi, vp2, stamps);
        else tag = phase1<false, 1>(a, lds, G, phi, vp2, stamps);
    }
    phase2(a, lds, G, tag, phi, vp2, stamps);
}

extern "C" void kernel_launch(void* const* d_in, const int* in_sizes, int n_in, void* d_out, int out_size, void* d_ws,
                              size_t ws_size, hipStream_t stream) {
    Args a;
    a.x = (const __bf16*)d_in[0]; a.wqkv = (const __bf16*)d_in[1]; a.wphi = (const __bf16*)d_in[2]; a.wo = (const __bf16*)d_in[3];
    a.w1 = (const __bf16*)d_in[5]; a.b1 = (const __bf16*)d_in[6]; a.w2 = (const __bf16*)d_in[7]; a.b2 = (const __bf16*)d_in[8];
    a.ws = (char*)d_ws + ((ws_size - WS_END) & ~(size_t)0xFFFFF); a.out = (__bf16*)d_out;
    (void)hipFuncSetAttribute((const void*)fused_kernel, hipFuncAttributeMaxDynamicSharedMemorySize, L_TOTAL);
    fused_kernel<<<256, 512, L_TOTAL, stream>>>(a);
}
```

```cpp
#include <hip/hip_runtime.h>
#include <hip/hip_bf16.h>
#include <stdint.h>
#include <type_traits>

typedef __attribute__((ext_vector_type(8))) __bf16 bf16x8;
typedef __attribute__((ext_vector_type(4))) __bf16 bf16x4;
typedef __attribute__((ext_vector_type(2))) __bf16 bf16x2;
typedef __attribute__((ext_vector_type(16))) float f32x16;
typedef __attribute__((ext_vector_type(4))) float f32x4;
typedef __attribute__((ext_vector_type(2))) float f32x2;
typedef __attribute__((ext_vector_type(4))) unsigned u32x4;
typedef __attribute__((ext_vector_type(2))) unsigned u32x2;
typedef __amdgpu_buffer_rsrc_t srd_t;
#define LAS __attribute__((address_space(3)))
#define GAS __attribute__((address_space(1)))
#define DI __device__ __forceinline__
#define MFMA32(a, b, c) __builtin_amdgcn_mfma_f32_32x32x16_bf16((a), (b), (c), 0, 0, 0)
#define SCHED0() __builtin_amdgcn_sched_barrier(0)
#define CFENCE() asm volatile("" ::: "memory")
#define RLX_AGENT __ATOMIC_RELAXED, __HIP_MEMORY_SCOPE_AGENT

constexpr int SEQ = 2048, DM = 512, NH = 32, FF = 1792;
constexpr float LOG2E = 1.4426950408889634f;
constexpr int SC1 = 16;
constexpr unsigned SPIN_CAP = 1u << 16;

constexpr unsigned WS_CTRL = 0;
constexpr unsigned WS_FLAG = 256;
constexpr unsigned WS_RST  = WS_FLAG + 256 * 64;
constexpr unsigned WS_CSG  = WS_RST + 2048 * 16;
constexpr unsigned WS_KVR  = WS_CSG + 3328 * 32;
constexpr unsigned WS_KVP  = WS_KVR + 32 * 2176;
constexpr unsigned WS_H    = (WS_KVP + 16u * 32 * 136 * 32 + 4095) & ~4095u;
constexpr unsigned WS_O    = WS_H + 64u * 112 * 1024;
constexpr unsigned WS_END  = WS_O + 64u * 32 * 1024;

#define ST(i)

constexpr int SLOT = 32768, XOFF = 16384;
constexpr int chunk_base(int c) { return c == 0 ? 98304 : (c == 1 ? 122880 : (c == 2 ? 40960 : 65536)); }
constexpr int L_UNIT = 131072;
constexpr int L_CSP = 147456;
constexpr int L_RR = L_CSP + 1024;
constexpr int L_AUX = L_RR + 1024;
constexpr int L_DUMMY = L_AUX + 2048;
constexpr int L_RED = 0;
constexpr int L_PHI = 98304;
constexpr int NR = 8;
constexpr int NB = 11;
constexpr int L2_CNT = NB * 8192;
constexpr int L2_A = 98304;
constexpr int L_TOTAL = 163840;
static_assert(L_DUMMY + 256 <= L_TOTAL, "lds");

template <int LOFF, int SOFF> DI void dma16(unsigned voff, srd_t srd, unsigned lbase) {
    unsigned t;
    asm volatile("s_add_i32 m0, %3, %4\n\ts_mov_b32 %0, %5\n\tbuffer_load_dwordx4 %1, %2, %0 offen lds"
                 : "=&s"(t) : "v"(voff), "s"(srd), "s"(lbase), "i"(LOFF), "i"(SOFF) : "memory", "scc");
}
template <int LOFF> DI void touch_ptr(const void* p, unsigned lbase) {
    asm volatile("s_add_i32 m0, %1, %2\n\ts_nop 0\n\tglobal_load_lds_dword %0, off" :: "v"(p), "s"(lbase), "i"(LOFF) : "memory", "scc");
}
template <int LOFF> DI void touch_c(unsigned voff, srd_t srd, unsigned lbase) {
    asm volatile("s_add_i32 m0, %2, %3\n\ts_nop 0\n\tbuffer_load_dword %0, %1, 0 offen sc1 lds" :: "v"(voff), "s"(srd), "s"(lbase), "i"(LOFF) : "memory", "scc");
}
template <int LOFF> DI void touch(unsigned voff, srd_t srd, unsigned lbase) {
    asm volatile("s_add_i32 m0, %2, %3\n\ts_nop 0\n\tbuffer_load_dword %0, %1, 0 offen lds" :: "v"(voff), "s"(srd), "s"(lbase), "i"(LOFF) : "memory", "scc");
}
template <int OFF> DI void ld16c(u32x4& d, unsigned voff, srd_t srd) {
    asm volatile("buffer_load_dwordx4 %0, %1, %2, %3 offen offset:%4 sc1" : "=v"(d) : "v"(voff), "s"(srd), "s"(OFF & ~4095), "i"(OFF & 4095) : "memory");
}
template <int OFF> DI void ld4c(unsigned& d, unsigned voff, srd_t srd) {
    asm volatile("buffer_load_dword %0, %1, %2, %3 offen offset:%4 sc1" : "=v"(d) : "v"(voff), "s"(srd), "s"(OFF & ~4095), "i"(OFF & 4095) : "memory");
}
template <int OFF> DI void ld8c(u32x2& d, unsigned voff, srd_t srd) {
    asm volatile("buffer_load_dwordx2 %0, %1, %2, %3 offen offset:%4 sc1" : "=v"(d) : "v"(voff), "s"(srd), "s"(OFF & ~4095), "i"(OFF & 4095) : "memory");
}
DI void dma16r(unsigned voff, srd_t srd, unsigned lds_dst) {
    asm volatile("s_mov_b32 m0, %2\n\ts_nop 0\n\tbuffer_load_dwordx4 %0, %1, 0 offen lds" :: "v"(voff), "s"(srd), "s"(lds_dst) : "memory");
}
DI void ld16(u32x4& d, unsigned voff, srd_t srd) { asm volatile("buffer_load_dwordx4 %0, %1, %2, 0 offen" : "=v"(d) : "v"(voff), "s"(srd) : "memory"); }
template <int OFF> DI void ld8(u32x2& d, unsigned voff, srd_t srd) { asm volatile("buffer_load_dwordx2 %0, %1, %2, 0 offen offset:%3" : "=v"(d) : "v"(voff), "s"(srd), "i"(OFF) : "memory"); }
DI float fmx(float a, float b) { return __builtin_elementwise_maximum(a, b); }
DI void wait_lgkm0() { __builtin_amdgcn_s_waitcnt(0xC07F); CFENCE(); }
template <int N> DI void wait_vm() { asm volatile("s_waitcnt vmcnt(%0)" ::"i"(N) : "memory"); }
DI void barrier() { __builtin_amdgcn_s_barrier(); CFENCE(); }
#define PIN(x) asm volatile("" : "+v"(x))
DI srd_t mk_srd(const void* p, unsigned bytes) { return __builtin_amdgcn_make_buffer_rsrc((void*)p, 0, (int)bytes, 0x00020000); }

DI bf16x4 tr_read(LAS char* p) { return __builtin_amdgcn_ds_read_tr16_b64_v4bf16((LAS bf16x4*)p); }
DI bf16x8 cat44(bf16x4 a, bf16x4 b) { return __builtin_shufflevector(a, b, 0, 1, 2, 3, 4, 5, 6, 7); }
DI float dot2(unsigned a, unsigned b, float c) {
    return __builtin_amdgcn_fdot2_f32_bf16(__builtin_bit_cast(bf16x2, a), __builtin_bit_cast(bf16x2, b), c, false);
}
DI unsigned pk2(float a, float b) { f32x2 v = {a, b}; return __builtin_bit_cast(unsigned, __builtin_convertvector(v, bf16x2)); }
template <int S> DI u32x4 pack8u(const f32x16& x) {
    u32x4 p = {pk2(x[8 * S], x[8 * S + 1]), pk2(x[8 * S + 2], x[8 * S + 3]), pk2(x[8 * S + 4], x[8 * S + 5]), pk2(x[8 * S + 6], x[8 * S + 7])};
    return p;
}
template <int S> DI bf16x8 pack8(const f32x16& x) { return __builtin_bit_cast(bf16x8, pack8u<S>(x)); }
DI bf16x8 asbf(u32x4 u) { return __builtin_bit_cast(bf16x8, u); }
DI float u2f(unsigned u) { return __builtin_bit_cast(float, u); }
DI unsigned f2u(float f) { return __builtin_bit_cast(unsigned, f); }
DI unsigned hi2(unsigned lo, unsigned hi) { return __builtin_amdgcn_perm(hi, lo, 0x07060302u); }
DI void split3(float v, unsigned& s0, unsigned& s1, unsigned& s2) {
    s0 = f2u(v) & 0xFFFF0000u;
    float r = v - u2f(s0);
    s1 = f2u(r) & 0xFFFF0000u;
    s2 = f2u(r - u2f(s1));
}
DI float half_sum(float v) { auto s = __builtin_amdgcn_permlane32_swap(f2u(v), f2u(v), false, false); return u2f(s[0]) + u2f(s[1]); }
DI float wave_sum(float v) {
#define DPPADD(ctrl, rm) v += u2f((unsigned)__builtin_amdgcn_update_dpp(0, (int)f2u(v), ctrl, rm, 0xf, true))
    DPPADD(0x111, 0xf); DPPADD(0x112, 0xf); DPPADD(0x114, 0xf); DPPADD(0x118, 0xf); DPPADD(0x142, 0xa); DPPADD(0x143, 0xc);
#undef DPPADD
    return u2f((unsigned)__builtin_amdgcn_readlane((int)f2u(v), 63));
}
template <int C, int N, class F> DI void static_for(F&& f) { if constexpr (C < N) { f(std::integral_constant<int, C>{}); static_for<C + 1, N>(f); } }

#define NO_L2W
#define TOUCH1 2
struct Args {
    const __bf16* x; const __bf16* wqkv; const __bf16* wphi; const __bf16* wo;
    const __bf16* w1; const __bf16* b1; const __bf16* w2; const __bf16* b2;
    char* ws; __bf16* out;
};
struct Geo { int bid, wave, lane, mt, nt, i16, mt2, nt2, qkv, rq; };
#define IC(v) std::integral_constant<int, (v)>{}

template <int N, int NG> DI void gather_wait(u32x4& gst, u32x2& gv2, u32x4 (&gcs)[NG]) {
    if constexpr (NG == 3) asm volatile("s_waitcnt vmcnt(%5) ; PINW %0 %1 %2 %3 %4" : "+v"(gst), "+v"(gv2), "+v"(gcs[0]), "+v"(gcs[1]), "+v"(gcs[2]) : "i"(N) : "memory");
    else asm volatile("s_waitcnt vmcnt(%6) ; PINW %0 %1 %2 %3 %4 %5" : "+v"(gst), "+v"(gv2), "+v"(gcs[0]), "+v"(gcs[1]), "+v"(gcs[2]), "+v"(gcs[NG - 1]) : "i"(N) : "memory");
}
DI void wait16(u32x4 (&v)[16]) {
    asm volatile("s_waitcnt vmcnt(0) ; PINW %0 %1 %2 %3 %4 %5 %6 %7 %8 %9 %10 %11 %12 %13 %14 %15"
                 : "+v"(v[0]), "+v"(v[1]), "+v"(v[2]), "+v"(v[3]), "+v"(v[4]), "+v"(v[5]), "+v"(v[6]), "+v"(v[7]),
                   "+v"(v[8]), "+v"(v[9]), "+v"(v[10]), "+v"(v[11]), "+v"(v[12]), "+v"(v[13]), "+v"(v[14]), "+v"(v[15]) :: "memory");
}

DI bool kv_reduce_publish(char* ws, u32x4 (&v)[16], int sl, int lane, unsigned tag) {
    unsigned bad = 0;
#pragma unroll
    for (int m = 0; m < 16; ++m) bad |= (v[m][1] ^ tag) | (v[m][3] ^ tag);
    if (!__all(bad == 0u)) return false;
    float s0[16], s1[16];
#pragma unroll
    for (int m = 0; m < 16; ++m) { s0[m] = u2f(v[m][0]); s1[m] = u2f(v[m][2]); }
#pragma unroll
    for (int st = 8; st >= 1; st >>= 1)
#pragma unroll
        for (int m = 0; m < st; ++m) { s0[m] += s0[m + st]; s1[m] += s1[m + st]; }
    const int g = (sl & 7) * 17 + (lane < 34 ? (lane >> 1) : 0), row = g >> 3, fq = g & 7;
    const unsigned off = WS_KVR + (sl >> 3) * 2176 + fq * 272 + row * 16 + (lane & 1) * 8;
    if (lane < 34)
        __hip_atomic_store((GAS unsigned long long*)(ws + off), (unsigned long long)pk2(s0[0], s1[0]) | ((unsigned long long)tag << 32), RLX_AGENT);
    return true;
}
template <bool QKV, int WN>
DI unsigned phase1(const Args& a, LAS char* lds, const Geo& G, const unsigned (&wvo0)[4], const unsigned (&xvo0)[4],
                   u32x4 (&ph)[16], float& vp2, unsigned long long* stamps) {
    constexpr int NTB = QKV ? 2 : 3;
    constexpr int NB2 = QKV ? 1 : (WN == 0 ? 1 : 2);
    constexpr int NG = 2 + NB2;
    constexpr int NU = QKV ? 24 : 28;
    constexpr int LDW = QKV ? 1536 : 1792;
    constexpr int CP = NTB * 2;
    const int lane = G.lane, wave = G.wave, wm = wave & 3, mt = G.mt, nt = G.nt;
    const unsigned ldsb = (unsigned)(size_t)lds;
    const int hh = lane >> 5, r32 = lane & 31, blk = (lane >> 4) & 1, q4 = (lane & 15) >> 2, p4 = lane & 3;
    const srd_t rs = mk_srd(a.ws, WS_END);
    const srd_t rx = mk_srd(a.x, SEQ * DM * 2);
    const srd_t rw = mk_srd(QKV ? a.wqkv : a.w1, DM * LDW * 2);
    const srd_t rw2 = mk_srd(a.w2, FF * DM * 2);
    const srd_t rwo = mk_srd(a.wo, DM * DM * 2);
    auto colb_of = [&](int c) { return QKV ? ((((c >> 1) + 1) % 3) * 512 + 64 * nt + 32 * (c & 1)) : (224 * nt + 32 * c); };
    auto tile_of = [&](int t) { return t < 2 ? 2 * t + WN : (QKV ? 4 + WN : 4 + WN + (t - 2)); };
    auto dma_pair = [&](auto JI, auto II) {
        constexpr int j = JI.value, i = II.value, sb = ((j + 1) % 3) * SLOT;
        if constexpr (i < 4) dma16<sb + i * 1024, j * 64 * LDW * 2>(wvo0[i], rw, ldsb + wm * 4096);
        else dma16<sb + XOFF + (i - 4) * 1024, j * 128>(xvo0[i - 4], rx, ldsb + wm * 4096);
    };
    const unsigned w2vo = (unsigned)((((lane >> 2) & 7) * DM + G.nt2 * 64 + hh * 32 + (lane & 3) * 8) * 2);
    unsigned w2q[3];
#pragma unroll
    for (int q = 0; q < 3; ++q) w2q[q] = w2vo + (unsigned)((wm * 448 + ((q + G.rq) & 3) * 112) * DM * 2);
    auto dma_w2 = [&](auto PI) {
        constexpr int r = PI.value >> 1, t = PI.value & 1;
        dma16<(r % NB) * 8192 + t * 1024, ((r % 7) * 16 + t * 8) * DM * 2>(w2q[r / 7], rw2, ldsb + wm * 2048);
    };
    const unsigned wovo = w2vo + wm * (128 * DM * 2);
    auto dma_wo = [&](auto PI) { dma16<L2_A + PI.value * 1024, PI.value * 8 * DM * 2>(wovo, rwo, ldsb + wm * 16384); };
    unsigned cvo[WN == 1 ? NTB : 1];
    if constexpr (WN == 1) {
#pragma unroll
        for (int i = 0; i < NTB; ++i) {
            const int st = 2 * i + hh, k8 = 4 * wm + (st >= NTB ? 1 : 0), c = st >= NTB ? st - NTB : st;
            cvo[i] = (unsigned)(((k8 * 8 + ((lane >> 2) & 7)) * LDW + colb_of(4 + c) + (lane & 3) * 8) * 2);
        }
    }
    auto dma_chunk = [&](auto CI, auto II) {
        constexpr int c = CI.value, i = II.value;
        dma16<chunk_base(c) + i * 1024, (c * 128 + (i / NTB) * 16) * LDW * 2>(cvo[i % NTB], rw, ldsb + wm * (CP * 1024));
    };
    unsigned epoch; u32x4 rowv[2];
    const int srow = mt * 128 + G.i16 * 8 + 2 * wm;
    const int u1 = mt + 16 < NU ? mt + 16 : mt;
    ld4c<WS_CTRL>(epoch, 0u, rs);
    if constexpr (WN == 0) static_for<0, 8>([&](auto I) { dma_pair(IC(1), I); });
    auto stat_loads = [&]() {
        ld16(rowv[0], (unsigned)(srow * DM * 2 + lane * 16), rx);
        ld16(rowv[1], (unsigned)(srow * DM * 2 + 1024 + lane * 16), rx);
        const unsigned rowpart = (unsigned)(((mt & 3) * 128 + 32 * wm + (lane >> 2)) * LDW + (lane & 3) * 8) * 2;
        dma16<L_UNIT, 0>(rowpart + colb_of(mt >> 2) * 2, rw, ldsb + wm * 2048);
        dma16<L_UNIT + 1024, 16 * LDW * 2>(rowpart + colb_of(mt >> 2) * 2, rw, ldsb + wm * 2048);
        dma16<L_UNIT + 8192, 0>(rowpart + colb_of(u1 >> 2) * 2, rw, ldsb + wm * 2048);
        dma16<L_UNIT + 8192 + 1024, 16 * LDW * 2>(rowpart + colb_of(u1 >> 2) * 2, rw, ldsb + wm * 2048);
        if (wave == 4) {
            if (QKV) {
                const srd_t rp = mk_srd(a.wphi, NH * 256 * 2);
                dma16<L_AUX, 0>((unsigned)(nt * 2048 + lane * 16), rp, ldsb);
                dma16<L_AUX + 1024, 1024>((unsigned)(nt * 2048 + lane * 16), rp, ldsb);
            } else {
                const srd_t rb = mk_srd(a.b1, FF * 2);
                dma16<L_AUX, 0>((unsigned)(nt * 448 + lane * 16), rb, ldsb);
            }
        }
    };
    if constexpr (WN == 1) stat_loads();
    ST(22);

    LAS char* wb[2]; LAS char* xb[2][4];
#pragma unroll
    for (int s = 0; s < 2; ++s) {
        wb[s] = lds + s * 65536 + hh * 2048 + WN * 512 + q4 * 64 + blk * 32 + p4 * 8;
        PIN(wb[s]);
        const int row = 32 * wm + r32;
#pragma unroll
        for (int k = 0; k < 4; ++k) { xb[s][k] = lds + s * 65536 + XOFF + row * 128 + (((2 * k + hh) ^ ((row >> 1) & 7)) * 16); PIN(xb[s][k]); }
    }
    const int csbase = QKV ? nt * 192 : 1536 + nt * 224;
    const unsigned trl = (4 * hh + q4) * 64 + blk * 32 + p4 * 8;

    f32x16 acc[2];
#pragma unroll
    for (int t = 0; t < 2; ++t)
#pragma unroll
        for (int i = 0; i < 16; ++i) acc[t][i] = 0.f;
    bf16x8 xf[32], wf[3][2];
    auto read_x = [&](int g) { const int sl = ((g >> 2) + 1) % 3; xf[g] = *(LAS bf16x8*)(xb[sl >> 1][g & 3] + (sl & 1) * SLOT); };
    auto read_w = [&](int g, int t) {
        const int sl = ((g >> 2) + 1) % 3;
        LAS char* p = wb[sl >> 1] + (sl & 1) * SLOT + (g & 3) * 4096 + t * 1024;
        wf[g % 3][t] = cat44(tr_read(p), tr_read(p + 256));
    };
    unsigned tag = 0;
    auto stat_task = [&]() {
        LAS char* p = lds + L_UNIT + wm * 2048 + trl;
        const unsigned one = 0x3F803F80u;
        float su[2];
#pragma unroll
        for (int u = 0; u < 2; ++u) {
            float s = 0.f;
#pragma unroll
            for (int i = 0; i < 4; ++i) { u32x2 w = __builtin_bit_cast(u32x2, tr_read(p + u * 8192 + i * 512)); s = dot2(w[1], one, dot2(w[0], one, s)); }
            su[u] = s;
        }
        auto sw = __builtin_amdgcn_permlane32_swap(f2u(su[0]), f2u(su[1]), false, false);
        *(LAS float*)(lds + L_CSP + ((hh * 4 + wm) * 32 + r32) * 4) = u2f(sw[0]) + u2f(sw[1]);
        float mean[2], vv[2];
#pragma unroll
        for (int r = 0; r < 2; ++r) {
            float e[8];
#pragma unroll
            for (int i = 0; i < 4; ++i) { e[2 * i] = u2f(rowv[r][i] << 16); e[2 * i + 1] = u2f(rowv[r][i] & 0xFFFF0000u); }
            mean[r] = wave_sum(((e[0] + e[1]) + (e[2] + e[3])) + ((e[4] + e[5]) + (e[6] + e[7]))) * (1.f / DM);
            float q = 0.f;
#pragma unroll
            for (int i = 0; i < 8; ++i) { float d = e[i] - mean[r]; q = __builtin_fmaf(d, d, q); }
            vv[r] = wave_sum(q) * (1.f / DM) + 1e-5f;
        }
        u32x4 g = {f2u(lane ? mean[1] : mean[0]), tag, f2u(lane ? vv[1] : vv[0]), tag};
        if (lane < 2) __builtin_amdgcn_raw_buffer_store_b128(g, rs, WS_RST + (srow + lane) * 16, 0, SC1);
    };
    u32x4 gst, gcs[NG]; u32x2 gv2;
    const unsigned gvo_st = (unsigned)(mt * 128 + 32 * wm + r32) * 16;
    const unsigned gvo_v2 = (unsigned)(G.mt2 * 64 + (wave & 1) * 32 + r32) * 16;
    const unsigned gvo_cs = (unsigned)(csbase + r32) * 32 + hh * 16;
    auto gather = [&]() {
        ld16c<WS_RST>(gst, gvo_st, rs);
        ld8c<WS_RST + 8>(gv2, gvo_v2, rs);
        static_for<0, NG>([&](auto T) { constexpr int t = T.value, c = t < 2 ? 2 * t + WN : (QKV ? 4 + WN : 4 + WN + (t - 2));
                                        ld16c<WS_CSG + c * 1024>(gcs[t], gvo_cs, rs); });
    };
    float vpe = 1.f, r_t = 1.f;
    bf16x8 fixT, fixF[NG];
    auto gather_check_and_fix = [&]() {
        for (unsigned spin = 0;; ++spin) {
            unsigned bad = (gst[1] ^ tag) | (gst[3] ^ tag) | (gv2[1] ^ tag);
#pragma unroll
            for (int t = 0; t < NG; ++t) bad |= (gcs[t][1] ^ tag) | (gcs[t][3] ^ tag);
            if (__all(bad == 0u) || spin > SPIN_CAP) break;
            __builtin_amdgcn_s_sleep(1);
            gather();
            gather_wait<0, NG>(gst, gv2, gcs);
        }
        vpe = u2f(gst[2]);
        vp2 = u2f(gv2[0]);
        r_t = __builtin_amdgcn_rsqf(vpe);
        unsigned s0, s1, s2;
        split3(hh ? vpe * r_t : -u2f(gst[0]), s0, s1, s2);
        u32x4 u;
        u[0] = hi2(s0, s1);
        u[1] = hh ? (s2 >> 16) : u[0];
        u[2] = hh ? 0u : hi2(s2, s0);
        u[3] = 0u;
        fixT = asbf(u);
        if (QKV) *(LAS float*)(lds + L_RR + (wave * 32 + r32) * 4) = r_t;
#pragma unroll
        for (int t = 0; t < NG; ++t) {
            unsigned c0, c1, c2;
            split3(half_sum(u2f(gcs[t][0]) + u2f(gcs[t][2])), c0, c1, c2);
            unsigned bb = 0;
            if (!QKV) bb = *(LAS unsigned short*)(lds + L_AUX + (tile_of(t) * 32 + r32) * 2);
            u32x4 w;
            w[0] = hh ? (bb | (bb << 16)) : hi2(c0, c0);
            w[1] = hh ? bb : hi2(c1, c1);
            w[2] = hh ? 0u : hi2(c0, c2);
            w[3] = 0u;
            fixF[t] = asbf(w);
        }
    };

    wait_vm<WN == 0 ? 9 : 7>();
    ST(23);
    barrier();
    ST(1);
#pragma unroll
    for (int g = 0; g < 2; ++g) { read_x(g); read_w(g, 0); read_w(g, 1); }
    static_for<0, 32>([&](auto GI) {
        constexpr int g = GI.value, j = g >> 2;
        SCHED0();
        if constexpr ((g & 3) == 2) {
            if constexpr ((WN == 1 && j == 0) || j == 7) { PIN(acc[0]); PIN(acc[1]); }
            if constexpr (WN == 0) {
                constexpr int n = j <= 5 ? 8 : (QKV ? 0 : (j == 6 ? 8 : 2));
                if constexpr (j == 0) asm volatile("s_waitcnt vmcnt(%1) ; PINW %0" : "+v"(epoch) : "i"(n) : "memory");
                else if constexpr (j == 7) gather_wait<n, NG>(gst, gv2, gcs);
                else wait_vm<n>();
                if constexpr (j == 0) tag = (unsigned)__builtin_amdgcn_readfirstlane((int)epoch) + 1u;
            } else {
                if constexpr (j == 0) {
                    asm volatile("s_waitcnt vmcnt(0) ; PINW %0 %1 %2" : "+v"(epoch), "+v"(rowv[0]), "+v"(rowv[1]) :: "memory");
                    tag = (unsigned)__builtin_amdgcn_readfirstlane((int)epoch) + 1u;
                    stat_task();
                }
                if constexpr (j == 7) gather_wait<CP, NG>(gst, gv2, gcs);
            }
            wait_lgkm0();
            barrier();
            ST(2 + j);
            if constexpr (WN == 1 && j == 0) {
                const int idx = 16 * wm + (lane & 15);
                LAS float* p = (LAS float*)(lds + L_CSP) + (idx >> 5) * 128 + (idx & 31);
                u32x2 gq = {f2u((p[0] + p[32]) + (p[64] + p[96])), tag};
                const int u = wm >= 2 ? u1 : mt;
                if (lane < 16) __builtin_amdgcn_raw_buffer_store_b64(gq, rs, WS_CSG + (csbase + (u >> 2) * 32 + (idx & 31)) * 32 + (u & 3) * 8, 0, SC1);
            }
            if constexpr (WN == 1 && j == 2) {
                const int gw = G.bid * 4 + wm;
                const char* p = lane < 14 ? (const char*)a.w2 + (size_t)(gw * 14 + lane) * 128 : (const char*)a.wo + (size_t)(gw * 4 + (lane & 3)) * 128;
                touch_ptr<L_DUMMY>(p, ldsb);
            }
            if constexpr (j == 6) gather();
            if constexpr (WN == 1 && j == 7) static_for<0, CP>([&](auto I) { dma_chunk(IC(3), I); });
            if constexpr (j == 7) gather_check_and_fix();
            SCHED0();
        }
        constexpr int jb = (g - 2) >> 2, q = (g - 2) & 3;
        static_for<0, 2>([&](auto TI) {
            constexpr int t = TI.value;
            if (QKV && t == 1) acc[t] = MFMA32(xf[g], wf[g % 3][t], acc[t]);
            else acc[t] = MFMA32(wf[g % 3][t], xf[g], acc[t]);
            if constexpr (g + 2 < 32) {
                if (t == 0) read_x(g + 2);
                read_w(g + 2, t);
            }
            constexpr int i = q * 2 + t;
            if constexpr (WN == 0) {
                if constexpr (g < 2) { dma_pair(IC(2), IC(4 * g + 2 * t)); dma_pair(IC(2), IC(4 * g + 2 * t + 1)); }
                else if constexpr (jb <= 4) dma_pair(IC(jb + 3), IC(i));
                else if constexpr (!QKV && jb == 5) dma_w2(IC(i));
                else if constexpr (!QKV && jb == 6 && i < 2) dma_w2(IC(8 + i));
            } else if constexpr (g >= 2 && jb >= 4 && jb <= 6 && i < CP) dma_chunk(IC(jb - 4), IC(i));
            SCHED0();
        });
    });
    ST(10);
    const int tt = mt * 4 + wm;
    f32x16 zero16;
#pragma unroll
    for (int i = 0; i < 16; ++i) zero16[i] = 0.f;
    auto store_h = [&](f32x16& c, int tile) {
#pragma unroll
        for (int i = 0; i < 16; ++i) { float v = fmx(c[i], 0.f); c[i] = v * v; }
        const unsigned off = WS_H + (unsigned)((tt * 112 + nt * 14 + tile * 2) * 64 + lane) * 16;
        __builtin_amdgcn_raw_buffer_store_b128(pack8u<0>(c), rs, off, 0, SC1);
        __builtin_amdgcn_raw_buffer_store_b128(pack8u<1>(c), rs, off + 1024, 0, SC1);
    };
    bf16x8 wfr[2];
    const float kscale = r_t * LOG2E;
    auto feature_map = [&](f32x16& p, u32x4& F0, u32x4& F1) {
        float mp = fmx(fmx(fmx(fmx(p[0], p[1]), p[2]), fmx(fmx(p[3], p[4]), p[5])), fmx(p[6], p[7]));
        float mn = fmx(fmx(fmx(fmx(p[8], p[9]), p[10]), fmx(fmx(p[11], p[12]), p[13])), fmx(p[14], p[15]));
        {
            auto s = __builtin_amdgcn_permlane32_swap(f2u(mp), f2u(mn), false, false);
            float c = -kscale * fmx(u2f(s[0]), u2f(s[1]));
            auto s2 = __builtin_amdgcn_permlane32_swap(f2u(c), f2u(c), false, false);
            mp = u2f(s2[0]); mn = u2f(s2[1]);
        }
#pragma unroll
        for (int i = 0; i < 8; ++i) {
            p[i] = __builtin_amdgcn_exp2f(__builtin_fmaf(p[i], kscale, mp));
            p[8 + i] = __builtin_amdgcn_exp2f(__builtin_fmaf(p[8 + i], kscale, mn));
        }
        float sp = ((p[0] + p[1]) + (p[2] + p[3])) + ((p[4] + p[5]) + (p[6] + p[7]));
        float sn = ((p[8] + p[9]) + (p[10] + p[11])) + ((p[12] + p[13]) + (p[14] + p[15]));
        {
            auto s = __builtin_amdgcn_permlane32_swap(f2u(sp), f2u(sn), false, false);
            float c = __builtin_amdgcn_rcpf(u2f(s[0]) + u2f(s[1]));
            auto s2 = __builtin_amdgcn_permlane32_swap(f2u(c), f2u(c), false, false);
            sp = u2f(s2[0]); sn = u2f(s2[1]);
        }
#pragma unroll
        for (int i = 0; i < 8; ++i) { p[i] *= sp; p[8 + i] *= sn; }
        F0 = pack8u<0>(p); F1 = pack8u<1>(p);
    };
    if constexpr (!QKV) {
        acc[0] = MFMA32(fixF[0], fixT, acc[0]);
        acc[1] = MFMA32(fixF[1], fixT, acc[1]);
        store_h(acc[0], WN);
        store_h(acc[1], 2 + WN);
    } else {
        acc[0] = MFMA32(fixF[0], fixT, acc[0]);
        acc[1] = MFMA32(fixT, fixF[1], acc[1]);
        bf16x8 idf[2];
#pragma unroll
        for (int s = 0; s < 2; ++s) {
            u32x4 u;
#pragma unroll
            for (int i = 0; i < 4; ++i) {
                int f0 = 16 * s + 8 * (i >> 1) + 4 * hh + 2 * (i & 1);
                u[i] = (r32 == f0 ? 0x3F80u : 0u) | (r32 == f0 + 1 ? 0x3F800000u : 0u);
            }
            idf[s] = asbf(u);
        }
#pragma unroll
        for (int X = 0; X < 2; ++X) {
            LAS char* p = lds + L_AUX + (2 * WN + X) * 512 + (4 * hh + q4) * 32 + p4 * 8;
            u32x4 u = __builtin_bit_cast(u32x4, cat44(tr_read(p), tr_read(p + 256)));
            const unsigned sg = blk ? 0x80008000u : 0u;
#pragma unroll
            for (int i = 0; i < 4; ++i) u[i] ^= sg;
            wfr[X] = asbf(u);
        }
#pragma unroll
        for (int g = 0; g < 4; ++g) {
            f32x4 rr = *(LAS f32x4*)(lds + L_RR + (wave * 32 + 8 * g + 4 * hh) * 4);
#pragma unroll
            for (int i = 0; i < 4; ++i) acc[1][4 * g + i] *= rr[i];
        }
        const u32x4 v0 = pack8u<0>(acc[1]), v1 = pack8u<1>(acc[1]);
        const bf16x8 kb0 = pack8<0>(acc[0]), kb1 = pack8<1>(acc[0]);
#pragma unroll
        for (int X = 0; X < 2; ++X) {
            f32x16 p = MFMA32(wfr[X], X ? kb1 : kb0, zero16);
            u32x4 F0, F1;
            feature_map(p, F0, F1);
            f32x16 d = MFMA32(asbf(F0), idf[0], zero16);
            d = MFMA32(asbf(F1), idf[1], d);
            u32x4 b0, b1;
#pragma unroll
            for (int i = 0; i < 4; ++i) { b0[i] = (blk == X) ? v0[i] : 0x3F803F80u; b1[i] = (blk == X) ? v1[i] : 0x3F803F80u; }
            f32x16 kv = MFMA32(pack8<0>(d), asbf(b0), zero16);
            kv = MFMA32(pack8<1>(d), asbf(b1), kv);
            const int row = X == 0 ? r32 : (r32 == 15 ? 16 : r32 - 16);
            if (X == 0 ? (r32 <= 16) : (r32 >= 15)) {
                LAS char* dst = lds + L_RED + (((WN * 2 + X) * 4 + wm) * 17 + row) * 144 + hh * 16;
#pragma unroll
                for (int g = 0; g < 4; ++g) {
                    f32x4 v = {kv[4 * g], kv[4 * g + 1], kv[4 * g + 2], kv[4 * g + 3]};
                    *(LAS f32x4*)(dst + g * 32) = v;
                }
            }
        }
        wait_lgkm0();
        ST(26);
        barrier();
        if constexpr (WN == 1) {
#pragma unroll
            for (int it = 0; it < 5; ++it) {
                const int o = it * 64 + lane;
                if (it < 4 || lane < 16) {
                    LAS char* src = lds + L_RED + (wm * 4 * 17 + (o >> 4)) * 144 + (o & 15) * 8;
                    f32x2 v = (*(LAS f32x2*)(src) + *(LAS f32x2*)(src + 2448)) + (*(LAS f32x2*)(src + 2 * 2448) + *(LAS f32x2*)(src + 3 * 2448));
                    u32x4 g0 = {f2u(v[0]), tag, f2u(v[1]), tag};
                    __builtin_amdgcn_raw_buffer_store_b128(g0, rs, WS_KVP + (unsigned)((mt * 32 + nt * 4 + wm) * 272 + o) * 16, 0, SC1);
                }
            }
        }
    }
    ST(27);
    f32x16 bc[NB2];
#pragma unroll
    for (int t = 0; t < NB2; ++t) bc[t] = MFMA32(fixF[2 + t], fixT, zero16);
    {
        LAS char* q0 = lds + hh * (NTB * 512) + WN * 512 + q4 * 64 + blk * 32 + p4 * 8 + chunk_base(2);
        LAS char* q1 = q0 + (chunk_base(0) - chunk_base(2));
        PIN(q0); PIN(q1);
        bf16x8 qf[4][NB2];
        auto read_q = [&](int g, int t) {
            const int c = g >> 3;
            LAS char* p = (c < 2 ? q1 + (chunk_base(c) - chunk_base(0)) : q0 + (chunk_base(c) - chunk_base(2))) + (g & 7) * (NTB * 1024) + t * 512;
            qf[g & 3][t] = cat44(tr_read(p), tr_read(p + 256));
        };
#pragma unroll
        for (int g = 0; g < 3; ++g)
#pragma unroll
            for (int t = 0; t < NB2; ++t) read_q(g, t);
        static_for<0, 32>([&](auto GI) {
            constexpr int g = GI.value;
            SCHED0();
            if constexpr (g == 8 || g == 16 || g == 24) {
                if constexpr (g == 8 && WN == 1) wait_vm<QKV ? 5 : 4>();
                wait_lgkm0(); barrier();
            }
            static_for<0, NB2>([&](auto TI) {
                constexpr int t = TI.value;
                bc[t] = MFMA32(qf[g & 3][t], xf[g], bc[t]);
                if constexpr (g + 3 < 32) read_q(g + 3, t);
                if constexpr (WN == 0) {
                    if constexpr (QKV && g >= 8 && g < 18) dma_w2(IC(g - 8));
                    if constexpr (g >= 24 && g < 30) dma_w2(IC(10 + g - 24));
                } else if constexpr (!QKV && g >= 16 && g < 24) dma_wo(IC(2 * (g - 16) + t));
                SCHED0();
            });
        });
    }
    ST(11);
    auto w2_to_regs = [&]() {
        LAS char* rdA = lds + wm * 2048 + trl;
#pragma unroll
        for (int s = 0; s < NR; ++s) {
            LAS char* p = rdA + s * 8192;
            ph[2 * s] = __builtin_bit_cast(u32x4, cat44(tr_read(p), tr_read(p + 1024)));
            ph[2 * s + 1] = __builtin_bit_cast(u32x4, cat44(tr_read(p + 512), tr_read(p + 1536)));
        }
        wait_lgkm0();
    };
    if constexpr (!QKV) {
#pragma unroll
        for (int t = 0; t < NB2; ++t) store_h(bc[t], 4 + WN + t);
        ST(12);
        if constexpr (WN == 0) {
            wait_vm<2>();
            w2_to_regs();
            static_for<2 * NB, 2 * NB + 8>([&](auto P) { dma_w2(P); });
            wait_vm<8>();
        } else {
            if (wm >= 2) {
                const int sl = (mt * 8 + nt) * 2 + (wm & 1);
                const unsigned vo = (unsigned)(((sl >> 3) * 136 + (sl & 7) * 17 + (lane < 34 ? (lane >> 1) : 0)) * 32 + (lane & 1) * 16);
                static_for<0, 16>([&](auto M) { ld16c<WS_KVP + M.value * 32 * 136 * 32>(ph[M.value], vo, rs); });
                wait16(ph);
                ph[0][0] = kv_reduce_publish(a.ws, ph, sl, lane, tag) ? 1u : 0u;
            } else wait_vm<0>();
        }
        wait_lgkm0();
        barrier();
        if (wave == 5 && lane == 0)
            __hip_atomic_store((GAS unsigned*)(a.ws + WS_FLAG + (mt * 16 + 8 + nt) * 64), tag, RLX_AGENT);
        if (wave == 4 && lane < 16) *(LAS unsigned*)(lds + L2_CNT + lane * 4) = 0u;
        wait_lgkm0();
        barrier();
    } else {
        const bf16x8 qb0 = pack8<0>(bc[0]), qb1 = pack8<1>(bc[0]);
        if constexpr (WN == 0) {
            wait_vm<0>(); w2_to_regs();
            static_for<2 * NB, 2 * NB + 8>([&](auto P) { dma_w2(P); });
        }
#pragma unroll
        for (int X = 0; X < 2; ++X) {
            f32x16 p = MFMA32(wfr[X], X ? qb1 : qb0, zero16);
            u32x4 F0, F1;
            feature_map(p, F0, F1);
            LAS char* dst = lds + L_PHI + ((2 * WN + X) * 4 + wm) * 2048 + lane * 16;
            *(LAS u32x4*)(dst) = F0;
            *(LAS u32x4*)(dst + 1024) = F1;
        }
        ST(12);
        wait_lgkm0();
        barrier();
        if constexpr (WN == 1) {
            if (wave == 4 && lane < 16) *(LAS unsigned*)(lds + L2_CNT + lane * 4) = 0u;
#pragma unroll
            for (int i = 0; i < 8; ++i) ph[i] = *(LAS u32x4*)(lds + L_PHI + wm * 8192 + i * 1024 + lane * 16);
            wait_lgkm0();
        }
        barrier();
    }
    ST(13);
    return tag;
}

#define HPV 6
constexpr int HP = HPV;
constexpr int p2_pos(bool isw, int idx) {
    int pos = 0;
    for (int s = 0; s < HP; ++s) { pos += 2; if (!isw && idx == s) return pos; }
    for (int i = 1; i < 28; ++i) {
        if (i - 1 + HP <= 27) { pos += 2; if (!isw && idx == i - 1 + HP) return pos; }
        if (i == 17) { pos += 2; if (isw) return pos; }
    }
    return 0;
}
constexpr int p2_issued(int step) {
    int pos = 2 * HP;
    for (int i = 1; i < step; ++i) { if (i - 1 + HP <= 27) pos += 2; if (i == 17) pos += 2; }
    return pos;
}
constexpr int p2_wait(int i) {
    int need = p2_pos(false, i);
    if (i + 1 == 27) { int w = p2_pos(true, 27); if (w > need) need = w; }
    return p2_issued(i) - need;
}
template <int SLEEP> DI void poll_words(srd_t rs, unsigned voff, unsigned tag) {
    for (unsigned spin = 0; spin <= SPIN_CAP; ++spin) {
        unsigned v;
        ld4c<0>(v, voff, rs);
        asm volatile("s_waitcnt vmcnt(0) ; PINW %0" : "+v"(v) :: "memory");
        if (__all(v == tag)) break;
        __builtin_amdgcn_s_sleep(SLEEP);
    }
}
DI void lds_wait_word(LAS char* p) {
    for (unsigned spin = 0; spin <= 16 * SPIN_CAP; ++spin) {
        if (__hip_atomic_load((LAS unsigned*)p, __ATOMIC_RELAXED, __HIP_MEMORY_SCOPE_WORKGROUP) != 0u) break;
        __builtin_amdgcn_s_sleep(1);
    }
    CFENCE();
}
DI void lds_set_word(LAS char* p) { __hip_atomic_store((LAS unsigned*)p, 1u, __ATOMIC_RELAXED, __HIP_MEMORY_SCOPE_WORKGROUP); }

struct Fin { f32x16 acc[2][2]; u32x2 xr0, xr1, br0, br1; };
template <int PART>
DI void phase2(const Args& a, LAS char* lds, const Geo& G, unsigned tag, u32x4 (&ph)[16], float vp2, Fin& F, unsigned long long* stamps) {
    const int lane = G.lane, wave = G.wave, mt = G.mt2, nt = G.nt2;
    const unsigned ldsb = (unsigned)(size_t)lds;
    const int hh = lane >> 5, r32 = lane & 31, blk = (lane >> 4) & 1, q4 = (lane & 15) >> 2, p4 = lane & 3;
    const int kq = wave & 3, hf = wave >> 2;
    const srd_t rs = mk_srd(a.ws, WS_END);
    const srd_t rw2 = mk_srd(a.w2, FF * DM * 2);
    const unsigned wvo = (unsigned)((((lane >> 2) & 7) * DM + nt * 64 + hh * 32 + (lane & 3) * 8) * 2);
    const unsigned trl = (4 * hh + q4) * 64 + blk * 32 + p4 * 8;
    const unsigned l16 = lane * 16;
    unsigned w2q[4];
#pragma unroll
    for (int q = 0; q < 4; ++q) w2q[q] = wvo + (unsigned)((kq * 448 + ((q + G.rq) & 3) * 112) * DM * 2);
    const int token = mt * 64 + (kq & 1) * 32 + r32;
    const int col0 = nt * 64 + (kq >> 1) * 32 + 16 * hf + 4 * hh;
    u32x2 &xr0 = F.xr0, &xr1 = F.xr1, &br0 = F.br0, &br1 = F.br1;
    auto final_loads = [&]() {
        const srd_t rx = mk_srd(a.x, SEQ * DM * 2), rb = mk_srd(a.b2, DM * 2);
        ld8<0>(xr0, (unsigned)(token * DM + col0) * 2, rx); ld8<16>(xr1, (unsigned)(token * DM + col0) * 2, rx);
        ld8<0>(br0, (unsigned)col0 * 2, rb); ld8<16>(br1, (unsigned)col0 * 2, rb);
    };
    f32x16 (&acc)[2][2] = F.acc;
    if constexpr (PART < 2) {
#pragma unroll
        for (int i = 0; i < 2; ++i)
#pragma unroll
            for (int j = 0; j < 2; ++j)
#pragma unroll
                for (int e = 0; e < 16; ++e) acc[i][j][e] = 0.f;
    }
    LAS char* upper = lds + L2_A + kq * 16384;
    if constexpr (PART == 0) {
        const unsigned lb = ldsb + kq * 2048;
        unsigned hq[4];
#pragma unroll
        for (int q = 0; q < 4; ++q) hq[q] = (unsigned)(((2 * mt) * 112 + 28 * kq + ((q + G.rq) & 3) * 7) * 1024) + l16;
        auto issue_w = [&](auto SI) {
            constexpr int s = SI.value;
            dma16<(s % NB) * 8192, (s % 7) * 16 * DM * 2>(w2q[s / 7], rw2, lb);
            dma16<(s % NB) * 8192 + 1024, ((s % 7) * 16 + 8) * DM * 2>(w2q[s / 7], rw2, lb);
        };
        u32x4 hb[HP][2];
        static_for<NR, NB>([&](auto SI) { issue_w(SI); });
        static_for<NB + 4, NR + NB>([&](auto SI) { issue_w(SI); });
        final_loads();
        LAS char* rdA = lds + kq * 2048 + trl;
        LAS char* rdB = rdA + 6 * 8192;
        LAS char* rdU = upper + trl;
        PIN(rdB); PIN(rdU);
        bf16x8 wf[2][2];
        auto rd = [&](int s, bf16x8 (&d)[2]) {
            LAS char* p = (s >= 19 && s <= 26) ? rdU + (s - 19) * 2048 : ((s % NB) < 6 ? rdA + (s % NB) * 8192 : rdB + (s % NB - 6) * 8192);
            d[0] = cat44(tr_read(p), tr_read(p + 1024));
            d[1] = cat44(tr_read(p + 512), tr_read(p + 1536));
        };
        ST(24);
        lds_wait_word(lds + L2_CNT + 16);
        ST(14);
        static_for<0, HP>([&](auto SI) {
            constexpr int s = SI.value;
            ld16c<WS_H + (s % 7) * 1024>(hb[s % HP][0], hq[s / 7], rs);
            ld16c<WS_H + (112 + s % 7) * 1024>(hb[s % HP][1], hq[s / 7], rs);
        });
        static_for<0, 28>([&](auto SI) {
            constexpr int s = SI.value;
            SCHED0();
            if constexpr (s == 18) {
                PIN(acc[0][0]); PIN(acc[0][1]); PIN(acc[1][0]); PIN(acc[1][1]);
                lds_wait_word(lds + L2_CNT + 32 + kq * 4);
            }
            asm volatile("s_waitcnt vmcnt(%2) ; PINW %0 %1" : "+v"(hb[s % HP][0]), "+v"(hb[s % HP][1]) : "i"(p2_wait(s)) : "memory");
            if constexpr (s == 0) ST(15);
            static_for<0, 4>([&](auto MI) {
                constexpr int m = MI.value, i = m >> 1, j = m & 1;
                acc[i][j] = MFMA32(s < NR ? asbf(ph[s < NR ? 2 * s + i : 0]) : wf[s & 1][i], asbf(hb[s % HP][j]), acc[i][j]);
                SCHED0();
                if constexpr (m == 0 && s + 1 >= NR && s + 1 < 28) rd(s + 1, wf[(s + 1) & 1]);
                constexpr int hs = s - 1 + HP;
                if constexpr (s >= 1 && hs <= 27 && m >= 1 && m < 3) ld16c<WS_H + ((m - 1) * 112 + hs % 7) * 1024>(hb[hs % HP][m - 1], hq[hs / 7], rs);
                if constexpr (s == 17 && m == 3) issue_w(IC(27));
                SCHED0();
            });
        });
        SCHED0();
        ST(16);
        asm volatile("s_waitcnt vmcnt(0) ; PINW %0 %1 %2 %3" : "+v"(xr0), "+v"(xr1), "+v"(br0), "+v"(br1) :: "memory");
    } else if constexpr (PART == 1) {
        const srd_t rwo = mk_srd(a.wo, DM * DM * 2);
        __builtin_amdgcn_s_setprio(3);
        bf16x8 wv[8][2];
        auto wo_on = [&]() {
#pragma unroll
            for (int h = 0; h < 8; ++h) {
                LAS char* p = upper + h * 2048 + trl;
                wv[h][0] = cat44(tr_read(p), tr_read(p + 1024));
                wv[h][1] = cat44(tr_read(p + 512), tr_read(p + 1536));
            }
            wait_lgkm0();
            static_for<0, 16>([&](auto P) { constexpr int s = 19 + (P.value >> 1);
                dma16<L2_A + P.value * 1024, ((s % 7) * 16 + (P.value & 1) * 8) * DM * 2>(w2q[s / 7], rw2, ldsb + kq * 16384); });
        };
        auto poll_h = [&]() {
            poll_words<1>(rs, WS_FLAG + (unsigned)((mt >> 1) * 16 + 8 + (lane & 7)) * 64, tag);
            if (lane == 0) lds_set_word(lds + L2_CNT + 16);
        };
        if (!G.qkv) {
            if (kq == 0) poll_h();
            if (kq >= 2 && __builtin_amdgcn_readfirstlane((int)ph[0][0]) == 0) {
                const int sl = (G.mt * 8 + G.nt) * 2 + (kq & 1);
                const unsigned vo = (unsigned)(((sl >> 3) * 136 + (sl & 7) * 17 + (lane < 34 ? (lane >> 1) : 0)) * 32 + (lane & 1) * 16);
                for (unsigned spin = 0; spin <= SPIN_CAP; ++spin) {
                    static_for<0, 16>([&](auto M) { ld16c<WS_KVP + M.value * 32 * 136 * 32>(ph[M.value], vo, rs); });
                    wait16(ph);
                    if (kv_reduce_publish(a.ws, ph, sl, lane, tag)) break;
                    __builtin_amdgcn_s_sleep(2);
                }
            }
            ST(24);
        } else {
            const unsigned kvo = (unsigned)((4 * G.nt + kq) * 2176 + hh * 272 + (r32 < 16 ? r32 : 16) * 16);
            const unsigned hfo = WS_FLAG + (unsigned)((mt >> 1) * 16 + 8 + (lane & 7)) * 64;
            const unsigned wovo = wvo + kq * (128 * DM * 2);
            u32x4 raw[4]; unsigned fl;
            bool hdone = kq != 0;
            auto request = [&]() { static_for<0, 4>([&](auto I) { ld16c<WS_KVR + I.value * 544>(raw[I.value], kvo, rs); }); ld4c<0>(fl, hfo, rs); };
            request();
            static_for<0, 16>([&](auto P) { dma16<L2_A + P.value * 1024, P.value * 8 * DM * 2>(wovo, rwo, ldsb + kq * 16384); });
            asm volatile("s_waitcnt vmcnt(16) ; PINW %0 %1 %2 %3 %4" : "+v"(raw[0]), "+v"(raw[1]), "+v"(raw[2]), "+v"(raw[3]), "+v"(fl) :: "memory");
            for (unsigned spin = 0; spin <= SPIN_CAP; ++spin) {
                if (!hdone && __all(fl == tag)) { hdone = true; if (lane == 0) lds_set_word(lds + L2_CNT + 16); }
                unsigned bad = 0;
#pragma unroll
                for (int i = 0; i < 4; ++i) bad |= (raw[i][1] ^ tag) | (raw[i][3] ^ tag);
                if (__all(bad == 0u)) break;
                __builtin_amdgcn_s_sleep(1);
                request();
                asm volatile("s_waitcnt vmcnt(0) ; PINW %0 %1 %2 %3 %4" : "+v"(raw[0]), "+v"(raw[1]), "+v"(raw[2]), "+v"(raw[3]), "+v"(fl) :: "memory");
            }
            ST(24);
            f32x16 zero16;
#pragma unroll
            for (int i = 0; i < 16; ++i) zero16[i] = 0.f;
            const u32x4 k0 = {raw[0][0], raw[0][2], raw[1][0], raw[1][2]}, k1 = {raw[2][0], raw[2][2], raw[3][0], raw[3][2]};
#pragma unroll
            for (int w = 0; w < 4; ++w) {
                f32x16 nd = MFMA32(asbf(k0), asbf(ph[2 * w]), zero16);
                nd = MFMA32(asbf(k1), asbf(ph[2 * w + 1]), nd);
                const float inv = __builtin_amdgcn_rcpf(nd[8] + 1e-6f);
#pragma unroll
                for (int e = 0; e < 8; ++e) nd[e] *= inv;
                __builtin_amdgcn_raw_buffer_store_b128(pack8u<0>(nd), rs, WS_O + (unsigned)(((G.mt * 4 + w) * 32 + G.nt * 4 + kq) * 1024) + l16, 0, SC1);
            }
            wait_vm<0>();
            unsigned old = 0;
            if (lane == 0) old = __hip_atomic_fetch_add((LAS unsigned*)(lds + L2_CNT), 1u, __ATOMIC_RELAXED, __HIP_MEMORY_SCOPE_WORKGROUP);
            if (lane == 0 && old == 3u) __hip_atomic_store((GAS unsigned*)(a.ws + WS_FLAG + (G.mt * 16 + G.nt) * 64), tag, RLX_AGENT);
            ST(25);
            if (!hdone) poll_h();
        }
        wo_on();
        final_loads();
        wait_vm<4>();
        if (lane == 0) lds_set_word(lds + L2_CNT + 32 + kq * 4);
        poll_words<4>(rs, WS_FLAG + (unsigned)((mt >> 1) * 16 + 2 * kq + (lane & 1)) * 64, tag);
        ST(14);
        const unsigned ovo = (unsigned)((2 * mt) * 32 + 8 * kq) * 1024 + l16;
        static_for<0, 16>([&](auto I) { constexpr int i = I.value; ld16c<WS_O + (i & 1) * 32768 + (i >> 1) * 1024>(ph[i], ovo, rs); });
        wait16(ph);
        ST(15);
#pragma unroll
        for (int h = 0; h < 8; ++h)
#pragma unroll
            for (int i = 0; i < 2; ++i)
#pragma unroll
                for (int j = 0; j < 2; ++j) acc[i][j] = MFMA32(wv[h][i], asbf(ph[2 * h + j]), acc[i][j]);
        ST(16);
        asm volatile("s_waitcnt vmcnt(0) ; PINW %0 %1 %2 %3" : "+v"(xr0), "+v"(xr1), "+v"(br0), "+v"(br1) :: "memory");
    } else {
    wait_lgkm0();
    barrier();
#pragma unroll
    for (int qd = 0; qd < 4; ++qd)
#pragma unroll
        for (int g = 0; g < 4; ++g) {
            const f32x16& c = acc[qd >> 1][qd & 1];
            f32x4 v = {c[4 * g], c[4 * g + 1], c[4 * g + 2], c[4 * g + 3]};
            *(LAS f32x4*)(lds + wave * 16384 + (qd * 4 + g) * 1024 + l16) = v;
        }
    wait_lgkm0();
    barrier();
    ST(17);
    const float r2 = __builtin_amdgcn_rcpf(vp2);
#pragma unroll
    for (int g = 0; g < 2; ++g) {
        LAS char* pf = lds + (kq * 4 + 2 * hf + g) * 1024 + l16;
        LAS char* pa = pf + 65536;
        f32x4 so = (*(LAS f32x4*)(pf) + *(LAS f32x4*)(pf + 16384)) + (*(LAS f32x4*)(pf + 2 * 16384) + *(LAS f32x4*)(pf + 3 * 16384));
        f32x4 ss = (*(LAS f32x4*)(pa) + *(LAS f32x4*)(pa + 16384)) + (*(LAS f32x4*)(pa + 2 * 16384) + *(LAS f32x4*)(pa + 3 * 16384));
        const u32x2 xr = g ? xr1 : xr0, br = g ? br1 : br0;
        float res[4];
#pragma unroll
        for (int i = 0; i < 4; ++i) {
            const float xv = u2f((i & 1) ? (xr[i >> 1] & 0xFFFF0000u) : (xr[i >> 1] << 16));
            const float bv = u2f((i & 1) ? (br[i >> 1] & 0xFFFF0000u) : (br[i >> 1] << 16));
            const float o = so[i] * r2 + bv;
            const float gt = o * __builtin_amdgcn_rcpf(1.f + __builtin_amdgcn_exp2f(-o * LOG2E));
            res[i] = xv + ss[i] * gt;
        }
        u32x2 ov = {pk2(res[0], res[1]), pk2(res[2], res[3])};
        *(GAS u32x2*)(a.out + (size_t)token * DM + col0 + 8 * g) = ov;
    }
    ST(18);
    if (G.bid == 0 && wave == 7) {
        for (unsigned spin = 0; spin <= SPIN_CAP; ++spin) {
            unsigned f0, f1, f2, f3;
            ld4c<WS_FLAG>(f0, (unsigned)lane * 64, rs); ld4c<WS_FLAG + 4096>(f1, (unsigned)lane * 64, rs);
            ld4c<WS_FLAG + 8192>(f2, (unsigned)lane * 64, rs); ld4c<WS_FLAG + 12288>(f3, (unsigned)lane * 64, rs);
            asm volatile("s_waitcnt vmcnt(0) ; PINW %0 %1 %2 %3" : "+v"(f0), "+v"(f1), "+v"(f2), "+v"(f3) :: "memory");
            if (__all(f0 == tag && f1 == tag && f2 == tag && f3 == tag)) break;
            __builtin_amdgcn_s_sleep(1);
        }
        if (lane == 0) __hip_atomic_store((GAS unsigned*)(a.ws + WS_CTRL), tag, RLX_AGENT);
    }
    }
}

__global__ void __launch_bounds__(512, 2) fused_kernel(Args a) {
    extern __shared__ __attribute__((aligned(16))) char smem[];
    LAS char* lds = (LAS char*)smem;
    unsigned long long stamps[28];
    ST(0);
    Geo G;
    G.bid = blockIdx.x;
    G.wave = __builtin_amdgcn_readfirstlane(threadIdx.x >> 6);
    G.lane = threadIdx.x & 63;
    const int xcd = G.bid & 7, loc = G.bid >> 3;
    const int sel = loc >> 2;
    G.mt = (xcd >> 1) * 4 + (loc & 3);
    G.nt = (xcd & 1) * 4 + (sel & 3);
    G.i16 = (xcd & 1) * 8 + sel;
    G.mt2 = G.mt * 2 + (xcd & 1);
    G.nt2 = sel;
    G.qkv = sel < 4;
#define RQ (sel & 3)
    G.rq = RQ;
    unsigned wvo0[4], xvo0[4];
    {
        const int lane = G.lane, wm = G.wave & 3, hh = lane >> 5;
        const unsigned ldsb = (unsigned)(size_t)lds;
        const int LDW = G.qkv ? 1536 : 1792;
        if (G.wave < TOUCH1) {
            const char* tp = (const char*)a.x + (size_t)(G.bid * 8 + (lane & 7)) * 1024 + G.wave * 128;
            if (lane >= 8 && lane < 14) tp = (const char*)a.wqkv + (size_t)G.wave * 196608 + (G.bid * 6 + lane - 8) * 128;
            if (lane >= 16 && lane < 23) tp = (const char*)a.w1 + (size_t)G.wave * 229376 + (G.bid * 7 + lane - 16) * 128;
            touch_ptr<L_DUMMY>(tp, ldsb);
        }
#pragma unroll
        for (int i = 0; i < 4; ++i) {
            const int row = 8 * (4 * wm + i) + (lane >> 3);
            xvo0[i] = (unsigned)(((G.mt * 128 + row) * DM + ((lane & 7) ^ ((row >> 1) & 7)) * 8) * 2);
            const int k8 = 2 * wm + (i >> 1), c = 2 * (i & 1) + hh;
            const int colb = G.qkv ? (((c >> 1) + 1) * 512 + 64 * G.nt + 32 * (c & 1)) : (224 * G.nt + 32 * c);
            wvo0[i] = (unsigned)(((k8 * 8 + ((lane >> 2) & 7)) * LDW + colb + (lane & 3) * 8) * 2);
        }
        if (G.wave >= 4) {
            const srd_t rx = mk_srd(a.x, SEQ * DM * 2);
            static_for<0, 4>([&](auto I) { dma16<SLOT + XOFF + I.value * 1024, 0>(xvo0[I.value], rx, ldsb + wm * 4096); });
        } else {
            const srd_t rw = mk_srd(G.qkv ? a.wqkv : a.w1, DM * LDW * 2);
            static_for<0, 4>([&](auto I) { dma16<SLOT + I.value * 1024, 0>(wvo0[I.value], rw, ldsb + wm * 4096); });
        }
        ST(21);
        barrier();
    }
    unsigned tag;
    float vp2 = 1.f;
    Fin F;
    if (G.wave < 4) {
        u32x4 ph[16];
        if (sel < 4) tag = phase1<true, 0>(a, lds, G, wvo0, xvo0, ph, vp2, stamps);
        else tag = phase1<false, 0>(a, lds, G, wvo0, xvo0, ph, vp2, stamps);
        phase2<0>(a, lds, G, tag, ph, vp2, F, stamps);
    } else {
        u32x4 ph[16];
#pragma unroll
        for (int i = 0; i < 16; ++i) ph[i] = u32x4{0u, 0u, 0u, 0u};
        if (sel < 4) tag = phase1<true, 1>(a, lds, G, wvo0, xvo0, ph, vp2, stamps);
        else tag = phase1<false, 1>(a, lds, G, wvo0, xvo0, ph, vp2, stamps);
        phase2<1>(a, lds, G, tag, ph, vp2, F, stamps);
    }
    {
        u32x4 ph[16];
        phase2<2>(a, lds, G, tag, ph, vp2, F, stamps);
    }
}

extern "C" void kernel_launch(void* const* d_in, const int* in_sizes, int n_in, void* d_out, int out_size, void* d_ws,
                              size_t ws_size, hipStream_t stream) {
    Args a;
    a.x = (const __bf16*)d_in[0]; a.wqkv = (const __bf16*)d_in[1]; a.wphi = (const __bf16*)d_in[2]; a.wo = (const __bf16*)d_in[3];
    a.w1 = (const __bf16*)d_in[5]; a.b1 = (const __bf16*)d_in[6]; a.w2 = (const __bf16*)d_in[7]; a.b2 = (const __bf16*)d_in[8];
    a.ws = (char*)d_ws; a.out = (__bf16*)d_out;
    (void)hipFuncSetAttribute((const void*)fused_kernel, hipFuncAttributeMaxDynamicSharedMemorySize, L_TOTAL);
    fused_kernel<<<256, 512, L_TOTAL, stream>>>(a);
}
```

```cpp
#include <hip/hip_runtime.h>
#include <hip/hip_bf16.h>
#include <stdint.h>
#include <type_traits>

typedef __attribute__((ext_vector_type(8))) __bf16 bf16x8;
typedef __attribute__((ext_vector_type(4))) __bf16 bf16x4;
typedef __attribute__((ext_vector_type(2))) __bf16 bf16x2;
typedef __attribute__((ext_vector_type(16))) float f32x16;
typedef __attribute__((ext_vector_type(4))) float f32x4;
typedef __attribute__((ext_vector_type(2))) float f32x2;
typedef __attribute__((ext_vector_type(4))) unsigned u32x4;
typedef __attribute__((ext_vector_type(2))) unsigned u32x2;
typedef __amdgpu_buffer_rsrc_t srd_t;
#define LAS __attribute__((address_space(3)))
#define GAS __attribute__((address_space(1)))
#define DI __device__ __forceinline__
#define MFMA32(a, b, c) __builtin_amdgcn_mfma_f32_32x32x16_bf16((a), (b), (c), 0, 0, 0)
#define SCHED0() __builtin_amdgcn_sched_barrier(0)
#define CFENCE() asm volatile("" ::: "memory")
#define RLX_AGENT __ATOMIC_RELAXED, __HIP_MEMORY_SCOPE_AGENT

constexpr int SEQ = 2048, DM = 512, NH = 32, FF = 1792;
constexpr float LOG2E = 1.4426950408889634f;
constexpr int SC1 = 16;
constexpr unsigned SPIN_CAP = 1u << 16;

constexpr unsigned WS_CTRL = 0;
constexpr unsigned WS_FLAG = 256;
constexpr unsigned WS_RST  = WS_FLAG + 256 * 64;
constexpr unsigned WS_CSG  = WS_RST + 2048 * 16;
constexpr unsigned WS_KVR  = WS_CSG + 3328 * 32;
constexpr unsigned WS_KVP  = WS_KVR + 32 * 2176;
constexpr unsigned WS_H    = (WS_KVP + 16u * 32 * 136 * 32 + 4095) & ~4095u;
constexpr unsigned WS_O    = WS_H + 64u * 112 * 1024;
constexpr unsigned WS_END  = WS_O + 64u * 32 * 1024;

#define ST(i)

constexpr int SLOT = 32768, XOFF = 16384;
constexpr int chunk_base(int c) { return c == 0 ? 98304 : (c == 1 ? 122880 : (c == 2 ? 40960 : 65536)); }
constexpr int L_UNIT = 131072;
constexpr int L_CSP = 147456;
constexpr int L_RR = L_CSP + 1024;
constexpr int L_AUX = L_RR + 1024;
constexpr int L_DUMMY = L_AUX + 2048;
constexpr int L_FIX = L_DUMMY + 256;
constexpr int L_RED = 0;
constexpr int L_PHI = 98304;
constexpr int NR = 8;
constexpr int NB = 11;
constexpr int L2_CNT = NB * 8192;
constexpr int L2_A = 98304;
constexpr int L_TOTAL = 163840;
static_assert(L_FIX + 8192 <= L_TOTAL, "lds");

template <int LOFF, int SOFF> DI void dma16(unsigned voff, srd_t srd, unsigned lbase) {
    unsigned t;
    lbase = (unsigned)__builtin_amdgcn_readfirstlane((int)lbase);
    asm volatile("s_add_i32 m0, %3, %4\n\ts_mov_b32 %0, %5\n\tbuffer_load_dwordx4 %1, %2, %0 offen lds"
                 : "=&s"(t) : "v"(voff), "s"(srd), "s"(lbase), "i"(LOFF), "i"(SOFF) : "memory", "scc");
}
template <int LOFF> DI void touch_ptr(const void* p, unsigned lbase) {
    asm volatile("s_add_i32 m0, %1, %2\n\ts_nop 0\n\tglobal_load_lds_dword %0, off" :: "v"(p), "s"(lbase), "i"(LOFF) : "memory", "scc");
}
template <int LOFF> DI void touch_c(unsigned voff, srd_t srd, unsigned lbase) {
    asm volatile("s_add_i32 m0, %2, %3\n\ts_nop 0\n\tbuffer_load_dword %0, %1, 0 offen sc1 lds" :: "v"(voff), "s"(srd), "s"(lbase), "i"(LOFF) : "memory", "scc");
}
template <int LOFF> DI void touch(unsigned voff, srd_t srd, unsigned lbase) {
    asm volatile("s_add_i32 m0, %2, %3\n\ts_nop 0\n\tbuffer_load_dword %0, %1, 0 offen lds" :: "v"(voff), "s"(srd), "s"(lbase), "i"(LOFF) : "memory", "scc");
}
template <int OFF> DI void ld16c(u32x4& d, unsigned voff, srd_t srd) {
    asm volatile("buffer_load_dwordx4 %0, %1, %2, %3 offen offset:%4 sc1" : "=v"(d) : "v"(voff), "s"(srd), "s"(OFF & ~4095), "i"(OFF & 4095) : "memory");
}
template <int OFF> DI void ld4c(unsigned& d, unsigned voff, srd_t srd) {
    asm volatile("buffer_load_dword %0, %1, %2, %3 offen offset:%4 sc1" : "=v"(d) : "v"(voff), "s"(srd), "s"(OFF & ~4095), "i"(OFF & 4095) : "memory");
}
template <int OFF> DI void ld8c(u32x2& d, unsigned voff, srd_t srd) {
    asm volatile("buffer_load_dwordx2 %0, %1, %2, %3 offen offset:%4 sc1" : "=v"(d) : "v"(voff), "s"(srd), "s"(OFF & ~4095), "i"(OFF & 4095) : "memory");
}
DI void dma16r(unsigned voff, srd_t srd, unsigned lds_dst) {
    asm volatile("s_mov_b32 m0, %2\n\ts_nop 0\n\tbuffer_load_dwordx4 %0, %1, 0 offen lds" :: "v"(voff), "s"(srd), "s"(lds_dst) : "memory");
}
DI void ld16(u32x4& d, unsigned voff, srd_t srd) { asm volatile("buffer_load_dwordx4 %0, %1, %2, 0 offen" : "=v"(d) : "v"(voff), "s"(srd) : "memory"); }
template <int OFF> DI void ld8(u32x2& d, unsigned voff, srd_t srd) { asm volatile("buffer_load_dwordx2 %0, %1, %2, 0 offen offset:%3" : "=v"(d) : "v"(voff), "s"(srd), "i"(OFF) : "memory"); }
DI float fmx(float a, float b) { return __builtin_elementwise_maximum(a, b); }
DI void wait_lgkm0() { __builtin_amdgcn_s_waitcnt(0xC07F); CFENCE(); }
template <int N> DI void wait_vm() { asm volatile("s_waitcnt vmcnt(%0)" ::"i"(N) : "memory"); }
DI void barrier() { __builtin_amdgcn_s_barrier(); CFENCE(); }
#define PIN(x) asm volatile("" : "+v"(x))
DI srd_t mk_srd(const void* p, unsigned bytes) { return __builtin_amdgcn_make_buffer_rsrc((void*)p, 0, (int)bytes, 0x00020000); }

DI bf16x4 tr_read(LAS char* p) { return __builtin_amdgcn_ds_read_tr16_b64_v4bf16((LAS bf16x4*)p); }
DI bf16x8 cat44(bf16x4 a, bf16x4 b) { return __builtin_shufflevector(a, b, 0, 1, 2, 3, 4, 5, 6, 7); }
DI float dot2(unsigned a, unsigned b, float c) {
    return __builtin_amdgcn_fdot2_f32_bf16(__builtin_bit_cast(bf16x2, a), __builtin_bit_cast(bf16x2, b), c, false);
}
DI unsigned pk2(float a, float b) { f32x2 v = {a, b}; return __builtin_bit_cast(unsigned, __builtin_convertvector(v, bf16x2)); }
template <int S> DI u32x4 pack8u(const f32x16& x) {
    u32x4 p = {pk2(x[8 * S], x[8 * S + 1]), pk2(x[8 * S + 2], x[8 * S + 3]), pk2(x[8 * S + 4], x[8 * S + 5]), pk2(x[8 * S + 6], x[8 * S + 7])};
    return p;
}
template <int S> DI bf16x8 pack8(const f32x16& x) { return __builtin_bit_cast(bf16x8, pack8u<S>(x)); }
DI bf16x8 asbf(u32x4 u) { return __builtin_bit_cast(bf16x8, u); }
DI float u2f(unsigned u) { return __builtin_bit_cast(float, u); }
DI unsigned f2u(float f) { return __builtin_bit_cast(unsigned, f); }
DI unsigned hi2(unsigned lo, unsigned hi) { return __builtin_amdgcn_perm(hi, lo, 0x07060302u); }
DI void split3(float v, unsigned& s0, unsigned& s1, unsigned& s2) {
    s0 = f2u(v) & 0xFFFF0000u;
    float r = v - u2f(s0);
    s1 = f2u(r) & 0xFFFF0000u;
    s2 = f2u(r - u2f(s1));
}
DI float half_sum(float v) { auto s = __builtin_amdgcn_permlane32_swap(f2u(v), f2u(v), false, false); return u2f(s[0]) + u2f(s[1]); }
DI float wave_sum(float v) {
#define DPPADD(ctrl, rm) v += u2f((unsigned)__builtin_amdgcn_update_dpp(0, (int)f2u(v), ctrl, rm, 0xf, true))
    DPPADD(0x111, 0xf); DPPADD(0x112, 0xf); DPPADD(0x114, 0xf); DPPADD(0x118, 0xf); DPPADD(0x142, 0xa); DPPADD(0x143, 0xc);
#undef DPPADD
    return u2f((unsigned)__builtin_amdgcn_readlane((int)f2u(v), 63));
}
template <int C, int N, class F> DI void static_for(F&& f) { if constexpr (C < N) { f(std::integral_constant<int, C>{}); static_for<C + 1, N>(f); } }

#define NO_L2W
#define TOUCH1 2
struct Args {
    const __bf16* x; const __bf16* wqkv; const __bf16* wphi; const __bf16* wo;
    const __bf16* w1; const __bf16* b1; const __bf16* w2; const __bf16* b2;
    char* ws; __bf16* out;
};
struct Geo { int bid, wave, lane, mt, nt, i16, mt2, nt2, qkv, rq; };
#define IC(v) std::integral_constant<int, (v)>{}

template <int N> DI void gather_wait(u32x4& gst, u32x2& gv2, u32x4& gcs) {
    asm volatile("s_waitcnt vmcnt(%3) ; PINW %0 %1 %2" : "+v"(gst), "+v"(gv2), "+v"(gcs) : "i"(N) : "memory");
}
DI void wait16(u32x4 (&v)[16]) {
    asm volatile("s_waitcnt vmcnt(0) ; PINW %0 %1 %2 %3 %4 %5 %6 %7 %8 %9 %10 %11 %12 %13 %14 %15"
                 : "+v"(v[0]), "+v"(v[1]), "+v"(v[2]), "+v"(v[3]), "+v"(v[4]), "+v"(v[5]), "+v"(v[6]), "+v"(v[7]),
                   "+v"(v[8]), "+v"(v[9]), "+v"(v[10]), "+v"(v[11]), "+v"(v[12]), "+v"(v[13]), "+v"(v[14]), "+v"(v[15]) :: "memory");
}

DI bool kv_reduce_publish(char* ws, u32x4 (&v)[16], int sl, int lane, unsigned tag) {
    unsigned bad = 0;
#pragma unroll
    for (int m = 0; m < 16; ++m) bad |= (v[m][1] ^ tag) | (v[m][3] ^ tag);
    if (!__all(bad == 0u)) return false;
    float s0[16], s1[16];
#pragma unroll
    for (int m = 0; m < 16; ++m) { s0[m] = u2f(v[m][0]); s1[m] = u2f(v[m][2]); }
#pragma unroll
    for (int st = 8; st >= 1; st >>= 1)
#pragma unroll
        for (int m = 0; m < st; ++m) { s0[m] += s0[m + st]; s1[m] += s1[m + st]; }
    const int g = (sl & 7) * 17 + (lane < 34 ? (lane >> 1) : 0), row = g >> 3, fq = g & 7;
    const unsigned off = WS_KVR + (sl >> 3) * 2176 + fq * 272 + row * 16 + (lane & 1) * 8;
    if (lane < 34)
        __hip_atomic_store((GAS unsigned long long*)(ws + off), (unsigned long long)pk2(s0[0], s1[0]) | ((unsigned long long)tag << 32), RLX_AGENT);
    return true;
}
template <bool QKV, int WN>
DI unsigned phase1(const Args& a, LAS char* lds, const Geo& G, const unsigned (&wvo0)[4], const unsigned (&xvo0)[4],
                   unsigned epoch, const u32x4 (&rowv)[2], u32x4 (&ph)[16], float& vp2, unsigned long long* stamps) {
    constexpr int NTB = QKV ? 2 : 3;
    constexpr int NB2 = QKV ? 1 : (WN == 0 ? 1 : 2);
    constexpr int NG = 2 + NB2;
    constexpr int NU = QKV ? 24 : 28;
    constexpr int LDW = QKV ? 1536 : 1792;
    constexpr int CP = NTB * 2;
    const int lane = G.lane, wave = G.wave, wm = wave & 3, mt = G.mt, nt = G.nt;
    const unsigned ldsb = (unsigned)(size_t)lds;
    const int hh = lane >> 5, r32 = lane & 31, blk = (lane >> 4) & 1, q4 = (lane & 15) >> 2, p4 = lane & 3;
    const srd_t rs = mk_srd(a.ws, WS_END);
    const srd_t rx = mk_srd(a.x, SEQ * DM * 2);
    const srd_t rw = mk_srd(QKV ? a.wqkv : a.w1, DM * LDW * 2);
    const srd_t rw2 = mk_srd(a.w2, FF * DM * 2);
    const srd_t rwo = mk_srd(a.wo, DM * DM * 2);
    auto colb_of = [&](int c) { return QKV ? ((((c >> 1) + 1) % 3) * 512 + 64 * nt + 32 * (c & 1)) : (224 * nt + 32 * c); };
    auto tile_of = [&](int t) { return t < 2 ? 2 * t + WN : (QKV ? 4 + WN : 4 + WN + (t - 2)); };
    auto dma_pair = [&](auto JI, auto II) {
        constexpr int j = JI.value, i = II.value, sb = ((j + 1) % 3) * SLOT;
        if constexpr (i < 4) dma16<sb + i * 1024, j * 64 * LDW * 2>(wvo0[i], rw, ldsb + wm * 4096);
        else dma16<sb + XOFF + (i - 4) * 1024, j * 128>(xvo0[i - 4], rx, ldsb + wm * 4096);
    };
    const unsigned w2vo = (unsigned)((((lane >> 2) & 7) * DM + G.nt2 * 64 + hh * 32 + (lane & 3) * 8) * 2);
    unsigned w2q[3];
#pragma unroll
    for (int q = 0; q < 3; ++q) w2q[q] = w2vo + (unsigned)((wm * 448 + ((q + G.rq) & 3) * 112) * DM * 2);
    auto dma_w2 = [&](auto PI) {
        constexpr int r = PI.value >> 1, t = PI.value & 1;
        dma16<(r % NB) * 8192 + t * 1024, ((r % 7) * 16 + t * 8) * DM * 2>(w2q[r / 7], rw2, ldsb + wm * 2048);
    };
    const unsigned wovo = w2vo + wm * (128 * DM * 2);
    auto dma_wo = [&](auto PI) { dma16<L2_A + PI.value * 1024, PI.value * 8 * DM * 2>(wovo, rwo, ldsb + wm * 16384); };
    unsigned cvo[WN == 1 ? NTB : 1];
    if constexpr (WN == 1) {
#pragma unroll
        for (int i = 0; i < NTB; ++i) {
            const int st = 2 * i + hh, k8 = 4 * wm + (st >= NTB ? 1 : 0), c = st >= NTB ? st - NTB : st;
            cvo[i] = (unsigned)(((k8 * 8 + ((lane >> 2) & 7)) * LDW + colb_of(4 + c) + (lane & 3) * 8) * 2);
        }
    }
    auto dma_chunk = [&](auto CI, auto II) {
        constexpr int c = CI.value, i = II.value;
        dma16<chunk_base(c) + i * 1024, (c * 128 + (i / NTB) * 16) * LDW * 2>(cvo[i % NTB], rw, ldsb + wm * (CP * 1024));
    };
    const int srow = mt * 128 + G.i16 * 8 + 2 * wm;
    const int u1 = mt + 16 < NU ? mt + 16 : mt;
    const unsigned tag = (unsigned)__builtin_amdgcn_readfirstlane((int)epoch) + 1u;
    const unsigned trl = (4 * hh + q4) * 64 + blk * 32 + p4 * 8;
    ST(20);
    if constexpr (WN == 0) {
        LAS char* p = lds + L_UNIT + wm * 2048 + trl;
        const unsigned one = 0x3F803F80u;
        float su[2];
#pragma unroll
        for (int u = 0; u < 2; ++u) {
            float s = 0.f;
#pragma unroll
            for (int i = 0; i < 4; ++i) { u32x2 w = __builtin_bit_cast(u32x2, tr_read(p + u * 8192 + i * 512)); s = dot2(w[1], one, dot2(w[0], one, s)); }
            su[u] = s;
        }
        auto sw = __builtin_amdgcn_permlane32_swap(f2u(su[0]), f2u(su[1]), false, false);
        *(LAS float*)(lds + L_CSP + ((hh * 4 + wm) * 32 + r32) * 4) = u2f(sw[0]) + u2f(sw[1]);
        static_for<0, 8>([&](auto I) { dma_pair(IC(1), I); });
    } else {
        float mean[2], vv[2];
#pragma unroll
        for (int r = 0; r < 2; ++r) {
            float e[8];
#pragma unroll
            for (int i = 0; i < 4; ++i) { e[2 * i] = u2f(rowv[r][i] << 16); e[2 * i + 1] = u2f(rowv[r][i] & 0xFFFF0000u); }
            mean[r] = wave_sum(((e[0] + e[1]) + (e[2] + e[3])) + ((e[4] + e[5]) + (e[6] + e[7]))) * (1.f / DM);
            float q = 0.f;
#pragma unroll
            for (int i = 0; i < 8; ++i) { float d = e[i] - mean[r]; q = __builtin_fmaf(d, d, q); }
            vv[r] = wave_sum(q) * (1.f / DM) + 1e-5f;
        }
        u32x4 g = {f2u(lane ? mean[1] : mean[0]), tag, f2u(lane ? vv[1] : vv[0]), tag};
        if (lane < 2) __builtin_amdgcn_raw_buffer_store_b128(g, rs, WS_RST + (srow + lane) * 16, 0, SC1);
    }
    ST(22);

    LAS char* wb[2]; LAS char* xb[2][4];
#pragma unroll
    for (int s = 0; s < 2; ++s) {
        wb[s] = lds + s * 65536 + hh * 2048 + WN * 512 + q4 * 64 + blk * 32 + p4 * 8;
        PIN(wb[s]);
        const int row = 32 * wm + r32;
#pragma unroll
        for (int k = 0; k < 4; ++k) { xb[s][k] = lds + s * 65536 + XOFF + row * 128 + (((2 * k + hh) ^ ((row >> 1) & 7)) * 16); PIN(xb[s][k]); }
    }
    const int csbase = QKV ? nt * 192 : 1536 + nt * 224;

    f32x16 acc[2];
#pragma unroll
    for (int t = 0; t < 2; ++t)
#pragma unroll
        for (int i = 0; i < 16; ++i) acc[t][i] = 0.f;
    bf16x8 xf[32], wf[3][2];
    auto read_x = [&](int g) { const int sl = ((g >> 2) + 1) % 3; xf[g] = *(LAS bf16x8*)(xb[sl >> 1][g & 3] + (sl & 1) * SLOT); };
    auto read_w = [&](int g, int t) {
        const int sl = ((g >> 2) + 1) % 3;
        LAS char* p = wb[sl >> 1] + (sl & 1) * SLOT + (g & 3) * 4096 + t * 1024;
        wf[g % 3][t] = cat44(tr_read(p), tr_read(p + 256));
    };
    u32x4 gst, gcs; u32x2 gv2;
    const int mytile = wm < NG ? (wm < 2 ? 2 * wm + WN : (QKV ? 4 + WN : 2 + WN + wm)) : WN;
    const unsigned gvo_st = (unsigned)(mt * 128 + 32 * wm + r32) * 16;
    const unsigned gvo_v2 = (unsigned)(G.mt2 * 64 + (wave & 1) * 32 + r32) * 16;
    const unsigned gvo_cs = (unsigned)(csbase + mytile * 32 + r32) * 32 + hh * 16;
    auto gather = [&]() {
        ld16c<WS_RST>(gst, gvo_st, rs);
        ld8c<WS_RST + 8>(gv2, gvo_v2, rs);
        ld16c<WS_CSG>(gcs, gvo_cs, rs);
    };
    float vpe = 1.f, r_t = 1.f;
    bf16x8 fixT, fixF[NG];
    auto gather_check_and_fix = [&]() {
        for (unsigned spin = 0;; ++spin) {
            const unsigned bad = (gst[1] ^ tag) | (gst[3] ^ tag) | (gv2[1] ^ tag) | (gcs[1] ^ tag) | (gcs[3] ^ tag);
            if (__all(bad == 0u) || spin > SPIN_CAP) break;
            __builtin_amdgcn_s_sleep(1);
            gather();
            gather_wait<0>(gst, gv2, gcs);
        }
        vpe = u2f(gst[2]);
        vp2 = u2f(gv2[0]);
        r_t = __builtin_amdgcn_rsqf(vpe);
        unsigned s0, s1, s2;
        split3(hh ? vpe * r_t : -u2f(gst[0]), s0, s1, s2);
        u32x4 u;
        u[0] = hi2(s0, s1);
        u[1] = hh ? (s2 >> 16) : u[0];
        u[2] = hh ? 0u : hi2(s2, s0);
        u[3] = 0u;
        fixT = asbf(u);
        if (QKV) *(LAS float*)(lds + L_RR + (wave * 32 + r32) * 4) = r_t;
        unsigned c0, c1, c2;
        split3(half_sum(u2f(gcs[0]) + u2f(gcs[2])), c0, c1, c2);
        unsigned bb = 0;
        if (!QKV) bb = *(LAS unsigned short*)(lds + L_AUX + (mytile * 32 + r32) * 2);
        u32x4 w;
        w[0] = hh ? (bb | (bb << 16)) : hi2(c0, c0);
        w[1] = hh ? bb : hi2(c1, c1);
        w[2] = hh ? 0u : hi2(c0, c2);
        w[3] = 0u;
        *(LAS u32x4*)(lds + L_FIX + wave * 1024 + lane * 16) = w;
    };

    wait_vm<WN == 0 ? 8 : 1>();
    wait_lgkm0();
    ST(23);
    barrier();
    ST(1);
#pragma unroll
    for (int g = 0; g < 2; ++g) { read_x(g); read_w(g, 0); read_w(g, 1); }
    if constexpr (WN == 1) {
        const int idx = 16 * wm + (lane & 15);
        LAS float* p = (LAS float*)(lds + L_CSP) + (idx >> 5) * 128 + (idx & 31);
        u32x2 gq = {f2u((p[0] + p[32]) + (p[64] + p[96])), tag};
        const int u = wm >= 2 ? u1 : mt;
        if (lane < 16) __builtin_amdgcn_raw_buffer_store_b64(gq, rs, WS_CSG + (csbase + (u >> 2) * 32 + (idx & 31)) * 32 + (u & 3) * 8, 0, SC1);
        if (wave == 4) {
            if (QKV) {
                const srd_t rp = mk_srd(a.wphi, NH * 256 * 2);
                dma16<L_AUX, 0>((unsigned)(nt * 2048 + lane * 16), rp, ldsb);
                dma16<L_AUX + 1024, 1024>((unsigned)(nt * 2048 + lane * 16), rp, ldsb);
            } else {
                const srd_t rb = mk_srd(a.b1, FF * 2);
                dma16<L_AUX, 0>((unsigned)(nt * 448 + lane * 16), rb, ldsb);
            }
        }
    }
    static_for<0, 32>([&](auto GI) {
        constexpr int g = GI.value, j = g >> 2;
        SCHED0();
        if constexpr ((g & 3) == 2) {
            constexpr int JG = WN == 0 ? 4 : 5;
            if constexpr (j == JG + 1) { PIN(acc[0]); PIN(acc[1]); }
            if constexpr (WN == 0) {
                constexpr int n = j <= 5 ? 8 : (j == 6 ? (QKV ? 0 : 8) : 63);
                if constexpr (j == JG + 1) gather_wait<n>(gst, gv2, gcs);
                else wait_vm<n>();
            } else if constexpr (j == 4) wait_vm<0>();
            else if constexpr (j == JG + 1) gather_wait<CP>(gst, gv2, gcs);
            else if constexpr (j == 7) wait_vm<CP>();
            wait_lgkm0();
            barrier();
            ST(2 + j);
            if constexpr (WN == 1 && j == 1) {
                const int gw = G.bid * 4 + wm;
                const char* p = lane < 14 ? (const char*)a.w2 + (size_t)(gw * 14 + lane) * 128 : (const char*)a.wo + (size_t)(gw * 4 + (lane & 3)) * 128;
                touch_ptr<L_DUMMY>(p, ldsb);
            }
            if constexpr (j == JG) gather();
            if constexpr (WN == 1 && j == 7) static_for<0, CP>([&](auto I) { dma_chunk(IC(3), I); });
            if constexpr (j == JG + 1) gather_check_and_fix();
            if constexpr (j == 7) {
#pragma unroll
                for (int t = 0; t < NG; ++t) fixF[t] = *(LAS bf16x8*)(lds + L_FIX + (WN * 4 + t) * 1024 + lane * 16);
            }
            SCHED0();
        }
        constexpr int jb = (g - 2) >> 2, q = (g - 2) & 3;
        static_for<0, 2>([&](auto TI) {
            constexpr int t = TI.value;
            if (QKV && t == 1) acc[t] = MFMA32(xf[g], wf[g % 3][t], acc[t]);
            else acc[t] = MFMA32(wf[g % 3][t], xf[g], acc[t]);
            if constexpr (g + 2 < 32) {
                if (t == 0) read_x(g + 2);
                read_w(g + 2, t);
            }
            constexpr int i = q * 2 + t;
            if constexpr (WN == 0) {
                if constexpr (g < 2) { dma_pair(IC(2), IC(4 * g + 2 * t)); dma_pair(IC(2), IC(4 * g + 2 * t + 1)); }
                else if constexpr (jb <= 4) dma_pair(IC(jb + 3), IC(i));
                else if constexpr (!QKV && jb == 5) dma_w2(IC(i));
                else if constexpr (!QKV && jb == 6 && i < 2) dma_w2(IC(8 + i));
            } else if constexpr (g >= 2 && jb >= 4 && jb <= 6 && i < CP) dma_chunk(IC(jb - 4), IC(i));
            SCHED0();
        });
    });
    ST(10);
    const int tt = mt * 4 + wm;
    f32x16 zero16;
#pragma unroll
    for (int i = 0; i < 16; ++i) zero16[i] = 0.f;
    auto store_h = [&](f32x16& c, int tile) {
#pragma unroll
        for (int i = 0; i < 16; ++i) { float v = fmx(c[i], 0.f); c[i] = v * v; }
        const unsigned off = WS_H + (unsigned)((tt * 112 + nt * 14 + tile * 2) * 64 + lane) * 16;
        __builtin_amdgcn_raw_buffer_store_b128(pack8u<0>(c), rs, off, 0, SC1);
        __builtin_amdgcn_raw_buffer_store_b128(pack8u<1>(c), rs, off + 1024, 0, SC1);
    };
    bf16x8 wfr[2];
    const float kscale = r_t * LOG2E;
    auto feature_map2 = [&](f32x16 (&p)[2], u32x4 (&F)[2][2]) {
        float m[2][2], sm[2][2];
#pragma unroll
        for (int X = 0; X < 2; ++X)
#pragma unroll
            for (int h = 0; h < 2; ++h) {
                const int o = 8 * h;
                m[X][h] = fmx(fmx(fmx(fmx(p[X][o], p[X][o + 1]), p[X][o + 2]), fmx(fmx(p[X][o + 3], p[X][o + 4]), p[X][o + 5])), fmx(p[X][o + 6], p[X][o + 7]));
            }
#pragma unroll
        for (int X = 0; X < 2; ++X) {
            auto s = __builtin_amdgcn_permlane32_swap(f2u(m[X][0]), f2u(m[X][1]), false, false);
            float c = -kscale * fmx(u2f(s[0]), u2f(s[1]));
            auto s2 = __builtin_amdgcn_permlane32_swap(f2u(c), f2u(c), false, false);
            m[X][0] = u2f(s2[0]); m[X][1] = u2f(s2[1]);
        }
#pragma unroll
        for (int i = 0; i < 16; ++i)
#pragma unroll
            for (int X = 0; X < 2; ++X) p[X][i] = __builtin_amdgcn_exp2f(__builtin_fmaf(p[X][i], kscale, m[X][i >> 3]));
#pragma unroll
        for (int X = 0; X < 2; ++X)
#pragma unroll
            for (int h = 0; h < 2; ++h) {
                const int o = 8 * h;
                sm[X][h] = ((p[X][o] + p[X][o + 1]) + (p[X][o + 2] + p[X][o + 3])) + ((p[X][o + 4] + p[X][o + 5]) + (p[X][o + 6] + p[X][o + 7]));
            }
#pragma unroll
        for (int X = 0; X < 2; ++X) {
            auto s = __builtin_amdgcn_permlane32_swap(f2u(sm[X][0]), f2u(sm[X][1]), false, false);
            float c = __builtin_amdgcn_rcpf(u2f(s[0]) + u2f(s[1]));
            auto s2 = __builtin_amdgcn_permlane32_swap(f2u(c), f2u(c), false, false);
            sm[X][0] = u2f(s2[0]); sm[X][1] = u2f(s2[1]);
        }
#pragma unroll
        for (int X = 0; X < 2; ++X) {
#pragma unroll
            for (int i = 0; i < 16; ++i) p[X][i] *= sm[X][i >> 3];
            F[X][0] = pack8u<0>(p[X]); F[X][1] = pack8u<1>(p[X]);
        }
    };
    if constexpr (!QKV) {
        acc[0] = MFMA32(fixF[0], fixT, acc[0]);
        acc[1] = MFMA32(fixF[1], fixT, acc[1]);
        store_h(acc[0], WN);
        store_h(acc[1], 2 + WN);
    } else {
        acc[0] = MFMA32(fixF[0], fixT, acc[0]);
        acc[1] = MFMA32(fixT, fixF[1], acc[1]);
        bf16x8 idf[2];
#pragma unroll
        for (int s = 0; s < 2; ++s) {
            u32x4 u;
#pragma unroll
            for (int i = 0; i < 4; ++i) {
                int f0 = 16 * s + 8 * (i >> 1) + 4 * hh + 2 * (i & 1);
                u[i] = (r32 == f0 ? 0x3F80u : 0u) | (r32 == f0 + 1 ? 0x3F800000u : 0u);
            }
            idf[s] = asbf(u);
        }
#pragma unroll
        for (int X = 0; X < 2; ++X) {
            LAS char* p = lds + L_AUX + (2 * WN + X) * 512 + (4 * hh + q4) * 32 + p4 * 8;
            u32x4 u = __builtin_bit_cast(u32x4, cat44(tr_read(p), tr_read(p + 256)));
            const unsigned sg = blk ? 0x80008000u : 0u;
#pragma unroll
            for (int i = 0; i < 4; ++i) u[i] ^= sg;
            wfr[X] = asbf(u);
        }
#pragma unroll
        for (int g = 0; g < 4; ++g) {
            f32x4 rr = *(LAS f32x4*)(lds + L_RR + (wave * 32 + 8 * g + 4 * hh) * 4);
#pragma unroll
            for (int i = 0; i < 4; ++i) acc[1][4 * g + i] *= rr[i];
        }
        const u32x4 v0 = pack8u<0>(acc[1]), v1 = pack8u<1>(acc[1]);
        const bf16x8 kb0 = pack8<0>(acc[0]), kb1 = pack8<1>(acc[0]);
        {
            f32x16 p[2], d[2], kv[2];
            u32x4 F[2][2];
            p[0] = MFMA32(wfr[0], kb0, zero16);
            p[1] = MFMA32(wfr[1], kb1, zero16);
            feature_map2(p, F);
#pragma unroll
            for (int X = 0; X < 2; ++X) d[X] = MFMA32(asbf(F[X][0]), idf[0], zero16);
#pragma unroll
            for (int X = 0; X < 2; ++X) d[X] = MFMA32(asbf(F[X][1]), idf[1], d[X]);
            u32x4 b[2][2];
#pragma unroll
            for (int X = 0; X < 2; ++X)
#pragma unroll
                for (int i = 0; i < 4; ++i) { b[X][0][i] = (blk == X) ? v0[i] : 0x3F803F80u; b[X][1][i] = (blk == X) ? v1[i] : 0x3F803F80u; }
#pragma unroll
            for (int X = 0; X < 2; ++X) kv[X] = MFMA32(pack8<0>(d[X]), asbf(b[X][0]), zero16);
#pragma unroll
            for (int X = 0; X < 2; ++X) kv[X] = MFMA32(pack8<1>(d[X]), asbf(b[X][1]), kv[X]);
#pragma unroll
            for (int X = 0; X < 2; ++X) {
                const int row = X == 0 ? r32 : (r32 == 15 ? 16 : r32 - 16);
                const bool use = X == 0 ? (r32 <= 16) : (r32 >= 15);
                LAS char* dst = lds + (use ? L_RED + (((WN * 2 + X) * 4 + wm) * 17 + row) * 144 : L2_CNT + 2048 + (r32 & 7) * 144) + hh * 16;
#pragma unroll
                for (int g = 0; g < 4; ++g) {
                    f32x4 v = {kv[X][4 * g], kv[X][4 * g + 1], kv[X][4 * g + 2], kv[X][4 * g + 3]};
                    *(LAS f32x4*)(dst + g * 32) = v;
                }
            }
        }
        wait_lgkm0();
        ST(26);
        barrier();
        if constexpr (WN == 1) {
#pragma unroll
            for (int it = 0; it < 5; ++it) {
                const int o = it * 64 + lane;
                if (it < 4 || lane < 16) {
                    LAS char* src = lds + L_RED + (wm * 4 * 17 + (o >> 4)) * 144 + (o & 15) * 8;
                    f32x2 v = (*(LAS f32x2*)(src) + *(LAS f32x2*)(src + 2448)) + (*(LAS f32x2*)(src + 2 * 2448) + *(LAS f32x2*)(src + 3 * 2448));
                    u32x4 g0 = {f2u(v[0]), tag, f2u(v[1]), tag};
                    __builtin_amdgcn_raw_buffer_store_b128(g0, rs, WS_KVP + (unsigned)((mt * 32 + nt * 4 + wm) * 272 + o) * 16, 0, SC1);
                }
            }
        }
    }
    ST(27);
    f32x16 bc[NB2];
#pragma unroll
    for (int t = 0; t < NB2; ++t) bc[t] = MFMA32(fixF[2 + t], fixT, zero16);
    {
        LAS char* q0 = lds + hh * (NTB * 512) + WN * 512 + q4 * 64 + blk * 32 + p4 * 8 + chunk_base(2);
        LAS char* q1 = q0 + (chunk_base(0) - chunk_base(2));
        PIN(q0); PIN(q1);
        bf16x8 qf[4][NB2];
        auto read_q = [&](int g, int t) {
            const int c = g >> 3;
            LAS char* p = (c < 2 ? q1 + (chunk_base(c) - chunk_base(0)) : q0 + (chunk_base(c) - chunk_base(2))) + (g & 7) * (NTB * 1024) + t * 512;
            qf[g & 3][t] = cat44(tr_read(p), tr_read(p + 256));
        };
#pragma unroll
        for (int g = 0; g < 3; ++g)
#pragma unroll
            for (int t = 0; t < NB2; ++t) read_q(g, t);
        static_for<0, 32>([&](auto GI) {
            constexpr int g = GI.value;
            SCHED0();
            if constexpr (g == 8 || g == 16 || g == 24) {
                if constexpr (g == 8 && WN == 1) wait_vm<QKV ? 5 : 4>();
                wait_lgkm0(); barrier();
            }
            static_for<0, NB2>([&](auto TI) {
                constexpr int t = TI.value;
                bc[t] = MFMA32(qf[g & 3][t], xf[g], bc[t]);
                if constexpr (g + 3 < 32) read_q(g + 3, t);
                if constexpr (WN == 0) {
                    if constexpr (QKV && g >= 8 && g < 18) dma_w2(IC(g - 8));
                    if constexpr (g >= 24 && g < 30) dma_w2(IC(10 + g - 24));
                } else if constexpr (!QKV && g >= 16 && g < 24) dma_wo(IC(2 * (g - 16) + t));
                SCHED0();
            });
        });
    }
    ST(11);
    auto w2_to_regs = [&]() {
        LAS char* rdA = lds + wm * 2048 + trl;
#pragma unroll
        for (int s = 0; s < NR; ++s) {
            LAS char* p = rdA + s * 8192;
            ph[2 * s] = __builtin_bit_cast(u32x4, cat44(tr_read(p), tr_read(p + 1024)));
            ph[2 * s + 1] = __builtin_bit_cast(u32x4, cat44(tr_read(p + 512), tr_read(p + 1536)));
        }
        wait_lgkm0();
    };
    if constexpr (!QKV) {
#pragma unroll
        for (int t = 0; t < NB2; ++t) store_h(bc[t], 4 + WN + t);
        ST(12);
        if constexpr (WN == 0) {
            wait_vm<2>();
            w2_to_regs();
            static_for<2 * NB, 2 * NB + 8>([&](auto P) { dma_w2(P); });
            wait_vm<8>();
            barrier();
        } else {
            if (wm >= 2) {
                const int sl = (mt * 8 + nt) * 2 + (wm & 1);
                const unsigned vo = (unsigned)(((sl >> 3) * 136 + (sl & 7) * 17 + (lane < 34 ? (lane >> 1) : 0)) * 32 + (lane & 1) * 16);
                static_for<0, 16>([&](auto M) { ld16c<WS_KVP + M.value * 32 * 136 * 32>(ph[M.value], vo, rs); });
                wait_vm<16>();
                barrier();
                wait16(ph);
                ph[0][0] = kv_reduce_publish(a.ws, ph, sl, lane, tag) ? 1u : 0u;
            } else {
                if (wave == 4 && lane < 16) *(LAS unsigned*)(lds + L2_CNT + lane * 4) = 0u;
                wait_vm<0>();
                wait_lgkm0();
                barrier();
                if (wave == 5 && lane == 0)
                    __hip_atomic_store((GAS unsigned*)(a.ws + WS_FLAG + (mt * 16 + 8 + nt) * 64), tag, RLX_AGENT);
            }
        }
    } else {
        const bf16x8 qb0 = pack8<0>(bc[0]), qb1 = pack8<1>(bc[0]);
        if constexpr (WN == 0) {
            wait_vm<0>(); w2_to_regs();
            static_for<2 * NB, 2 * NB + 8>([&](auto P) { dma_w2(P); });
        }
        {
            f32x16 p[2];
            u32x4 F[2][2];
            p[0] = MFMA32(wfr[0], qb0, zero16);
            p[1] = MFMA32(wfr[1], qb1, zero16);
            feature_map2(p, F);
#pragma unroll
            for (int X = 0; X < 2; ++X) {
                LAS char* dst = lds + L_PHI + ((2 * WN + X) * 4 + wm) * 2048 + lane * 16;
                *(LAS u32x4*)(dst) = F[X][0];
                *(LAS u32x4*)(dst + 1024) = F[X][1];
            }
        }
        ST(12);
        wait_lgkm0();
        barrier();
        if constexpr (WN == 1) {
            if (wave == 4 && lane < 16) *(LAS unsigned*)(lds + L2_CNT + lane * 4) = 0u;
#pragma unroll
            for (int i = 0; i < 8; ++i) ph[i] = *(LAS u32x4*)(lds + L_PHI + wm * 8192 + i * 1024 + lane * 16);
            wait_lgkm0();
        }
        barrier();
    }
    ST(13);
    return tag;
}

#define HPV 6
constexpr int HP = HPV;
constexpr int w_at(int i) { return i >= NR && i + NB <= 27 ? i + NB : -1; }
constexpr int p2_pos(bool isw, int idx) {
    int pos = 0;
    for (int s = 0; s < HP; ++s) { pos += 2; if (!isw && idx == s) return pos; }
    for (int i = 1; i < 28; ++i) {
        if (i - 1 + HP <= 27) { pos += 2; if (!isw && idx == i - 1 + HP) return pos; }
        if (w_at(i - 1) >= 0) { pos += 2; if (isw && idx == w_at(i - 1)) return pos; }
    }
    return 0;
}
constexpr int p2_issued(int step) {
    int pos = 2 * HP;
    for (int i = 1; i < step; ++i) { if (i - 1 + HP <= 27) pos += 2; if (w_at(i - 1) >= 0) pos += 2; }
    return pos;
}
constexpr int p2_wait(int i) {
    int need = p2_pos(false, i);
    if (i + 1 >= NR + NB && i + 1 <= 27) { int w = p2_pos(true, i + 1); if (w > need) need = w; }
    return p2_issued(i) - need;
}
template <int SLEEP> DI void poll_words(srd_t rs, unsigned voff, unsigned tag) {
    for (unsigned spin = 0; spin <= SPIN_CAP; ++spin) {
        unsigned v;
        ld4c<0>(v, voff, rs);
        asm volatile("s_waitcnt vmcnt(0) ; PINW %0" : "+v"(v) :: "memory");
        if (__all(v == tag)) break;
        __builtin_amdgcn_s_sleep(SLEEP);
    }
}
DI void lds_wait_word(LAS char* p) {
    for (unsigned spin = 0; spin <= 16 * SPIN_CAP; ++spin) {
        if (__hip_atomic_load((LAS unsigned*)p, __ATOMIC_RELAXED, __HIP_MEMORY_SCOPE_WORKGROUP) != 0u) break;
        __builtin_amdgcn_s_sleep(1);
    }
    CFENCE();
}
DI void lds_set_word(LAS char* p) { __hip_atomic_store((LAS unsigned*)p, 1u, __ATOMIC_RELAXED, __HIP_MEMORY_SCOPE_WORKGROUP); }

struct Fin { f32x16 acc[2][2]; u32x2 xr0, xr1, br0, br1; };
template <int PART>
DI void phase2(const Args& a, LAS char* lds, const Geo& G, unsigned tag, u32x4 (&ph)[16], float vp2, Fin& F, unsigned long long* stamps) {
    const int lane = G.lane, wave = G.wave, mt = G.mt2, nt = G.nt2;
    const unsigned ldsb = (unsigned)(size_t)lds;
    const int hh = lane >> 5, r32 = lane & 31, blk = (lane >> 4) & 1, q4 = (lane & 15) >> 2, p4 = lane & 3;
    const int kq = wave & 3, hf = wave >> 2;
    const srd_t rs = mk_srd(a.ws, WS_END);
    const srd_t rw2 = mk_srd(a.w2, FF * DM * 2);
    const unsigned wvo = (unsigned)((((lane >> 2) & 7) * DM + nt * 64 + hh * 32 + (lane & 3) * 8) * 2);
    const unsigned trl = (4 * hh + q4) * 64 + blk * 32 + p4 * 8;
    const unsigned l16 = lane * 16;
    unsigned w2q[4];
#pragma unroll
    for (int q = 0; q < 4; ++q) w2q[q] = wvo + (unsigned)((kq * 448 + ((q + G.rq) & 3) * 112) * DM * 2);
    const int token = mt * 64 + (kq & 1) * 32 + r32;
    const int col0 = nt * 64 + (kq >> 1) * 32 + 16 * hf + 4 * hh;
    u32x2 &xr0 = F.xr0, &xr1 = F.xr1, &br0 = F.br0, &br1 = F.br1;
    auto final_loads = [&]() {
        const srd_t rx = mk_srd(a.x, SEQ * DM * 2), rb = mk_srd(a.b2, DM * 2);
        ld8<0>(xr0, (unsigned)(token * DM + col0) * 2, rx); ld8<16>(xr1, (unsigned)(token * DM + col0) * 2, rx);
        ld8<0>(br0, (unsigned)col0 * 2, rb); ld8<16>(br1, (unsigned)col0 * 2, rb);
    };
    f32x16 (&acc)[2][2] = F.acc;
    if constexpr (PART < 2) {
#pragma unroll
        for (int i = 0; i < 2; ++i)
#pragma unroll
            for (int j = 0; j < 2; ++j)
#pragma unroll
                for (int e = 0; e < 16; ++e) acc[i][j][e] = 0.f;
    }
    LAS char* upper = lds + L2_A + kq * 16384;
    if constexpr (PART == 0) {
        const unsigned lb = ldsb + kq * 2048;
        unsigned hq[4];
#pragma unroll
        for (int q = 0; q < 4; ++q) hq[q] = (unsigned)(((2 * mt) * 112 + 28 * kq + ((q + G.rq) & 3) * 7) * 1024) + l16;
        auto issue_w = [&](auto SI) {
            constexpr int s = SI.value;
            dma16<(s % NB) * 8192, (s % 7) * 16 * DM * 2>(w2q[s / 7], rw2, lb);
            dma16<(s % NB) * 8192 + 1024, ((s % 7) * 16 + 8) * DM * 2>(w2q[s / 7], rw2, lb);
        };
        u32x4 hb[HP][2];
        static_for<NR, NB>([&](auto SI) { issue_w(SI); });
        static_for<NB + 4, NR + NB>([&](auto SI) { issue_w(SI); });
        final_loads();
        LAS char* rdA = lds + kq * 2048 + trl;
        LAS char* rdB = rdA + 6 * 8192;
        PIN(rdB);
        bf16x8 wf[2][2];
        auto rd = [&](int s, bf16x8 (&d)[2]) {
            LAS char* p = (s % NB) < 6 ? rdA + (s % NB) * 8192 : rdB + (s % NB - 6) * 8192;
            d[0] = cat44(tr_read(p), tr_read(p + 1024));
            d[1] = cat44(tr_read(p + 512), tr_read(p + 1536));
        };
        ST(24);
        lds_wait_word(lds + L2_CNT + 16);
        ST(14);
        static_for<0, HP>([&](auto SI) {
            constexpr int s = SI.value;
            ld16c<WS_H + (s % 7) * 1024>(hb[s % HP][0], hq[s / 7], rs);
            ld16c<WS_H + (112 + s % 7) * 1024>(hb[s % HP][1], hq[s / 7], rs);
        });
        static_for<0, 28>([&](auto SI) {
            constexpr int s = SI.value;
            SCHED0();
            asm volatile("s_waitcnt vmcnt(%2) ; PINW %0 %1" : "+v"(hb[s % HP][0]), "+v"(hb[s % HP][1]) : "i"(p2_wait(s)) : "memory");
            if constexpr (s == 0) ST(15);
            static_for<0, 4>([&](auto MI) {
                constexpr int m = MI.value, i = m >> 1, j = m & 1;
                acc[i][j] = MFMA32(s < NR ? asbf(ph[s < NR ? 2 * s + i : 0]) : wf[s & 1][i], asbf(hb[s % HP][j]), acc[i][j]);
                SCHED0();
                if constexpr (m == 0 && s + 1 >= NR && s + 1 < 28) rd(s + 1, wf[(s + 1) & 1]);
                constexpr int hs = s - 1 + HP;
                constexpr int ws = s >= 1 ? w_at(s - 1) : -1, wz = ws >= 0 ? ws : 0;
                if constexpr (s >= 1 && hs <= 27 && m < 2) ld16c<WS_H + (m * 112 + hs % 7) * 1024>(hb[hs % HP][m], hq[hs / 7], rs);
                if constexpr (ws >= 0 && m >= 2) dma16<(wz % NB) * 8192 + (m - 2) * 1024, ((wz % 7) * 16 + (m - 2) * 8) * DM * 2>(w2q[wz / 7], rw2, lb);
                SCHED0();
            });
        });
        SCHED0();
        ST(16);
        wait_lgkm0();
#pragma unroll
        for (int p = 0; p < 16; ++p) {
            const f32x16& c = acc[p >> 3][(p >> 2) & 1];
            f32x4 v = {c[4 * (p & 3)], c[4 * (p & 3) + 1], c[4 * (p & 3) + 2], c[4 * (p & 3) + 3]};
            *(LAS f32x4*)(lds + (p >> 1) * 8192 + kq * 2048 + (p & 1) * 1024 + l16) = v;
        }
        asm volatile("s_waitcnt vmcnt(0) ; PINW %0 %1 %2 %3" : "+v"(xr0), "+v"(xr1), "+v"(br0), "+v"(br1) :: "memory");
    } else if constexpr (PART == 1) {
        const srd_t rwo = mk_srd(a.wo, DM * DM * 2);
        __builtin_amdgcn_s_setprio(3);
        bf16x8 wv[8][2];
        auto wo_on = [&]() {
#pragma unroll
            for (int h = 0; h < 8; ++h) {
                LAS char* p = upper + h * 2048 + trl;
                wv[h][0] = cat44(tr_read(p), tr_read(p + 1024));
                wv[h][1] = cat44(tr_read(p + 512), tr_read(p + 1536));
            }
        };
        auto poll_h = [&]() {
            poll_words<1>(rs, WS_FLAG + (unsigned)((mt >> 1) * 16 + 8 + (lane & 7)) * 64, tag);
            if (lane == 0) lds_set_word(lds + L2_CNT + 16);
        };
        if (!G.qkv) {
            if (kq == 0) poll_h();
            if (kq >= 2 && __builtin_amdgcn_readfirstlane((int)ph[0][0]) == 0) {
                const int sl = (G.mt * 8 + G.nt) * 2 + (kq & 1);
                const unsigned vo = (unsigned)(((sl >> 3) * 136 + (sl & 7) * 17 + (lane < 34 ? (lane >> 1) : 0)) * 32 + (lane & 1) * 16);
                for (unsigned spin = 0; spin <= SPIN_CAP; ++spin) {
                    static_for<0, 16>([&](auto M) { ld16c<WS_KVP + M.value * 32 * 136 * 32>(ph[M.value], vo, rs); });
                    wait16(ph);
                    if (kv_reduce_publish(a.ws, ph, sl, lane, tag)) break;
                    __builtin_amdgcn_s_sleep(2);
                }
            }
            ST(24);
        } else {
            const unsigned kvo = (unsigned)((4 * G.nt + kq) * 2176 + hh * 272 + (r32 < 16 ? r32 : 16) * 16);
            const unsigned hfo = WS_FLAG + (unsigned)((mt >> 1) * 16 + 8 + (lane & 7)) * 64;
            const unsigned wovo = wvo + kq * (128 * DM * 2);
            u32x4 raw[4]; unsigned fl;
            bool hdone = kq != 0;
            auto request = [&]() { static_for<0, 4>([&](auto I) { ld16c<WS_KVR + I.value * 544>(raw[I.value], kvo, rs); }); ld4c<0>(fl, hfo, rs); };
            request();
            asm volatile("s_waitcnt vmcnt(0) ; PINW %0 %1 %2 %3 %4" : "+v"(raw[0]), "+v"(raw[1]), "+v"(raw[2]), "+v"(raw[3]), "+v"(fl) :: "memory");
            for (unsigned spin = 0; spin <= SPIN_CAP; ++spin) {
                if (!hdone && __all(fl == tag)) { hdone = true; if (lane == 0) lds_set_word(lds + L2_CNT + 16); }
                unsigned bad = 0;
#pragma unroll
                for (int i = 0; i < 4; ++i) bad |= (raw[i][1] ^ tag) | (raw[i][3] ^ tag);
                if (__all(bad == 0u)) break;
                __builtin_amdgcn_s_sleep(1);
                request();
                asm volatile("s_waitcnt vmcnt(0) ; PINW %0 %1 %2 %3 %4" : "+v"(raw[0]), "+v"(raw[1]), "+v"(raw[2]), "+v"(raw[3]), "+v"(fl) :: "memory");
            }
            ST(24);
            f32x16 zero16;
#pragma unroll
            for (int i = 0; i < 16; ++i) zero16[i] = 0.f;
            const u32x4 k0 = {raw[0][0], raw[0][2], raw[1][0], raw[1][2]}, k1 = {raw[2][0], raw[2][2], raw[3][0], raw[3][2]};
#pragma unroll
            for (int w = 0; w < 4; ++w) {
                f32x16 nd = MFMA32(asbf(k0), asbf(ph[2 * w]), zero16);
                nd = MFMA32(asbf(k1), asbf(ph[2 * w + 1]), nd);
                const float inv = __builtin_amdgcn_rcpf(nd[8] + 1e-6f);
#pragma unroll
                for (int e = 0; e < 8; ++e) nd[e] *= inv;
                __builtin_amdgcn_raw_buffer_store_b128(pack8u<0>(nd), rs, WS_O + (unsigned)(((G.mt * 4 + w) * 32 + G.nt * 4 + kq) * 1024) + l16, 0, SC1);
            }
            wait_vm<0>();
            unsigned old = 0;
            if (lane == 0) old = __hip_atomic_fetch_add((LAS unsigned*)(lds + L2_CNT), 1u, __ATOMIC_RELAXED, __HIP_MEMORY_SCOPE_WORKGROUP);
            if (lane == 0 && old == 3u) __hip_atomic_store((GAS unsigned*)(a.ws + WS_FLAG + (G.mt * 16 + G.nt) * 64), tag, RLX_AGENT);
            ST(25);
            if (!hdone) poll_h();
            static_for<0, 16>([&](auto P) { dma16<L2_A + P.value * 1024, P.value * 8 * DM * 2>(wovo, rwo, ldsb + kq * 16384); });
        }
        final_loads();
        poll_words<4>(rs, WS_FLAG + (unsigned)((mt >> 1) * 16 + 2 * kq + (lane & 1)) * 64, tag);
        ST(14);
        const unsigned ovo = (unsigned)((2 * mt) * 32 + 8 * kq) * 1024 + l16;
        static_for<0, 16>([&](auto I) { constexpr int i = I.value; ld16c<WS_O + (i & 1) * 32768 + (i >> 1) * 1024>(ph[i], ovo, rs); });
        wo_on();
        wait16(ph);
        ST(15);
#pragma unroll
        for (int h = 0; h < 8; ++h)
#pragma unroll
            for (int i = 0; i < 2; ++i)
#pragma unroll
                for (int j = 0; j < 2; ++j) acc[i][j] = MFMA32(wv[h][i], asbf(ph[2 * h + j]), acc[i][j]);
        ST(16);
        wait_lgkm0();
#pragma unroll
        for (int p = 0; p < 16; ++p) {
            const f32x16& c = acc[p >> 3][(p >> 2) & 1];
            f32x4 v = {c[4 * (p & 3)], c[4 * (p & 3) + 1], c[4 * (p & 3) + 2], c[4 * (p & 3) + 3]};
            *(LAS f32x4*)(upper + p * 1024 + l16) = v;
        }
        asm volatile("s_waitcnt vmcnt(0) ; PINW %0 %1 %2 %3" : "+v"(xr0), "+v"(xr1), "+v"(br0), "+v"(br1) :: "memory");
    } else {
    wait_lgkm0();
    barrier();
    ST(17);
    const float r2 = __builtin_amdgcn_rcpf(vp2);
#pragma unroll
    for (int g = 0; g < 2; ++g) {
        LAS char* pa = lds + L2_A + (kq * 4 + 2 * hf + g) * 1024 + l16;
        LAS char* pf = lds + (kq * 2 + hf) * 8192 + g * 1024 + l16;
        f32x4 ss = (*(LAS f32x4*)(pa) + *(LAS f32x4*)(pa + 16384)) + (*(LAS f32x4*)(pa + 2 * 16384) + *(LAS f32x4*)(pa + 3 * 16384));
        f32x4 so = (*(LAS f32x4*)(pf) + *(LAS f32x4*)(pf + 2048)) + (*(LAS f32x4*)(pf + 4096) + *(LAS f32x4*)(pf + 6144));
        const u32x2 xr = g ? xr1 : xr0, br = g ? br1 : br0;
        float res[4];
#pragma unroll
        for (int i = 0; i < 4; ++i) {
            const float xv = u2f((i & 1) ? (xr[i >> 1] & 0xFFFF0000u) : (xr[i >> 1] << 16));
            const float bv = u2f((i & 1) ? (br[i >> 1] & 0xFFFF0000u) : (br[i >> 1] << 16));
            const float o = so[i] * r2 + bv;
            const float gt = o * __builtin_amdgcn_rcpf(1.f + __builtin_amdgcn_exp2f(-o * LOG2E));
            res[i] = xv + ss[i] * gt;
        }
        u32x2 ov = {pk2(res[0], res[1]), pk2(res[2], res[3])};
        *(GAS u32x2*)(a.out + (size_t)token * DM + col0 + 8 * g) = ov;
    }
    ST(18);
    if (G.bid == 0 && wave == 7) {
        for (unsigned spin = 0; spin <= SPIN_CAP; ++spin) {
            unsigned f0, f1, f2, f3;
            ld4c<WS_FLAG>(f0, (unsigned)lane * 64, rs); ld4c<WS_FLAG + 4096>(f1, (unsigned)lane * 64, rs);
            ld4c<WS_FLAG + 8192>(f2, (unsigned)lane * 64, rs); ld4c<WS_FLAG + 12288>(f3, (unsigned)lane * 64, rs);
            asm volatile("s_waitcnt vmcnt(0) ; PINW %0 %1 %2 %3" : "+v"(f0), "+v"(f1), "+v"(f2), "+v"(f3) :: "memory");
            if (__all(f0 == tag && f1 == tag && f2 == tag && f3 == tag)) break;
            __builtin_amdgcn_s_sleep(1);
        }
        if (lane == 0) __hip_atomic_store((GAS unsigned*)(a.ws + WS_CTRL), tag, RLX_AGENT);
    }
    }
}

__global__ void __launch_bounds__(512, 2) fused_kernel(Args a) {
    extern __shared__ __attribute__((aligned(16))) char smem[];
    LAS char* lds = (LAS char*)smem;
    unsigned long long stamps[28];
    ST(0);
    Geo G;
    G.bid = blockIdx.x;
    G.wave = __builtin_amdgcn_readfirstlane(threadIdx.x >> 6);
    G.lane = threadIdx.x & 63;
    const int xcd = G.bid & 7, loc = G.bid >> 3;
    const int sel = loc >> 2;
    G.mt = (xcd >> 1) * 4 + (loc & 3);
    G.nt = (xcd & 1) * 4 + (sel & 3);
    G.i16 = (xcd & 1) * 8 + sel;
    G.mt2 = G.mt * 2 + (xcd & 1);
    G.nt2 = sel;
    G.qkv = sel < 4;
#define RQ (sel & 3)
    G.rq = RQ;
    unsigned wvo0[4], xvo0[4], epoch;
    u32x4 rowv[2] = {u32x4{0u, 0u, 0u, 0u}, u32x4{0u, 0u, 0u, 0u}};
    {
        const int lane = G.lane, wm = G.wave & 3, hh = lane >> 5;
        const unsigned ldsb = (unsigned)(size_t)lds;
        const int LDW = G.qkv ? 1536 : 1792;
        const srd_t rx = mk_srd(a.x, SEQ * DM * 2);
        const srd_t rw = mk_srd(G.qkv ? a.wqkv : a.w1, DM * LDW * 2);
        ld4c<WS_CTRL>(epoch, 0u, mk_srd(a.ws, WS_END));
        if (G.wave >= 4) {
            const unsigned ro = (unsigned)((G.mt * 128 + G.i16 * 8 + 2 * wm) * DM * 2 + lane * 16);
            ld16(rowv[0], ro, rx); ld16(rowv[1], ro + 1024, rx);
        } else {
            const int u1 = G.mt + 16 < (G.qkv ? 24 : 28) ? G.mt + 16 : G.mt;
            const unsigned rowpart = (unsigned)(((G.mt & 3) * 128 + 32 * wm + (lane >> 2)) * LDW + (lane & 3) * 8) * 2;
#pragma unroll
            for (int u = 0; u < 2; ++u) {
                const int c = (u ? u1 : G.mt) >> 2;
                const int colb = G.qkv ? ((((c >> 1) + 1) % 3) * 512 + 64 * G.nt + 32 * (c & 1)) : (224 * G.nt + 32 * c);
                dma16r(rowpart + colb * 2, rw, ldsb + L_UNIT + u * 8192 + wm * 2048);
                dma16r(rowpart + colb * 2 + 16 * LDW * 2, rw, ldsb + L_UNIT + u * 8192 + wm * 2048 + 1024);
            }
        }
#pragma unroll
        for (int i = 0; i < 4; ++i) {
            const int row = 8 * (4 * wm + i) + (lane >> 3);
            xvo0[i] = (unsigned)(((G.mt * 128 + row) * DM + ((lane & 7) ^ ((row >> 1) & 7)) * 8) * 2);
            const int k8 = 2 * wm + (i >> 1), c = 2 * (i & 1) + hh;
            const int colb = G.qkv ? (((c >> 1) + 1) * 512 + 64 * G.nt + 32 * (c & 1)) : (224 * G.nt + 32 * c);
            wvo0[i] = (unsigned)(((k8 * 8 + ((lane >> 2) & 7)) * LDW + colb + (lane & 3) * 8) * 2);
        }
        if (G.wave >= 4) {
            static_for<0, 4>([&](auto I) { dma16<SLOT + XOFF + I.value * 1024, 0>(xvo0[I.value], rx, ldsb + wm * 4096); });
        } else {
            static_for<0, 4>([&](auto I) { dma16<SLOT + I.value * 1024, 0>(wvo0[I.value], rw, ldsb + wm * 4096); });
        }
        ST(21);
        asm volatile("s_waitcnt vmcnt(4) ; PINW %0 %1 %2" : "+v"(epoch), "+v"(rowv[0]), "+v"(rowv[1]) :: "memory");
    }
    unsigned tag;
    float vp2 = 1.f;
    Fin F;
    if (G.wave < 4) {
        u32x4 ph[16];
        if (sel < 4) tag = phase1<true, 0>(a, lds, G, wvo0, xvo0, epoch, rowv, ph, vp2, stamps);
        else tag = phase1<false, 0>(a, lds, G, wvo0, xvo0, epoch, rowv, ph, vp2, stamps);
        phase2<0>(a, lds, G, tag, ph, vp2, F, stamps);
    } else {
        u32x4 ph[16];
#pragma unroll
        for (int i = 0; i < 16; ++i) ph[i] = u32x4{0u, 0u, 0u, 0u};
        if (sel < 4) tag = phase1<true, 1>(a, lds, G, wvo0, xvo0, epoch, rowv, ph, vp2, stamps);
        else tag = phase1<false, 1>(a, lds, G, wvo0, xvo0, epoch, rowv, ph, vp2, stamps);
        phase2<1>(a, lds, G, tag, ph, vp2, F, stamps);
    }
    {
        u32x4 ph[16];
        phase2<2>(a, lds, G, tag, ph, vp2, F, stamps);
    }
}

extern "C" void kernel_launch(void* const* d_in, const int* in_sizes, int n_in, void* d_out, int out_size, void* d_ws,
                              size_t ws_size, hipStream_t stream) {
    Args a;
    a.x = (const __bf16*)d_in[0]; a.wqkv = (const __bf16*)d_in[1]; a.wphi = (const __bf16*)d_in[2]; a.wo = (const __bf16*)d_in[3];
    a.w1 = (const __bf16*)d_in[5]; a.b1 = (const __bf16*)d_in[6]; a.w2 = (const __bf16*)d_in[7]; a.b2 = (const __bf16*)d_in[8];
    a.ws = (char*)d_ws; a.out = (__bf16*)d_out;
    (void)hipFuncSetAttribute((const void*)fused_kernel, hipFuncAttributeMaxDynamicSharedMemorySize, L_TOTAL);
    fused_kernel<<<256, 512, L_TOTAL, stream>>>(a);
}
```

```cpp
#include <hip/hip_runtime.h>
#include <hip/hip_bf16.h>
#include <stdint.h>
#include <type_traits>

typedef __attribute__((ext_vector_type(8))) __bf16 bf16x8;
typedef __attribute__((ext_vector_type(4))) __bf16 bf16x4;
typedef __attribute__((ext_vector_type(2))) __bf16 bf16x2;
typedef __attribute__((ext_vector_type(16))) float f32x16;
typedef __attribute__((ext_vector_type(4))) float f32x4;
typedef __attribute__((ext_vector_type(2))) float f32x2;
typedef __attribute__((ext_vector_type(4))) unsigned u32x4;
typedef __attribute__((ext_vector_type(2))) unsigned u32x2;
typedef __amdgpu_buffer_rsrc_t srd_t;
#define LAS __attribute__((address_space(3)))
#define GAS __attribute__((address_space(1)))
#define DI __device__ __forceinline__
#define MFMA32(a, b, c) __builtin_amdgcn_mfma_f32_32x32x16_bf16((a), (b), (c), 0, 0, 0)
#define SCHED0() __builtin_amdgcn_sched_barrier(0)
#define CFENCE() asm volatile("" ::: "memory")
#define RLX_AGENT __ATOMIC_RELAXED, __HIP_MEMORY_SCOPE_AGENT

constexpr int SEQ = 2048, DM = 512, NH = 32, FF = 1792;
constexpr float LOG2E = 1.4426950408889634f;
constexpr int SC1 = 16;
constexpr unsigned SPIN_CAP = 1u << 16;

constexpr unsigned WS_CTRL = 0;
constexpr unsigned WS_FLAG = 256;
constexpr unsigned WS_RST  = WS_FLAG + 256 * 64;
constexpr unsigned WS_CSG  = WS_RST + 2048 * 16;
constexpr unsigned WS_KVR  = WS_CSG + 3328 * 32;
constexpr unsigned WS_KVP  = WS_KVR + 32 * 2176;
constexpr unsigned WS_H    = (WS_KVP + 16u * 32 * 136 * 32 + 4095) & ~4095u;
constexpr unsigned WS_O    = WS_H + 64u * 112 * 1024;
constexpr unsigned WS_END  = WS_O + 64u * 32 * 1024;

constexpr int SLOT = 32768, XOFF = 16384;
constexpr int chunk_base(bool qkv, int c) { return qkv ? (c == 0 ? 131072 : (c == 3 ? 81920 : 98304 + (c - 1) * 16384)) : (c == 0 ? 98304 : (c == 1 ? 122880 : (c == 2 ? 40960 : 65536))); }
constexpr int L_UNIT = 131072;
constexpr int L_CSP = 147456;
constexpr int L_RR = L_CSP + 1024;
constexpr int L_AUX = L_RR + 1024;
constexpr int L_DUMMY = L_AUX + 2048;
constexpr int L_FIX = L_DUMMY + 256;
constexpr int L_RED = 40960;
constexpr int L_PHI = 98304;
constexpr int NR = 8;
constexpr int NB = 11;
constexpr int L2_CNT = NB * 8192;
constexpr int L2_A = 98304;
constexpr int L_TOTAL = 163840;
static_assert(L_FIX + 8192 <= L_TOTAL, "lds");

template <int LOFF, int SOFF> DI void dma16(unsigned voff, srd_t srd, unsigned lbase) {
    unsigned t;
    lbase = (unsigned)__builtin_amdgcn_readfirstlane((int)lbase);
    asm volatile("s_add_i32 m0, %3, %4\n\ts_mov_b32 %0, %5\n\tbuffer_load_dwordx4 %1, %2, %0 offen lds"
                 : "=&s"(t) : "v"(voff), "s"(srd), "s"(lbase), "i"(LOFF), "i"(SOFF) : "memory", "scc");
}
template <int LOFF> DI void touch_ptr(const void* p, unsigned lbase) {
    asm volatile("s_add_i32 m0, %1, %2\n\ts_nop 0\n\tglobal_load_lds_dword %0, off" :: "v"(p), "s"(lbase), "i"(LOFF) : "memory", "scc");
}
template <int OFF> DI void ld16c(u32x4& d, unsigned voff, srd_t srd) {
    asm volatile("buffer_load_dwordx4 %0, %1, %2, %3 offen offset:%4 sc1" : "=v"(d) : "v"(voff), "s"(srd), "s"(OFF & ~4095), "i"(OFF & 4095) : "memory");
}
template <int OFF> DI void ld4c(unsigned& d, unsigned voff, srd_t srd) {
    asm volatile("buffer_load_dword %0, %1, %2, %3 offen offset:%4 sc1" : "=v"(d) : "v"(voff), "s"(srd), "s"(OFF & ~4095), "i"(OFF & 4095) : "memory");
}
template <int OFF> DI void ld8c(u32x2& d, unsigned voff, srd_t srd) {
    asm volatile("buffer_load_dwordx2 %0, %1, %2, %3 offen offset:%4 sc1" : "=v"(d) : "v"(voff), "s"(srd), "s"(OFF & ~4095), "i"(OFF & 4095) : "memory");
}
DI void dma16r(unsigned voff, srd_t srd, unsigned lds_dst) {
    asm volatile("s_mov_b32 m0, %2\n\ts_nop 0\n\tbuffer_load_dwordx4 %0, %1, 0 offen lds" :: "v"(voff), "s"(srd), "s"(lds_dst) : "memory");
}
DI void ld16(u32x4& d, unsigned voff, srd_t srd) { asm volatile("buffer_load_dwordx4 %0, %1, %2, 0 offen" : "=v"(d) : "v"(voff), "s"(srd) : "memory"); }
template <int OFF> DI void ld8(u32x2& d, unsigned voff, srd_t srd) { asm volatile("buffer_load_dwordx2 %0, %1, %2, 0 offen offset:%3" : "=v"(d) : "v"(voff), "s"(srd), "i"(OFF) : "memory"); }
DI float fmx(float a, float b) { return __builtin_elementwise_maximum(a, b); }
DI void wait_lgkm0() { __builtin_amdgcn_s_waitcnt(0xC07F); CFENCE(); }
template <int N> DI void wait_vm() { asm volatile("s_waitcnt vmcnt(%0)" ::"i"(N) : "memory"); }
DI void barrier() { __builtin_amdgcn_s_barrier(); CFENCE(); }
#define PIN(x) asm volatile("" : "+v"(x))
DI srd_t mk_srd(const void* p, unsigned bytes) { return __builtin_amdgcn_make_buffer_rsrc((void*)p, 0, (int)bytes, 0x00020000); }

DI bf16x4 tr_read(LAS char* p) { return __builtin_amdgcn_ds_read_tr16_b64_v4bf16((LAS bf16x4*)p); }
DI bf16x8 cat44(bf16x4 a, bf16x4 b) { return __builtin_shufflevector(a, b, 0, 1, 2, 3, 4, 5, 6, 7); }
DI float dot2(unsigned a, unsigned b, float c) {
    return __builtin_amdgcn_fdot2_f32_bf16(__builtin_bit_cast(bf16x2, a), __builtin_bit_cast(bf16x2, b), c, false);
}
DI unsigned pk2(float a, float b) { f32x2 v = {a, b}; return __builtin_bit_cast(unsigned, __builtin_convertvector(v, bf16x2)); }
template <int S> DI u32x4 pack8u(const f32x16& x) {
    u32x4 p = {pk2(x[8 * S], x[8 * S + 1]), pk2(x[8 * S + 2], x[8 * S + 3]), pk2(x[8 * S + 4], x[8 * S + 5]), pk2(x[8 * S + 6], x[8 * S + 7])};
    return p;
}
template <int S> DI bf16x8 pack8(const f32x16& x) { return __builtin_bit_cast(bf16x8, pack8u<S>(x)); }
DI bf16x8 asbf(u32x4 u) { return __builtin_bit_cast(bf16x8, u); }
DI float u2f(unsigned u) { return __builtin_bit_cast(float, u); }
DI unsigned f2u(float f) { return __builtin_bit_cast(unsigned, f); }
DI unsigned hi2(unsigned lo, unsigned hi) { return __builtin_amdgcn_perm(hi, lo, 0x07060302u); }
DI void split3(float v, unsigned& s0, unsigned& s1, unsigned& s2) {
    s0 = f2u(v) & 0xFFFF0000u;
    float r = v - u2f(s0);
    s1 = f2u(r) & 0xFFFF0000u;
    s2 = f2u(r - u2f(s1));
}
DI float half_sum(float v) { auto s = __builtin_amdgcn_permlane32_swap(f2u(v), f2u(v), false, false); return u2f(s[0]) + u2f(s[1]); }
DI float wave_sum(float v) {
#define DPPADD(ctrl, rm) v += u2f((unsigned)__builtin_amdgcn_update_dpp(0, (int)f2u(v), ctrl, rm, 0xf, true))
    DPPADD(0x111, 0xf); DPPADD(0x112, 0xf); DPPADD(0x114, 0xf); DPPADD(0x118, 0xf); DPPADD(0x142, 0xa); DPPADD(0x143, 0xc);
#undef DPPADD
    return u2f((unsigned)__builtin_amdgcn_readlane((int)f2u(v), 63));
}
template <int C, int N, class F> DI void static_for(F&& f) { if constexpr (C < N) { f(std::integral_constant<int, C>{}); static_for<C + 1, N>(f); } }

struct Args {
    const __bf16* x; const __bf16* wqkv; const __bf16* wphi; const __bf16* wo;
    const __bf16* w1; const __bf16* b1; const __bf16* w2; const __bf16* b2;
    char* ws; __bf16* out;
};
struct Geo { int bid, wave, lane, mt, nt, i16, mt2, nt2, qkv, rq; };
#define IC(v) std::integral_constant<int, (v)>{}

template <int N> DI void gather_wait(u32x4& gst, u32x2& gv2, u32x4& gcs) {
    asm volatile("s_waitcnt vmcnt(%3) ; PINW %0 %1 %2" : "+v"(gst), "+v"(gv2), "+v"(gcs) : "i"(N) : "memory");
}
DI void wait16(u32x4 (&v)[16]) {
    asm volatile("s_waitcnt vmcnt(0) ; PINW %0 %1 %2 %3 %4 %5 %6 %7 %8 %9 %10 %11 %12 %13 %14 %15"
                 : "+v"(v[0]), "+v"(v[1]), "+v"(v[2]), "+v"(v[3]), "+v"(v[4]), "+v"(v[5]), "+v"(v[6]), "+v"(v[7]),
                   "+v"(v[8]), "+v"(v[9]), "+v"(v[10]), "+v"(v[11]), "+v"(v[12]), "+v"(v[13]), "+v"(v[14]), "+v"(v[15]) :: "memory");
}

DI bool kv_reduce_publish(char* ws, u32x4 (&v)[16], int sl, int lane, unsigned tag) {
    unsigned bad = 0;
#pragma unroll
    for (int m = 0; m < 16; ++m) bad |= (v[m][1] ^ tag) | (v[m][3] ^ tag);
    if (!__all(bad == 0u)) return false;
    float s0[16], s1[16];
#pragma unroll
    for (int m = 0; m < 16; ++m) { s0[m] = u2f(v[m][0]); s1[m] = u2f(v[m][2]); }
#pragma unroll
    for (int st = 8; st >= 1; st >>= 1)
#pragma unroll
        for (int m = 0; m < st; ++m) { s0[m] += s0[m + st]; s1[m] += s1[m + st]; }
    const int g = (sl & 7) * 17 + (lane < 34 ? (lane >> 1) : 0), row = g >> 3, fq = g & 7;
    const unsigned off = WS_KVR + (sl >> 3) * 2176 + fq * 272 + row * 16 + (lane & 1) * 8;
    if (lane < 34)
        __hip_atomic_store((GAS unsigned long long*)(ws + off), (unsigned long long)pk2(s0[0], s1[0]) | ((unsigned long long)tag << 32), RLX_AGENT);
    return true;
}
template <bool QKV, int WN>
DI unsigned phase1(const Args& a, LAS char* lds, const Geo& G, const unsigned (&wvo0)[4], const unsigned (&xvo0)[4],
                   unsigned epoch, const u32x4 (&rowv)[2], u32x4 (&ph)[16], float& vp2) {
    constexpr int NTB = QKV ? 2 : 3;
    constexpr int NB2 = QKV ? 1 : (WN == 0 ? 1 : 2);
    constexpr int NG = 2 + NB2;
    constexpr int NU = QKV ? 24 : 28;
    constexpr int LDW = QKV ? 1536 : 1792;
    constexpr int CP = NTB * 2;
    const int lane = G.lane, wave = G.wave, wm = wave & 3, mt = G.mt, nt = G.nt;
    const unsigned ldsb = (unsigned)(size_t)lds;
    const int hh = lane >> 5, r32 = lane & 31, blk = (lane >> 4) & 1, q4 = (lane & 15) >> 2, p4 = lane & 3;
    const srd_t rs = mk_srd(a.ws, WS_END);
    const srd_t rx = mk_srd(a.x, SEQ * DM * 2);
    const srd_t rw = mk_srd(QKV ? a.wqkv : a.w1, DM * LDW * 2);
    const srd_t rw2 = mk_srd(a.w2, FF * DM * 2);
    const srd_t rwo = mk_srd(a.wo, DM * DM * 2);
    auto colb_of = [&](int c) { return QKV ? ((((c >> 1) + 1) % 3) * 512 + 64 * nt + 32 * (c & 1)) : (224 * nt + 32 * c); };
    auto tile_of = [&](int t) { return t < 2 ? 2 * t + WN : (QKV ? 4 + WN : 4 + WN + (t - 2)); };
    auto dma_pair = [&](auto JI, auto II) {
        constexpr int j = JI.value, i = II.value, sb = ((j + 1) % 3) * SLOT;
        if constexpr (i < 4) dma16<sb + i * 1024, j * 64 * LDW * 2>(wvo0[i], rw, ldsb + wm * 4096);
        else dma16<sb + XOFF + (i - 4) * 1024, j * 128>(xvo0[i - 4], rx, ldsb + wm * 4096);
    };
    const unsigned w2vo = (unsigned)((((lane >> 2) & 7) * DM + G.nt2 * 64 + hh * 32 + (lane & 3) * 8) * 2);
    unsigned w2q[3];
#pragma unroll
    for (int q = 0; q < 3; ++q) w2q[q] = w2vo + (unsigned)((wm * 448 + ((q + G.rq) & 3) * 112) * DM * 2);
    auto dma_w2 = [&](auto PI) {
        constexpr int r = PI.value >> 1, t = PI.value & 1;
        dma16<(r % NB) * 8192 + t * 1024, ((r % 7) * 16 + t * 8) * DM * 2>(w2q[r / 7], rw2, ldsb + wm * 2048);
    };
    const unsigned wovo = w2vo + wm * (128 * DM * 2);
    auto dma_wo = [&](auto PI) { dma16<L2_A + PI.value * 1024, PI.value * 8 * DM * 2>(wovo, rwo, ldsb + wm * 16384); };
    unsigned cvo[WN == 1 ? NTB : 1];
    if constexpr (WN == 1) {
#pragma unroll
        for (int i = 0; i < NTB; ++i) {
            const int st = 2 * i + hh, k8 = 4 * wm + (st >= NTB ? 1 : 0), c = st >= NTB ? st - NTB : st;
            cvo[i] = (unsigned)(((k8 * 8 + ((lane >> 2) & 7)) * LDW + colb_of(4 + c) + (lane & 3) * 8) * 2);
        }
    }
    auto dma_chunk = [&](auto CI, auto II) {
        constexpr int c = CI.value, i = II.value;
        dma16<chunk_base(QKV, c) + i * 1024, (c * 128 + (i / NTB) * 16) * LDW * 2>(cvo[i % NTB], rw, ldsb + wm * (CP * 1024));
    };
    const int srow = mt * 128 + G.i16 * 8 + 2 * wm;
    const int u1 = mt + 16 < NU ? mt + 16 : mt;
    const unsigned tag = (unsigned)__builtin_amdgcn_readfirstlane((int)epoch) + 1u;
    const unsigned trl = (4 * hh + q4) * 64 + blk * 32 + p4 * 8;
    if constexpr (WN == 0) {
        LAS char* p = lds + L_UNIT + wm * 2048 + trl;
        const unsigned one = 0x3F803F80u;
        float su[2];
#pragma unroll
        for (int u = 0; u < 2; ++u) {
            float s = 0.f;
#pragma unroll
            for (int i = 0; i < 4; ++i) { u32x2 w = __builtin_bit_cast(u32x2, tr_read(p + u * 8192 + i * 512)); s = dot2(w[1], one, dot2(w[0], one, s)); }
            su[u] = s;
        }
        auto sw = __builtin_amdgcn_permlane32_swap(f2u(su[0]), f2u(su[1]), false, false);
        *(LAS float*)(lds + L_CSP + ((hh * 4 + wm) * 32 + r32) * 4) = u2f(sw[0]) + u2f(sw[1]);
        static_for<0, 8>([&](auto I) { dma_pair(IC(1), I); });
    } else {
        float mean[2], vv[2];
#pragma unroll
        for (int r = 0; r < 2; ++r) {
            float e[8];
#pragma unroll
            for (int i = 0; i < 4; ++i) { e[2 * i] = u2f(rowv[r][i] << 16); e[2 * i + 1] = u2f(rowv[r][i] & 0xFFFF0000u); }
            mean[r] = wave_sum(((e[0] + e[1]) + (e[2] + e[3])) + ((e[4] + e[5]) + (e[6] + e[7]))) * (1.f / DM);
            float q = 0.f;
#pragma unroll
            for (int i = 0; i < 8; ++i) { float d = e[i] - mean[r]; q = __builtin_fmaf(d, d, q); }
            vv[r] = wave_sum(q) * (1.f / DM) + 1e-5f;
        }
        u32x4 g = {f2u(lane ? mean[1] : mean[0]), tag, f2u(lane ? vv[1] : vv[0]), tag};
        if (lane < 2) __builtin_amdgcn_raw_buffer_store_b128(g, rs, WS_RST + (srow + lane) * 16, 0, SC1);
    }

    LAS char* wb[2]; LAS char* xb[2][4];
#pragma unroll
    for (int s = 0; s < 2; ++s) {
        wb[s] = lds + s * 65536 + hh * 2048 + WN * 512 + q4 * 64 + blk * 32 + p4 * 8;
        PIN(wb[s]);
        const int row = 32 * wm + r32;
#pragma unroll
        for (int k = 0; k < 4; ++k) { xb[s][k] = lds + s * 65536 + XOFF + row * 128 + (((2 * k + hh) ^ ((row >> 1) & 7)) * 16); PIN(xb[s][k]); }
    }
    const int csbase = QKV ? nt * 192 : 1536 + nt * 224;

    f32x16 acc[2];
#pragma unroll
    for (int t = 0; t < 2; ++t)
#pragma unroll
        for (int i = 0; i < 16; ++i) acc[t][i] = 0.f;
    bf16x8 xf[32], wf[3][2];
    auto read_x = [&](int g) { const int sl = ((g >> 2) + 1) % 3; xf[g] = *(LAS bf16x8*)(xb[sl >> 1][g & 3] + (sl & 1) * SLOT); };
    auto read_w = [&](int g, int t) {
        const int sl = ((g >> 2) + 1) % 3;
        LAS char* p = wb[sl >> 1] + (sl & 1) * SLOT + (g & 3) * 4096 + t * 1024;
        wf[g % 3][t] = cat44(tr_read(p), tr_read(p + 256));
    };
    u32x4 gst, gcs; u32x2 gv2;
    const int mytile = wm < NG ? (wm < 2 ? 2 * wm + WN : (QKV ? 4 + WN : 2 + WN + wm)) : WN;
    const unsigned gvo_st = (unsigned)(mt * 128 + 32 * wm + r32) * 16;
    const unsigned gvo_v2 = (unsigned)(G.mt2 * 64 + (wave & 1) * 32 + r32) * 16;
    const unsigned gvo_cs = (unsigned)(csbase + mytile * 32 + r32) * 32 + hh * 16;
    auto gather = [&]() {
        ld16c<WS_RST>(gst, gvo_st, rs);
        ld8c<WS_RST + 8>(gv2, gvo_v2, rs);
        ld16c<WS_CSG>(gcs, gvo_cs, rs);
    };
    float vpe = 1.f, r_t = 1.f;
    bf16x8 fixT, fixF[NG];
    auto gather_check_and_fix = [&]() {
        for (unsigned spin = 0;; ++spin) {
            const unsigned bad = (gst[1] ^ tag) | (gst[3] ^ tag) | (gv2[1] ^ tag) | (gcs[1] ^ tag) | (gcs[3] ^ tag);
            if (__all(bad == 0u) || spin > SPIN_CAP) break;
            __builtin_amdgcn_s_sleep(1);
            gather();
            gather_wait<0>(gst, gv2, gcs);
        }
        vpe = u2f(gst[2]);
        vp2 = u2f(gv2[0]);
        r_t = __builtin_amdgcn_rsqf(vpe);
        unsigned s0, s1, s2;
        split3(hh ? vpe * r_t : -u2f(gst[0]), s0, s1, s2);
        u32x4 u;
        u[0] = hi2(s0, s1);
        u[1] = hh ? (s2 >> 16) : u[0];
        u[2] = hh ? 0u : hi2(s2, s0);
        u[3] = 0u;
        fixT = asbf(u);
        if (QKV) *(LAS float*)(lds + L_RR + (wave * 32 + r32) * 4) = r_t;
        unsigned c0, c1, c2;
        split3(half_sum(u2f(gcs[0]) + u2f(gcs[2])), c0, c1, c2);
        unsigned bb = 0;
        if (!QKV) bb = *(LAS unsigned short*)(lds + L_AUX + (mytile * 32 + r32) * 2);
        u32x4 w;
        w[0] = hh ? (bb | (bb << 16)) : hi2(c0, c0);
        w[1] = hh ? bb : hi2(c1, c1);
        w[2] = hh ? 0u : hi2(c0, c2);
        w[3] = 0u;
        *(LAS u32x4*)(lds + L_FIX + wave * 1024 + lane * 16) = w;
    };

    wait_vm<WN == 0 ? 8 : 1>();
    wait_lgkm0();
    barrier();
#pragma unroll
    for (int g = 0; g < 2; ++g) { read_x(g); read_w(g, 0); read_w(g, 1); }
    if constexpr (WN == 1) {
        const int idx = 16 * wm + (lane & 15);
        LAS float* p = (LAS float*)(lds + L_CSP) + (idx >> 5) * 128 + (idx & 31);
        u32x2 gq = {f2u((p[0] + p[32]) + (p[64] + p[96])), tag};
        const int u = wm >= 2 ? u1 : mt;
        if (lane < 16) __builtin_amdgcn_raw_buffer_store_b64(gq, rs, WS_CSG + (csbase + (u >> 2) * 32 + (idx & 31)) * 32 + (u & 3) * 8, 0, SC1);
        if (wave == 4) {
            if (QKV) {
                const srd_t rp = mk_srd(a.wphi, NH * 256 * 2);
                dma16<L_AUX, 0>((unsigned)(nt * 2048 + lane * 16), rp, ldsb);
                dma16<L_AUX + 1024, 1024>((unsigned)(nt * 2048 + lane * 16), rp, ldsb);
            } else {
                const srd_t rb = mk_srd(a.b1, FF * 2);
                dma16<L_AUX, 0>((unsigned)(nt * 448 + lane * 16), rb, ldsb);
            }
        }
    }
    static_for<0, 32>([&](auto GI) {
        constexpr int g = GI.value, j = g >> 2;
        SCHED0();
        if constexpr ((g & 3) == 2) {
            constexpr int JG = WN == 0 ? 4 : 5;
            if constexpr (j == JG + 1) { PIN(acc[0]); PIN(acc[1]); }
            if constexpr (WN == 0) {
                constexpr int n = j <= 6 ? 8 : 63;
                if constexpr (j == JG + 1) gather_wait<n>(gst, gv2, gcs);
                else wait_vm<n>();
            } else if constexpr (j == 4) wait_vm<0>();
            else if constexpr (j == JG + 1) gather_wait<CP>(gst, gv2, gcs);
            else if constexpr (j == 7) wait_vm<CP>();
            wait_lgkm0();
            barrier();
            if constexpr (WN == 1 && j == 1) {
                const int gw = G.bid * 4 + wm;
                const char* p = lane < 14 ? (const char*)a.w2 + (size_t)(gw * 14 + lane) * 128 : (const char*)a.wo + (size_t)(gw * 4 + (lane & 3)) * 128;
                touch_ptr<L_DUMMY>(p, ldsb);
            }
            if constexpr (j == JG) gather();
            if constexpr (WN == 1 && j == 7) static_for<0, CP>([&](auto I) { dma_chunk(IC(3), I); });
            if constexpr (j == JG + 1) gather_check_and_fix();
            if constexpr (j == 7) {
#pragma unroll
                for (int t = 0; t < NG; ++t) fixF[t] = *(LAS bf16x8*)(lds + L_FIX + (WN * 4 + t) * 1024 + lane * 16);
            }
            SCHED0();
        }
        constexpr int jb = (g - 2) >> 2, q = (g - 2) & 3;
        static_for<0, 2>([&](auto TI) {
            constexpr int t = TI.value;
            if (QKV && t == 1) acc[t] = MFMA32(xf[g], wf[g % 3][t], acc[t]);
            else acc[t] = MFMA32(wf[g % 3][t], xf[g], acc[t]);
            if constexpr (g + 2 < 32) {
                if (t == 0) read_x(g + 2);
                read_w(g + 2, t);
            }
            constexpr int i = q * 2 + t;
            if constexpr (WN == 0) {
                if constexpr (g < 2) { dma_pair(IC(2), IC(4 * g + 2 * t)); dma_pair(IC(2), IC(4 * g + 2 * t + 1)); }
                else if constexpr (jb <= 4) dma_pair(IC(jb + 3), IC(i));
                else if constexpr (jb == 5) dma_w2(IC(i));
                else if constexpr (jb == 6 && i < 2) dma_w2(IC(8 + i));
            } else if constexpr (g >= 2 && jb >= 4 && jb <= 6 && i < CP) dma_chunk(IC(jb - 4), IC(i));
            SCHED0();
        });
    });
    const int tt = mt * 4 + wm;
    f32x16 zero16;
#pragma unroll
    for (int i = 0; i < 16; ++i) zero16[i] = 0.f;
    auto store_h = [&](f32x16& c, int tile) {
#pragma unroll
        for (int i = 0; i < 16; ++i) { float v = fmx(c[i], 0.f); c[i] = v * v; }
        const unsigned off = WS_H + (unsigned)((tt * 112 + nt * 14 + tile * 2) * 64 + lane) * 16;
        __builtin_amdgcn_raw_buffer_store_b128(pack8u<0>(c), rs, off, 0, SC1);
        __builtin_amdgcn_raw_buffer_store_b128(pack8u<1>(c), rs, off + 1024, 0, SC1);
    };
    bf16x8 wfr[2];
    const float kscale = r_t * LOG2E;
    auto feature_map2 = [&](f32x16 (&p)[2], u32x4 (&F)[2][2]) {
        float m[2][2], sm[2][2];
#pragma unroll
        for (int X = 0; X < 2; ++X)
#pragma unroll
            for (int h = 0; h < 2; ++h) {
                const int o = 8 * h;
                m[X][h] = fmx(fmx(fmx(fmx(p[X][o], p[X][o + 1]), p[X][o + 2]), fmx(fmx(p[X][o + 3], p[X][o + 4]), p[X][o + 5])), fmx(p[X][o + 6], p[X][o + 7]));
            }
#pragma unroll
        for (int X = 0; X < 2; ++X) {
            auto s = __builtin_amdgcn_permlane32_swap(f2u(m[X][0]), f2u(m[X][1]), false, false);
            float c = -kscale * fmx(u2f(s[0]), u2f(s[1]));
            auto s2 = __builtin_amdgcn_permlane32_swap(f2u(c), f2u(c), false, false);
            m[X][0] = u2f(s2[0]); m[X][1] = u2f(s2[1]);
        }
#pragma unroll
        for (int i = 0; i < 16; ++i)
#pragma unroll
            for (int X = 0; X < 2; ++X) p[X][i] = __builtin_amdgcn_exp2f(__builtin_fmaf(p[X][i], kscale, m[X][i >> 3]));
#pragma unroll
        for (int X = 0; X < 2; ++X)
#pragma unroll
            for (int h = 0; h < 2; ++h) {
                const int o = 8 * h;
                sm[X][h] = ((p[X][o] + p[X][o + 1]) + (p[X][o + 2] + p[X][o + 3])) + ((p[X][o + 4] + p[X][o + 5]) + (p[X][o + 6] + p[X][o + 7]));
            }
#pragma unroll
        for (int X = 0; X < 2; ++X) {
            auto s = __builtin_amdgcn_permlane32_swap(f2u(sm[X][0]), f2u(sm[X][1]), false, false);
            float c = __builtin_amdgcn_rcpf(u2f(s[0]) + u2f(s[1]));
            auto s2 = __builtin_amdgcn_permlane32_swap(f2u(c), f2u(c), false, false);
            sm[X][0] = u2f(s2[0]); sm[X][1] = u2f(s2[1]);
        }
#pragma unroll
        for (int X = 0; X < 2; ++X) {
#pragma unroll
            for (int i = 0; i < 16; ++i) p[X][i] *= sm[X][i >> 3];
            F[X][0] = pack8u<0>(p[X]); F[X][1] = pack8u<1>(p[X]);
        }
    };
    if constexpr (!QKV) {
        acc[0] = MFMA32(fixF[0], fixT, acc[0]);
        acc[1] = MFMA32(fixF[1], fixT, acc[1]);
        store_h(acc[0], WN);
        store_h(acc[1], 2 + WN);
    } else {
        acc[0] = MFMA32(fixF[0], fixT, acc[0]);
        acc[1] = MFMA32(fixT, fixF[1], acc[1]);
        bf16x8 idf[2];
#pragma unroll
        for (int s = 0; s < 2; ++s) {
            u32x4 u;
#pragma unroll
            for (int i = 0; i < 4; ++i) {
                int f0 = 16 * s + 8 * (i >> 1) + 4 * hh + 2 * (i & 1);
                u[i] = (r32 == f0 ? 0x3F80u : 0u) | (r32 == f0 + 1 ? 0x3F800000u : 0u);
            }
            idf[s] = asbf(u);
        }
#pragma unroll
        for (int X = 0; X < 2; ++X) {
            LAS char* p = lds + L_AUX + (2 * WN + X) * 512 + (4 * hh + q4) * 32 + p4 * 8;
            u32x4 u = __builtin_bit_cast(u32x4, cat44(tr_read(p), tr_read(p + 256)));
            const unsigned sg = blk ? 0x80008000u : 0u;
#pragma unroll
            for (int i = 0; i < 4; ++i) u[i] ^= sg;
            wfr[X] = asbf(u);
        }
#pragma unroll
        for (int g = 0; g < 4; ++g) {
            f32x4 rr = *(LAS f32x4*)(lds + L_RR + (wave * 32 + 8 * g + 4 * hh) * 4);
#pragma unroll
            for (int i = 0; i < 4; ++i) acc[1][4 * g + i] *= rr[i];
        }
        const u32x4 v0 = pack8u<0>(acc[1]), v1 = pack8u<1>(acc[1]);
        const bf16x8 kb0 = pack8<0>(acc[0]), kb1 = pack8<1>(acc[0]);
        {
            f32x16 p[2], d[2], kv[2];
            u32x4 F[2][2];
            p[0] = MFMA32(wfr[0], kb0, zero16);
            p[1] = MFMA32(wfr[1], kb1, zero16);
            feature_map2(p, F);
#pragma unroll
            for (int X = 0; X < 2; ++X) d[X] = MFMA32(asbf(F[X][0]), idf[0], zero16);
#pragma unroll
            for (int X = 0; X < 2; ++X) d[X] = MFMA32(asbf(F[X][1]), idf[1], d[X]);
            u32x4 b[2][2];
#pragma unroll
            for (int X = 0; X < 2; ++X)
#pragma unroll
                for (int i = 0; i < 4; ++i) { b[X][0][i] = (blk == X) ? v0[i] : 0x3F803F80u; b[X][1][i] = (blk == X) ? v1[i] : 0x3F803F80u; }
#pragma unroll
            for (int X = 0; X < 2; ++X) kv[X] = MFMA32(pack8<0>(d[X]), asbf(b[X][0]), zero16);
#pragma unroll
            for (int X = 0; X < 2; ++X) kv[X] = MFMA32(pack8<1>(d[X]), asbf(b[X][1]), kv[X]);
#pragma unroll
            for (int X = 0; X < 2; ++X) {
                const int row = X == 0 ? r32 : (r32 == 15 ? 16 : r32 - 16);
                const bool use = X == 0 ? (r32 <= 16) : (r32 >= 15);
                LAS char* dst = lds + (use ? L_RED + (((WN * 2 + X) * 4 + wm) * 17 + row) * 144 : L_RED + 39168 + (r32 & 7) * 144) + hh * 16;
#pragma unroll
                for (int g = 0; g < 4; ++g) {
                    f32x4 v = {kv[X][4 * g], kv[X][4 * g + 1], kv[X][4 * g + 2], kv[X][4 * g + 3]};
                    *(LAS f32x4*)(dst + g * 32) = v;
                }
            }
        }
        wait_lgkm0();
        barrier();
        if constexpr (WN == 1) {
#pragma unroll
            for (int it = 0; it < 5; ++it) {
                const int o = it * 64 + lane;
                if (it < 4 || lane < 16) {
                    LAS char* src = lds + L_RED + (wm * 4 * 17 + (o >> 4)) * 144 + (o & 15) * 8;
                    f32x2 v = (*(LAS f32x2*)(src) + *(LAS f32x2*)(src + 2448)) + (*(LAS f32x2*)(src + 2 * 2448) + *(LAS f32x2*)(src + 3 * 2448));
                    u32x4 g0 = {f2u(v[0]), tag, f2u(v[1]), tag};
                    __builtin_amdgcn_raw_buffer_store_b128(g0, rs, WS_KVP + (unsigned)((mt * 32 + nt * 4 + wm) * 272 + o) * 16, 0, SC1);
                }
            }
        }
    }
    f32x16 bc[NB2];
#pragma unroll
    for (int t = 0; t < NB2; ++t) bc[t] = MFMA32(fixF[2 + t], fixT, zero16);
    {
        constexpr int CB0 = QKV ? 81920 : 40960;
        LAS char* q0 = lds + hh * (NTB * 512) + WN * 512 + q4 * 64 + blk * 32 + p4 * 8 + CB0;
        LAS char* q1 = q0 + (98304 - CB0);
        PIN(q0); PIN(q1);
        bf16x8 qf[4][NB2];
        auto read_q = [&](int g, int t) {
            const int cb = chunk_base(QKV, g >> 3);
            LAS char* p = (cb >= 98304 ? q1 + (cb - 98304) : q0 + (cb - CB0)) + (g & 7) * (NTB * 1024) + t * 512;
            qf[g & 3][t] = cat44(tr_read(p), tr_read(p + 256));
        };
#pragma unroll
        for (int g = 0; g < 3; ++g)
#pragma unroll
            for (int t = 0; t < NB2; ++t) read_q(g, t);
        static_for<0, 32>([&](auto GI) {
            constexpr int g = GI.value;
            SCHED0();
            if constexpr (g == 8 || g == 16 || g == 24) {
                if constexpr (g == 8 && WN == 1) wait_vm<QKV ? 5 : 4>();
                wait_lgkm0(); barrier();
            }
            static_for<0, NB2>([&](auto TI) {
                constexpr int t = TI.value;
                bc[t] = MFMA32(qf[g & 3][t], xf[g], bc[t]);
                if constexpr (g + 3 < 32) read_q(g + 3, t);
                if constexpr (WN == 0) {
                    if constexpr (QKV && g >= 8 && g < 18) dma_w2(IC(10 + g - 8));
                    if constexpr (!QKV && g >= 24 && g < 30) dma_w2(IC(10 + g - 24));
                } else if constexpr (!QKV && g >= 16 && g < 24) dma_wo(IC(2 * (g - 16) + t));
                else if constexpr (QKV && g >= 9 && g < 25) { if (wm >= 2) dma_wo(IC(g - 9)); }
                SCHED0();
            });
        });
    }
    auto w2_to_regs = [&]() {
        LAS char* rdA = lds + wm * 2048 + trl;
#pragma unroll
        for (int s = 0; s < NR; ++s) {
            LAS char* p = rdA + s * 8192;
            ph[2 * s] = __builtin_bit_cast(u32x4, cat44(tr_read(p), tr_read(p + 1024)));
            ph[2 * s + 1] = __builtin_bit_cast(u32x4, cat44(tr_read(p + 512), tr_read(p + 1536)));
        }
        wait_lgkm0();
    };
    if constexpr (!QKV) {
#pragma unroll
        for (int t = 0; t < NB2; ++t) store_h(bc[t], 4 + WN + t);
        if constexpr (WN == 0) {
            wait_vm<2>();
            w2_to_regs();
            static_for<2 * NB, 2 * NB + 8>([&](auto P) { dma_w2(P); });
            wait_vm<8>();
            barrier();
        } else {
            if (wm >= 2) {
                const int sl = (mt * 8 + nt) * 2 + (wm & 1);
                const unsigned vo = (unsigned)(((sl >> 3) * 136 + (sl & 7) * 17 + (lane < 34 ? (lane >> 1) : 0)) * 32 + (lane & 1) * 16);
                static_for<0, 16>([&](auto M) { ld16c<WS_KVP + M.value * 32 * 136 * 32>(ph[M.value], vo, rs); });
                wait_vm<16>();
                barrier();
                wait16(ph);
                ph[0][0] = kv_reduce_publish(a.ws, ph, sl, lane, tag) ? 1u : 0u;
            } else {
                if (wave == 4 && lane < 16) *(LAS unsigned*)(lds + L2_CNT + lane * 4) = 0u;
                wait_vm<0>();
                wait_lgkm0();
                barrier();
                if (wave == 5 && lane == 0)
                    __hip_atomic_store((GAS unsigned*)(a.ws + WS_FLAG + (mt * 16 + 8 + nt) * 64), tag, RLX_AGENT);
            }
        }
    } else {
        const bf16x8 qb0 = pack8<0>(bc[0]), qb1 = pack8<1>(bc[0]);
        if constexpr (WN == 0) {
            wait_vm<0>(); w2_to_regs();
            static_for<2 * NB, 2 * NB + 8>([&](auto P) { dma_w2(P); });
        }
        {
            f32x16 p[2];
            u32x4 F[2][2];
            p[0] = MFMA32(wfr[0], qb0, zero16);
            p[1] = MFMA32(wfr[1], qb1, zero16);
            feature_map2(p, F);
#pragma unroll
            for (int X = 0; X < 2; ++X) {
                LAS char* dst = lds + L_PHI + ((2 * WN + X) * 4 + wm) * 2048 + lane * 16;
                *(LAS u32x4*)(dst) = F[X][0];
                *(LAS u32x4*)(dst + 1024) = F[X][1];
            }
        }
        wait_lgkm0();
        barrier();
        if constexpr (WN == 1) {
            if (wave == 4 && lane < 16) *(LAS unsigned*)(lds + L2_CNT + lane * 4) = 0u;
#pragma unroll
            for (int i = 0; i < 8; ++i) ph[i] = *(LAS u32x4*)(lds + L_PHI + wm * 8192 + i * 1024 + lane * 16);
            wait_lgkm0();
        }
        barrier();
    }
    return tag;
}

constexpr int HP = 6;
constexpr int w_at(int i) { return i >= NR && i + NB <= 27 ? i + NB : -1; }
constexpr int p2_pos(bool isw, int idx) {
    int pos = 0;
    for (int s = 0; s < HP; ++s) { pos += 2; if (!isw && idx == s) return pos; }
    for (int i = 1; i < 28; ++i) {
        if (i - 1 + HP <= 27) { pos += 2; if (!isw && idx == i - 1 + HP) return pos; }
        if (w_at(i - 1) >= 0) { pos += 2; if (isw && idx == w_at(i - 1)) return pos; }
    }
    return 0;
}
constexpr int p2_issued(int step) {
    int pos = 2 * HP;
    for (int i = 1; i < step; ++i) { if (i - 1 + HP <= 27) pos += 2; if (w_at(i - 1) >= 0) pos += 2; }
    return pos;
}
constexpr int p2_wait(int i) {
    int need = p2_pos(false, i);
    if (i + 1 >= NR + NB && i + 1 <= 27) { int w = p2_pos(true, i + 1); if (w > need) need = w; }
    return p2_issued(i) - need;
}
template <int SLEEP> DI void poll_words(srd_t rs, unsigned voff, unsigned tag) {
    for (unsigned spin = 0; spin <= SPIN_CAP; ++spin) {
        unsigned v;
        ld4c<0>(v, voff, rs);
        asm volatile("s_waitcnt vmcnt(0) ; PINW %0" : "+v"(v) :: "memory");
        if (__all(v == tag)) break;
        __builtin_amdgcn_s_sleep(SLEEP);
    }
}
DI void lds_wait_word(LAS char* p) {
    for (unsigned spin = 0; spin <= 16 * SPIN_CAP; ++spin) {
        if (__hip_atomic_load((LAS unsigned*)p, __ATOMIC_RELAXED, __HIP_MEMORY_SCOPE_WORKGROUP) != 0u) break;
        __builtin_amdgcn_s_sleep(1);
    }
    CFENCE();
}
DI void lds_set_word(LAS char* p) { __hip_atomic_store((LAS unsigned*)p, 1u, __ATOMIC_RELAXED, __HIP_MEMORY_SCOPE_WORKGROUP); }

struct Fin { f32x16 acc[2][2]; u32x2 xr0, xr1, br0, br1; };
template <int PART>
DI void phase2(const Args& a, LAS char* lds, const Geo& G, unsigned tag, u32x4 (&ph)[16], float vp2, Fin& F) {
    const int lane = G.lane, wave = G.wave, mt = G.mt2, nt = G.nt2;
    const unsigned ldsb = (unsigned)(size_t)lds;
    const int hh = lane >> 5, r32 = lane & 31, blk = (lane >> 4) & 1, q4 = (lane & 15) >> 2, p4 = lane & 3;
    const int kq = wave & 3, hf = wave >> 2;
    const srd_t rs = mk_srd(a.ws, WS_END);
    const srd_t rw2 = mk_srd(a.w2, FF * DM * 2);
    const unsigned wvo = (unsigned)((((lane >> 2) & 7) * DM + nt * 64 + hh * 32 + (lane & 3) * 8) * 2);
    const unsigned trl = (4 * hh + q4) * 64 + blk * 32 + p4 * 8;
    const unsigned l16 = lane * 16;
    unsigned w2q[4];
#pragma unroll
    for (int q = 0; q < 4; ++q) w2q[q] = wvo + (unsigned)((kq * 448 + ((q + G.rq) & 3) * 112) * DM * 2);
    const int token = mt * 64 + (kq & 1) * 32 + r32;
    const int col0 = nt * 64 + (kq >> 1) * 32 + 16 * hf + 4 * hh;
    u32x2 &xr0 = F.xr0, &xr1 = F.xr1, &br0 = F.br0, &br1 = F.br1;
    auto final_loads = [&]() {
        const srd_t rx = mk_srd(a.x, SEQ * DM * 2), rb = mk_srd(a.b2, DM * 2);
        ld8<0>(xr0, (unsigned)(token * DM + col0) * 2, rx); ld8<16>(xr1, (unsigned)(token * DM + col0) * 2, rx);
        ld8<0>(br0, (unsigned)col0 * 2, rb); ld8<16>(br1, (unsigned)col0 * 2, rb);
    };
    f32x16 (&acc)[2][2] = F.acc;
    if constexpr (PART < 2) {
#pragma unroll
        for (int i = 0; i < 2; ++i)
#pragma unroll
            for (int j = 0; j < 2; ++j)
#pragma unroll
                for (int e = 0; e < 16; ++e) acc[i][j][e] = 0.f;
    }
    LAS char* upper = lds + L2_A + kq * 16384;
    if constexpr (PART == 0) {
        const unsigned lb = ldsb + kq * 2048;
        unsigned hq[4];
#pragma unroll
        for (int q = 0; q < 4; ++q) hq[q] = (unsigned)(((2 * mt) * 112 + 28 * kq + ((q + G.rq) & 3) * 7) * 1024) + l16;
        auto issue_w = [&](auto SI) {
            constexpr int s = SI.value;
            dma16<(s % NB) * 8192, (s % 7) * 16 * DM * 2>(w2q[s / 7], rw2, lb);
            dma16<(s % NB) * 8192 + 1024, ((s % 7) * 16 + 8) * DM * 2>(w2q[s / 7], rw2, lb);
        };
        u32x4 hb[HP][2];
        if (!G.qkv) { issue_w(IC(8)); issue_w(IC(9)); }
        issue_w(IC(10));
        static_for<NB + 4, NR + NB>([&](auto SI) { issue_w(SI); });
        final_loads();
        LAS char* rdA = lds + kq * 2048 + trl;
        LAS char* rdB = rdA + 6 * 8192;
        PIN(rdB);
        bf16x8 wf[2][2];
        auto rd = [&](int s, bf16x8 (&d)[2]) {
            LAS char* p = (s % NB) < 6 ? rdA + (s % NB) * 8192 : rdB + (s % NB - 6) * 8192;
            d[0] = cat44(tr_read(p), tr_read(p + 1024));
            d[1] = cat44(tr_read(p + 512), tr_read(p + 1536));
        };
        lds_wait_word(lds + L2_CNT + 16);
        static_for<0, HP>([&](auto SI) {
            constexpr int s = SI.value;
            ld16c<WS_H + (s % 7) * 1024>(hb[s % HP][0], hq[s / 7], rs);
            ld16c<WS_H + (112 + s % 7) * 1024>(hb[s % HP][1], hq[s / 7], rs);
        });
        static_for<0, 28>([&](auto SI) {
            constexpr int s = SI.value;
            SCHED0();
            asm volatile("s_waitcnt vmcnt(%2) ; PINW %0 %1" : "+v"(hb[s % HP][0]), "+v"(hb[s % HP][1]) : "i"(p2_wait(s)) : "memory");
            static_for<0, 4>([&](auto MI) {
                constexpr int m = MI.value, i = m >> 1, j = m & 1;
                acc[i][j] = MFMA32(s < NR ? asbf(ph[s < NR ? 2 * s + i : 0]) : wf[s & 1][i], asbf(hb[s % HP][j]), acc[i][j]);
                SCHED0();
                if constexpr (m == 0 && s + 1 >= NR && s + 1 < 28) rd(s + 1, wf[(s + 1) & 1]);
                constexpr int hs = s - 1 + HP;
                constexpr int ws = s >= 1 ? w_at(s - 1) : -1, wz = ws >= 0 ? ws : 0;
                if constexpr (s >= 1 && hs <= 27 && m < 2) ld16c<WS_H + (m * 112 + hs % 7) * 1024>(hb[hs % HP][m], hq[hs / 7], rs);
                if constexpr (ws >= 0 && m >= 2) dma16<(wz % NB) * 8192 + (m - 2) * 1024, ((wz % 7) * 16 + (m - 2) * 8) * DM * 2>(w2q[wz / 7], rw2, lb);
                SCHED0();
            });
        });
        SCHED0();
        wait_lgkm0();
#pragma unroll
        for (int p = 0; p < 16; ++p) {
            const f32x16& c = acc[p >> 3][(p >> 2) & 1];
            f32x4 v = {c[4 * (p & 3)], c[4 * (p & 3) + 1], c[4 * (p & 3) + 2], c[4 * (p & 3) + 3]};
            *(LAS f32x4*)(lds + (p >> 1) * 8192 + kq * 2048 + (p & 1) * 1024 + l16) = v;
        }
        asm volatile("s_waitcnt vmcnt(0) ; PINW %0 %1 %2 %3" : "+v"(xr0), "+v"(xr1), "+v"(br0), "+v"(br1) :: "memory");
    } else if constexpr (PART == 1) {
        const srd_t rwo = mk_srd(a.wo, DM * DM * 2);
        __builtin_amdgcn_s_setprio(3);
        bf16x8 wv[8][2];
        auto wo_on = [&]() {
#pragma unroll
            for (int h = 0; h < 8; ++h) {
                LAS char* p = upper + h * 2048 + trl;
                wv[h][0] = cat44(tr_read(p), tr_read(p + 1024));
                wv[h][1] = cat44(tr_read(p + 512), tr_read(p + 1536));
            }
        };
        auto poll_h = [&]() {
            poll_words<1>(rs, WS_FLAG + (unsigned)((mt >> 1) * 16 + 8 + (lane & 7)) * 64, tag);
            if (lane == 0) lds_set_word(lds + L2_CNT + 16);
        };
        if (!G.qkv) {
            if (kq == 0) poll_h();
            if (kq >= 2 && __builtin_amdgcn_readfirstlane((int)ph[0][0]) == 0) {
                const int sl = (G.mt * 8 + G.nt) * 2 + (kq & 1);
                const unsigned vo = (unsigned)(((sl >> 3) * 136 + (sl & 7) * 17 + (lane < 34 ? (lane >> 1) : 0)) * 32 + (lane & 1) * 16);
                for (unsigned spin = 0; spin <= SPIN_CAP; ++spin) {
                    static_for<0, 16>([&](auto M) { ld16c<WS_KVP + M.value * 32 * 136 * 32>(ph[M.value], vo, rs); });
                    wait16(ph);
                    if (kv_reduce_publish(a.ws, ph, sl, lane, tag)) break;
                    __builtin_amdgcn_s_sleep(2);
                }
            }
        } else {
            const unsigned kvo = (unsigned)((4 * G.nt + kq) * 2176 + hh * 272 + (r32 < 16 ? r32 : 16) * 16);
            const unsigned hfo = WS_FLAG + (unsigned)((mt >> 1) * 16 + 8 + (lane & 7)) * 64;
            const unsigned wovo = wvo + kq * (128 * DM * 2);
            u32x4 raw[4]; unsigned fl;
            bool hdone = kq != 2;
            auto request = [&]() { static_for<0, 4>([&](auto I) { ld16c<WS_KVR + I.value * 544>(raw[I.value], kvo, rs); }); ld4c<0>(fl, hfo, rs); };
            if (kq < 2) static_for<0, 16>([&](auto P) { dma16<L2_A + P.value * 1024, P.value * 8 * DM * 2>(wovo, rwo, ldsb + kq * 16384); });
            request();
            asm volatile("s_waitcnt vmcnt(0) ; PINW %0 %1 %2 %3 %4" : "+v"(raw[0]), "+v"(raw[1]), "+v"(raw[2]), "+v"(raw[3]), "+v"(fl) :: "memory");
            for (unsigned spin = 0; spin <= SPIN_CAP; ++spin) {
                if (!hdone && __all(fl == tag)) { hdone = true; if (lane == 0) lds_set_word(lds + L2_CNT + 16); }
                unsigned bad = 0;
#pragma unroll
                for (int i = 0; i < 4; ++i) bad |= (raw[i][1] ^ tag) | (raw[i][3] ^ tag);
                if (__all(bad == 0u)) break;
                __builtin_amdgcn_s_sleep(1);
                request();
                asm volatile("s_waitcnt vmcnt(0) ; PINW %0 %1 %2 %3 %4" : "+v"(raw[0]), "+v"(raw[1]), "+v"(raw[2]), "+v"(raw[3]), "+v"(fl) :: "memory");
            }
            f32x16 zero16;
#pragma unroll
            for (int i = 0; i < 16; ++i) zero16[i] = 0.f;
            const u32x4 k0 = {raw[0][0], raw[0][2], raw[1][0], raw[1][2]}, k1 = {raw[2][0], raw[2][2], raw[3][0], raw[3][2]};
#pragma unroll
            for (int w = 0; w < 4; ++w) {
                f32x16 nd = MFMA32(asbf(k0), asbf(ph[2 * w]), zero16);
                nd = MFMA32(asbf(k1), asbf(ph[2 * w + 1]), nd);
                const float inv = __builtin_amdgcn_rcpf(nd[8] + 1e-6f);
#pragma unroll
                for (int e = 0; e < 8; ++e) nd[e] *= inv;
                __builtin_amdgcn_raw_buffer_store_b128(pack8u<0>(nd), rs, WS_O + (unsigned)(((G.mt * 4 + w) * 32 + G.nt * 4 + kq) * 1024) + l16, 0, SC1);
            }
            wait_vm<0>();
            unsigned old = 0;
            if (lane == 0) old = __hip_atomic_fetch_add((LAS unsigned*)(lds + L2_CNT), 1u, __ATOMIC_RELAXED, __HIP_MEMORY_SCOPE_WORKGROUP);
            if (lane == 0 && old == 3u) __hip_atomic_store((GAS unsigned*)(a.ws + WS_FLAG + (G.mt * 16 + G.nt) * 64), tag, RLX_AGENT);
            if (!hdone) poll_h();
            __builtin_amdgcn_s_setprio(0);
        }
        __builtin_amdgcn_s_setprio(0);
        final_loads();
        poll_words<4>(rs, WS_FLAG + (unsigned)((mt >> 1) * 16 + 2 * kq + (lane & 1)) * 64, tag);
        const unsigned ovo = (unsigned)((2 * mt) * 32 + 8 * kq) * 1024 + l16;
        static_for<0, 16>([&](auto I) { constexpr int i = I.value; ld16c<WS_O + (i & 1) * 32768 + (i >> 1) * 1024>(ph[i], ovo, rs); });
        wo_on();
        wait16(ph);
#pragma unroll
        for (int h = 0; h < 8; ++h)
#pragma unroll
            for (int i = 0; i < 2; ++i)
#pragma unroll
                for (int j = 0; j < 2; ++j) acc[i][j] = MFMA32(wv[h][i], asbf(ph[2 * h + j]), acc[i][j]);
        wait_lgkm0();
#pragma unroll
        for (int p = 0; p < 16; ++p) {
            const f32x16& c = acc[p >> 3][(p >> 2) & 1];
            f32x4 v = {c[4 * (p & 3)], c[4 * (p & 3) + 1], c[4 * (p & 3) + 2], c[4 * (p & 3) + 3]};
            *(LAS f32x4*)(upper + p * 1024 + l16) = v;
        }
        asm volatile("s_waitcnt vmcnt(0) ; PINW %0 %1 %2 %3" : "+v"(xr0), "+v"(xr1), "+v"(br0), "+v"(br1) :: "memory");
    } else {
    wait_lgkm0();
    barrier();
    const float r2 = __builtin_amdgcn_rcpf(vp2);
#pragma unroll
    for (int g = 0; g < 2; ++g) {
        LAS char* pa = lds + L2_A + (kq * 4 + 2 * hf + g) * 1024 + l16;
        LAS char* pf = lds + (kq * 2 + hf) * 8192 + g * 1024 + l16;
        f32x4 ss = (*(LAS f32x4*)(pa) + *(LAS f32x4*)(pa + 16384)) + (*(LAS f32x4*)(pa + 2 * 16384) + *(LAS f32x4*)(pa + 3 * 16384));
        f32x4 so = (*(LAS f32x4*)(pf) + *(LAS f32x4*)(pf + 2048)) + (*(LAS f32x4*)(pf + 4096) + *(LAS f32x4*)(pf + 6144));
        const u32x2 xr = g ? xr1 : xr0, br = g ? br1 : br0;
        float res[4];
#pragma unroll
        for (int i = 0; i < 4; ++i) {
            const float xv = u2f((i & 1) ? (xr[i >> 1] & 0xFFFF0000u) : (xr[i >> 1] << 16));
            const float bv = u2f((i & 1) ? (br[i >> 1] & 0xFFFF0000u) : (br[i >> 1] << 16));
            const float o = so[i] * r2 + bv;
            const float gt = o * __builtin_amdgcn_rcpf(1.f + __builtin_amdgcn_exp2f(-o * LOG2E));
            res[i] = xv + ss[i] * gt;
        }
        u32x2 ov = {pk2(res[0], res[1]), pk2(res[2], res[3])};
        *(GAS u32x2*)(a.out + (size_t)token * DM + col0 + 8 * g) = ov;
    }
    if (G.bid == 0 && wave == 7) {
        for (unsigned spin = 0; spin <= SPIN_CAP; ++spin) {
            unsigned f0, f1, f2, f3;
            ld4c<WS_FLAG>(f0, (unsigned)lane * 64, rs); ld4c<WS_FLAG + 4096>(f1, (unsigned)lane * 64, rs);
            ld4c<WS_FLAG + 8192>(f2, (unsigned)lane * 64, rs); ld4c<WS_FLAG + 12288>(f3, (unsigned)lane * 64, rs);
            asm volatile("s_waitcnt vmcnt(0) ; PINW %0 %1 %2 %3" : "+v"(f0), "+v"(f1), "+v"(f2), "+v"(f3) :: "memory");
            if (__all(f0 == tag && f1 == tag && f2 == tag && f3 == tag)) break;
            __builtin_amdgcn_s_sleep(1);
        }
        if (lane == 0) __hip_atomic_store((GAS unsigned*)(a.ws + WS_CTRL), tag, RLX_AGENT);
    }
    }
}

__global__ void __launch_bounds__(512, 2) fused_kernel(Args a) {
    extern __shared__ __attribute__((aligned(16))) char smem[];
    LAS char* lds = (LAS char*)smem;
    Geo G;
    G.bid = blockIdx.x;
    G.wave = __builtin_amdgcn_readfirstlane(threadIdx.x >> 6);
    G.lane = threadIdx.x & 63;
    const int xcd = G.bid & 7, loc = G.bid >> 3;
    const int sel = loc >> 2;
    G.mt = (xcd >> 1) * 4 + (loc & 3);
    G.nt = (xcd & 1) * 4 + (sel & 3);
    G.i16 = (xcd & 1) * 8 + sel;
    G.mt2 = G.mt * 2 + (xcd & 1);
    G.nt2 = sel;
    G.qkv = sel < 4;
    G.rq = sel & 3;
    unsigned wvo0[4], xvo0[4], epoch;
    u32x4 rowv[2] = {u32x4{0u, 0u, 0u, 0u}, u32x4{0u, 0u, 0u, 0u}};
    {
        const int lane = G.lane, wm = G.wave & 3, hh = lane >> 5;
        const unsigned ldsb = (unsigned)(size_t)lds;
        const int LDW = G.qkv ? 1536 : 1792;
        const srd_t rx = mk_srd(a.x, SEQ * DM * 2);
        const srd_t rw = mk_srd(G.qkv ? a.wqkv : a.w1, DM * LDW * 2);
        ld4c<WS_CTRL>(epoch, 0u, mk_srd(a.ws, WS_END));
        if (G.wave >= 4) {
            const unsigned ro = (unsigned)((G.mt * 128 + G.i16 * 8 + 2 * wm) * DM * 2 + lane * 16);
            ld16(rowv[0], ro, rx); ld16(rowv[1], ro + 1024, rx);
        } else {
            const int u1 = G.mt + 16 < (G.qkv ? 24 : 28) ? G.mt + 16 : G.mt;
            const unsigned rowpart = (unsigned)(((G.mt & 3) * 128 + 32 * wm + (lane >> 2)) * LDW + (lane & 3) * 8) * 2;
#pragma unroll
            for (int u = 0; u < 2; ++u) {
                const int c = (u ? u1 : G.mt) >> 2;
                const int colb = G.qkv ? ((((c >> 1) + 1) % 3) * 512 + 64 * G.nt + 32 * (c & 1)) : (224 * G.nt + 32 * c);
                dma16r(rowpart + colb * 2, rw, ldsb + L_UNIT + u * 8192 + wm * 2048);
                dma16r(rowpart + colb * 2 + 16 * LDW * 2, rw, ldsb + L_UNIT + u * 8192 + wm * 2048 + 1024);
            }
        }
#pragma unroll
        for (int i = 0; i < 4; ++i) {
            const int row = 8 * (4 * wm + i) + (lane >> 3);
            xvo0[i] = (unsigned)(((G.mt * 128 + row) * DM + ((lane & 7) ^ ((row >> 1) & 7)) * 8) * 2);
            const int k8 = 2 * wm + (i >> 1), c = 2 * (i & 1) + hh;
            const int colb = G.qkv ? (((c >> 1) + 1) * 512 + 64 * G.nt + 32 * (c & 1)) : (224 * G.nt + 32 * c);
            wvo0[i] = (unsigned)(((k8 * 8 + ((lane >> 2) & 7)) * LDW + colb + (lane & 3) * 8) * 2);
        }
        if (G.wave >= 4) {
            static_for<0, 4>([&](auto I) { dma16<SLOT + XOFF + I.value * 1024, 0>(xvo0[I.value], rx, ldsb + wm * 4096); });
        } else {
            static_for<0, 4>([&](auto I) { dma16<SLOT + I.value * 1024, 0>(wvo0[I.value], rw, ldsb + wm * 4096); });
        }
        asm volatile("s_waitcnt vmcnt(4) ; PINW %0 %1 %2" : "+v"(epoch), "+v"(rowv[0]), "+v"(rowv[1]) :: "memory");
    }
    unsigned tag;
    float vp2 = 1.f;
    Fin F;
    if (G.wave < 4) {
        u32x4 ph[16];
        if (sel < 4) tag = phase1<true, 0>(a, lds, G, wvo0, xvo0, epoch, rowv, ph, vp2);
        else tag = phase1<false, 0>(a, lds, G, wvo0, xvo0, epoch, rowv, ph, vp2);
        phase2<0>(a, lds, G, tag, ph, vp2, F);
    } else {
        u32x4 ph[16];
#pragma unroll
        for (int i = 0; i < 16; ++i) ph[i] = u32x4{0u, 0u, 0u, 0u};
        if (sel < 4) tag = phase1<true, 1>(a, lds, G, wvo0, xvo0, epoch, rowv, ph, vp2);
        else tag = phase1<false, 1>(a, lds, G, wvo0, xvo0, epoch, rowv, ph, vp2);
        phase2<1>(a, lds, G, tag, ph, vp2, F);
    }
    {
        u32x4 ph[16];
        phase2<2>(a, lds, G, tag, ph, vp2, F);
    }
}

extern "C" void kernel_launch(void* const* d_in, const int* in_sizes, int n_in, void* d_out, int out_size, void* d_ws,
                              size_t ws_size, hipStream_t stream) {
    Args a;
    a.x = (const __bf16*)d_in[0]; a.wqkv = (const __bf16*)d_in[1]; a.wphi = (const __bf16*)d_in[2]; a.wo = (const __bf16*)d_in[3];
    a.w1 = (const __bf16*)d_in[5]; a.b1 = (const __bf16*)d_in[6]; a.w2 = (const __bf16*)d_in[7]; a.b2 = (const __bf16*)d_in[8];
    a.ws = (char*)d_ws; a.out = (__bf16*)d_out;
    (void)hipFuncSetAttribute((const void*)fused_kernel, hipFuncAttributeMaxDynamicSharedMemorySize, L_TOTAL);
    fused_kernel<<<256, 512, L_TOTAL, stream>>>(a);
}
```

```cpp
#include <hip/hip_runtime.h>
#include <hip/hip_bf16.h>
#include <stdint.h>
#include <type_traits>

typedef __attribute__((ext_vector_type(8))) __bf16 bf16x8;
typedef __attribute__((ext_vector_type(4))) __bf16 bf16x4;
typedef __attribute__((ext_vector_type(2))) __bf16 bf16x2;
typedef __attribute__((ext_vector_type(16))) float f32x16;
typedef __attribute__((ext_vector_type(4))) float f32x4;
typedef __attribute__((ext_vector_type(2))) float f32x2;
typedef __attribute__((ext_vector_type(4))) unsigned u32x4;
typedef __attribute__((ext_vector_type(2))) unsigned u32x2;
typedef __amdgpu_buffer_rsrc_t srd_t;
#define LAS __attribute__((address_space(3)))
#define GAS __attribute__((address_space(1)))
#define DI __device__ __forceinline__
#define MFMA32(a, b, c) __builtin_amdgcn_mfma_f32_32x32x16_bf16((a), (b), (c), 0, 0, 0)
#define SCHED0() __builtin_amdgcn_sched_barrier(0)
#define CFENCE() asm volatile("" ::: "memory")
#define RLX_AGENT __ATOMIC_RELAXED, __HIP_MEMORY_SCOPE_AGENT

constexpr int SEQ = 2048, DM = 512, NH = 32, FF = 1792;
constexpr float LOG2E = 1.4426950408889634f;
constexpr int SC1 = 16;
constexpr unsigned SPIN_CAP = 1u << 16;
constexpr unsigned CODE_LINES = 380;

constexpr unsigned WS_CTRL = 0;
constexpr unsigned WS_FLAG = 256;
constexpr unsigned WS_RST  = WS_FLAG + 256 * 64;
constexpr unsigned WS_CSG  = WS_RST + 2048 * 16;
constexpr unsigned WS_KVR  = WS_CSG + 3328 * 32;
constexpr unsigned WS_KVP  = WS_KVR + 32 * 2176;
constexpr unsigned WS_H    = (WS_KVP + 16u * 32 * 136 * 32 + 4095) & ~4095u;
constexpr unsigned WS_O    = WS_H + 64u * 112 * 1024;
constexpr unsigned WS_END  = WS_O + 64u * 32 * 1024;

#define ST(i)

constexpr int SLOT = 32768, XOFF = 16384;
constexpr int chunk_base(bool qkv, int c) { return qkv ? (c == 0 ? 131072 : (c == 3 ? 81920 : 98304 + (c - 1) * 16384)) : (c == 0 ? 98304 : (c == 1 ? 122880 : (c == 2 ? 40960 : 65536))); }
constexpr int L_UNIT = 131072;
constexpr int L_CSP = 147456;
constexpr int L_RR = L_CSP + 1024;
constexpr int L_AUX = L_RR + 1024;
constexpr int L_DUMMY = L_AUX + 2048;
constexpr int L_FIX = L_DUMMY + 256;
constexpr int L_RED = 40960;
constexpr int L_PHI = 98304;
constexpr int NR = 8;
constexpr int NB = 11;
constexpr int L2_CNT = NB * 8192;
constexpr int L2_A = 98304;
constexpr int L_TOTAL = 163840;
static_assert(L_FIX + 8192 <= L_TOTAL, "lds");

template <int LOFF, int SOFF> DI void dma16(unsigned voff, srd_t srd, unsigned lbase) {
    unsigned t;
    lbase = (unsigned)__builtin_amdgcn_readfirstlane((int)lbase);
    asm volatile("s_add_i32 m0, %3, %4\n\ts_mov_b32 %0, %5\n\tbuffer_load_dwordx4 %1, %2, %0 offen lds"
                 : "=&s"(t) : "v"(voff), "s"(srd), "s"(lbase), "i"(LOFF), "i"(SOFF) : "memory", "scc");
}
template <int LOFF> DI void touch_ptr(const void* p, unsigned lbase) {
    asm volatile("s_add_i32 m0, %1, %2\n\ts_nop 0\n\tglobal_load_lds_dword %0, off" :: "v"(p), "s"(lbase), "i"(LOFF) : "memory", "scc");
}
template <int LOFF> DI void touch_c(unsigned voff, srd_t srd, unsigned lbase) {
    asm volatile("s_add_i32 m0, %2, %3\n\ts_nop 0\n\tbuffer_load_dword %0, %1, 0 offen sc1 lds" :: "v"(voff), "s"(srd), "s"(lbase), "i"(LOFF) : "memory", "scc");
}
template <int LOFF> DI void touch(unsigned voff, srd_t srd, unsigned lbase) {
    asm volatile("s_add_i32 m0, %2, %3\n\ts_nop 0\n\tbuffer_load_dword %0, %1, 0 offen lds" :: "v"(voff), "s"(srd), "s"(lbase), "i"(LOFF) : "memory", "scc");
}
template <int OFF> DI void ld16c(u32x4& d, unsigned voff, srd_t srd) {
    asm volatile("buffer_load_dwordx4 %0, %1, %2, %3 offen offset:%4 sc1" : "=v"(d) : "v"(voff), "s"(srd), "s"(OFF & ~4095), "i"(OFF & 4095) : "memory");
}
template <int OFF> DI void ld4c(unsigned& d, unsigned voff, srd_t srd) {
    asm volatile("buffer_load_dword %0, %1, %2, %3 offen offset:%4 sc1" : "=v"(d) : "v"(voff), "s"(srd), "s"(OFF & ~4095), "i"(OFF & 4095) : "memory");
}
template <int OFF> DI void ld8c(u32x2& d, unsigned voff, srd_t srd) {
    asm volatile("buffer_load_dwordx2 %0, %1, %2, %3 offen offset:%4 sc1" : "=v"(d) : "v"(voff), "s"(srd), "s"(OFF & ~4095), "i"(OFF & 4095) : "memory");
}
DI void dma16r(unsigned voff, srd_t srd, unsigned lds_dst) {
    asm volatile("s_mov_b32 m0, %2\n\ts_nop 0\n\tbuffer_load_dwordx4 %0, %1, 0 offen lds" :: "v"(voff), "s"(srd), "s"(lds_dst) : "memory");
}
DI void ld16(u32x4& d, unsigned voff, srd_t srd) { asm volatile("buffer_load_dwordx4 %0, %1, %2, 0 offen" : "=v"(d) : "v"(voff), "s"(srd) : "memory"); }
template <int OFF> DI void ld8(u32x2& d, unsigned voff, srd_t srd) { asm volatile("buffer_load_dwordx2 %0, %1, %2, 0 offen offset:%3" : "=v"(d) : "v"(voff), "s"(srd), "i"(OFF) : "memory"); }
DI float fmx(float a, float b) { return __builtin_elementwise_maximum(a, b); }
DI void wait_lgkm0() { __builtin_amdgcn_s_waitcnt(0xC07F); CFENCE(); }
template <int N> DI void wait_vm() { asm volatile("s_waitcnt vmcnt(%0)" ::"i"(N) : "memory"); }
DI void barrier() { __builtin_amdgcn_s_barrier(); CFENCE(); }
#define PIN(x) asm volatile("" : "+v"(x))
DI srd_t mk_srd(const void* p, unsigned bytes) { return __builtin_amdgcn_make_buffer_rsrc((void*)p, 0, (int)bytes, 0x00020000); }

DI bf16x4 tr_read(LAS char* p) { return __builtin_amdgcn_ds_read_tr16_b64_v4bf16((LAS bf16x4*)p); }
DI bf16x8 cat44(bf16x4 a, bf16x4 b) { return __builtin_shufflevector(a, b, 0, 1, 2, 3, 4, 5, 6, 7); }
DI float dot2(unsigned a, unsigned b, float c) {
    return __builtin_amdgcn_fdot2_f32_bf16(__builtin_bit_cast(bf16x2, a), __builtin_bit_cast(bf16x2, b), c, false);
}
DI unsigned pk2(float a, float b) { f32x2 v = {a, b}; return __builtin_bit_cast(unsigned, __builtin_convertvector(v, bf16x2)); }
template <int S> DI u32x4 pack8u(const f32x16& x) {
    u32x4 p = {pk2(x[8 * S], x[8 * S + 1]), pk2(x[8 * S + 2], x[8 * S + 3]), pk2(x[8 * S + 4], x[8 * S + 5]), pk2(x[8 * S + 6], x[8 * S + 7])};
    return p;
}
template <int S> DI bf16x8 pack8(const f32x16& x) { return __builtin_bit_cast(bf16x8, pack8u<S>(x)); }
DI bf16x8 asbf(u32x4 u) { return __builtin_bit_cast(bf16x8, u); }
DI float u2f(unsigned u) { return __builtin_bit_cast(float, u); }
DI unsigned f2u(float f) { return __builtin_bit_cast(unsigned, f); }
DI unsigned hi2(unsigned lo, unsigned hi) { return __builtin_amdgcn_perm(hi, lo, 0x07060302u); }
DI void split3(float v, unsigned& s0, unsigned& s1, unsigned& s2) {
    s0 = f2u(v) & 0xFFFF0000u;
    float r = v - u2f(s0);
    s1 = f2u(r) & 0xFFFF0000u;
    s2 = f2u(r - u2f(s1));
}
DI float half_sum(float v) { auto s = __builtin_amdgcn_permlane32_swap(f2u(v), f2u(v), false, false); return u2f(s[0]) + u2f(s[1]); }
DI float wave_sum(float v) {
#define DPPADD(ctrl, rm) v += u2f((unsigned)__builtin_amdgcn_update_dpp(0, (int)f2u(v), ctrl, rm, 0xf, true))
    DPPADD(0x111, 0xf); DPPADD(0x112, 0xf); DPPADD(0x114, 0xf); DPPADD(0x118, 0xf); DPPADD(0x142, 0xa); DPPADD(0x143, 0xc);
#undef DPPADD
    return u2f((unsigned)__builtin_amdgcn_readlane((int)f2u(v), 63));
}
template <int C, int N, class F> DI void static_for(F&& f) { if constexpr (C < N) { f(std::integral_constant<int, C>{}); static_for<C + 1, N>(f); } }

#define NO_L2W
#define OPRIO 0
#define TOUCH1 2
struct Args {
    const __bf16* x; const __bf16* wqkv; const __bf16* wphi; const __bf16* wo;
    const __bf16* w1; const __bf16* b1; const __bf16* w2; const __bf16* b2;
    char* ws; __bf16* out;
};
struct Geo { int bid, wave, lane, mt, nt, i16, mt2, nt2, qkv, rq; };
#define IC(v) std::integral_constant<int, (v)>{}

template <int N> DI void gather_wait(u32x4& gst, u32x2& gv2, u32x4& gcs) {
    asm volatile("s_waitcnt vmcnt(%3) ; PINW %0 %1 %2" : "+v"(gst), "+v"(gv2), "+v"(gcs) : "i"(N) : "memory");
}
DI void wait16(u32x4 (&v)[16]) {
    asm volatile("s_waitcnt vmcnt(0) ; PINW %0 %1 %2 %3 %4 %5 %6 %7 %8 %9 %10 %11 %12 %13 %14 %15"
                 : "+v"(v[0]), "+v"(v[1]), "+v"(v[2]), "+v"(v[3]), "+v"(v[4]), "+v"(v[5]), "+v"(v[6]), "+v"(v[7]),
                   "+v"(v[8]), "+v"(v[9]), "+v"(v[10]), "+v"(v[11]), "+v"(v[12]), "+v"(v[13]), "+v"(v[14]), "+v"(v[15]) :: "memory");
}

DI bool kv_reduce_publish(char* ws, u32x4 (&v)[16], int sl, int lane, unsigned tag) {
    unsigned bad = 0;
#pragma unroll
    for (int m = 0; m < 16; ++m) bad |= (v[m][1] ^ tag) | (v[m][3] ^ tag);
    if (!__all(bad == 0u)) return false;
    float s0[16], s1[16];
#pragma unroll
    for (int m = 0; m < 16; ++m) { s0[m] = u2f(v[m][0]); s1[m] = u2f(v[m][2]); }
#pragma unroll
    for (int st = 8; st >= 1; st >>= 1)
#pragma unroll
        for (int m = 0; m < st; ++m) { s0[m] += s0[m + st]; s1[m] += s1[m + st]; }
    const int g = (sl & 7) * 17 + (lane < 34 ? (lane >> 1) : 0), row = g >> 3, fq = g & 7;
    const unsigned off = WS_KVR + (sl >> 3) * 2176 + fq * 272 + row * 16 + (lane & 1) * 8;
    if (lane < 34)
        __hip_atomic_store((GAS unsigned long long*)(ws + off), (unsigned long long)pk2(s0[0], s1[0]) | ((unsigned long long)tag << 32), RLX_AGENT);
    return true;
}
template <bool QKV, int WN>
DI unsigned phase1(const Args& a, LAS char* lds, const Geo& G, const unsigned (&wvo0)[4], const unsigned (&xvo0)[4],
                   unsigned epoch, const u32x4 (&rowv)[2], u32x4 (&ph)[16], float& vp2, unsigned long long* stamps) {
    constexpr int NTB = QKV ? 2 : 3;
    constexpr int NB2 = QKV ? 1 : (WN == 0 ? 1 : 2);
    constexpr int NG = 2 + NB2;
    constexpr int NU = QKV ? 24 : 28;
    constexpr int LDW = QKV ? 1536 : 1792;
    constexpr int CP = NTB * 2;
    const int lane = G.lane, wave = G.wave, wm = wave & 3, mt = G.mt, nt = G.nt;
    const unsigned ldsb = (unsigned)(size_t)lds;
    const int hh = lane >> 5, r32 = lane & 31, blk = (lane >> 4) & 1, q4 = (lane & 15) >> 2, p4 = lane & 3;
    const srd_t rs = mk_srd(a.ws, WS_END);
    const srd_t rx = mk_srd(a.x, SEQ * DM * 2);
    const srd_t rw = mk_srd(QKV ? a.wqkv : a.w1, DM * LDW * 2);
    const srd_t rw2 = mk_srd(a.w2, FF * DM * 2);
    const srd_t rwo = mk_srd(a.wo, DM * DM * 2);
    auto ptile = [&](int c) { const int p = c + (nt & 1); return p >= 7 ? p - 7 : p; };
    auto colb_of = [&](int c) { return QKV ? ((((c >> 1) + 1) % 3) * 512 + 64 * nt + 32 * (c & 1)) : (224 * nt + 32 * ptile(c)); };
    auto tile_of = [&](int t) { return t < 2 ? 2 * t + WN : (QKV ? 4 + WN : 4 + WN + (t - 2)); };
    auto dma_pair = [&](auto JI, auto II) {
        constexpr int j = JI.value, i = II.value, sb = ((j + 1) % 3) * SLOT;
        if constexpr (i < 4) dma16<sb + i * 1024, j * 64 * LDW * 2>(wvo0[i], rw, ldsb + wm * 4096);
        else dma16<sb + XOFF + (i - 4) * 1024, j * 128>(xvo0[i - 4], rx, ldsb + wm * 4096);
    };
    const unsigned w2vo = (unsigned)((((lane >> 2) & 7) * DM + G.nt2 * 64 + hh * 32 + (lane & 3) * 8) * 2);
    unsigned w2q[3];
#pragma unroll
    for (int q = 0; q < 3; ++q) w2q[q] = w2vo + (unsigned)((wm * 448 + ((q + G.rq) & 3) * 112) * DM * 2);
    auto dma_w2 = [&](auto PI) {
        constexpr int r = PI.value >> 1, t = PI.value & 1;
        dma16<(r % NB) * 8192 + t * 1024, ((r % 7) * 16 + t * 8) * DM * 2>(w2q[r / 7], rw2, ldsb + wm * 2048);
    };
    const unsigned wovo = w2vo + wm * (128 * DM * 2);
    auto dma_wo = [&](auto PI) { dma16<L2_A + PI.value * 1024, PI.value * 8 * DM * 2>(wovo, rwo, ldsb + wm * 16384); };
    unsigned cvo[WN == 1 ? NTB : 1];
    if constexpr (WN == 1) {
#pragma unroll
        for (int i = 0; i < NTB; ++i) {
            const int st = 2 * i + hh, k8 = 4 * wm + (st >= NTB ? 1 : 0), c = st >= NTB ? st - NTB : st;
            cvo[i] = (unsigned)(((k8 * 8 + ((lane >> 2) & 7)) * LDW + colb_of(4 + c) + (lane & 3) * 8) * 2);
        }
    }
    auto dma_chunk = [&](auto CI, auto II) {
        constexpr int c = CI.value, i = II.value;
        dma16<chunk_base(QKV, c) + i * 1024, (c * 128 + (i / NTB) * 16) * LDW * 2>(cvo[i % NTB], rw, ldsb + wm * (CP * 1024));
    };
    const int srow = mt * 128 + G.i16 * 8 + 2 * wm;
    const unsigned tag = (unsigned)__builtin_amdgcn_readfirstlane((int)epoch) + 1u;
    const unsigned trl = (4 * hh + q4) * 64 + blk * 32 + p4 * 8;
    ST(20);
    if constexpr (WN == 0) {
        LAS char* p = lds + L_UNIT + wm * 4096 + trl;
        const unsigned one = 0x3F803F80u;
        float su[2];
#pragma unroll
        for (int u = 0; u < 2; ++u) {
            float s = 0.f;
#pragma unroll
            for (int i = 0; i < 4; ++i) { u32x2 w = __builtin_bit_cast(u32x2, tr_read(p + u * 512 + i * 1024)); s = dot2(w[1], one, dot2(w[0], one, s)); }
            su[u] = s;
        }
        auto sw = __builtin_amdgcn_permlane32_swap(f2u(su[0]), f2u(su[1]), false, false);
        *(LAS float*)(lds + L_CSP + ((hh * 4 + wm) * 32 + r32) * 4) = u2f(sw[0]) + u2f(sw[1]);
        static_for<0, 8>([&](auto I) { dma_pair(IC(1), I); });
    } else {
        float mean[2], vv[2];
#pragma unroll
        for (int r = 0; r < 2; ++r) {
            float e[8];
#pragma unroll
            for (int i = 0; i < 4; ++i) { e[2 * i] = u2f(rowv[r][i] << 16); e[2 * i + 1] = u2f(rowv[r][i] & 0xFFFF0000u); }
            mean[r] = wave_sum(((e[0] + e[1]) + (e[2] + e[3])) + ((e[4] + e[5]) + (e[6] + e[7]))) * (1.f / DM);
            float q = 0.f;
#pragma unroll
            for (int i = 0; i < 8; ++i) { float d = e[i] - mean[r]; q = __builtin_fmaf(d, d, q); }
            vv[r] = wave_sum(q) * (1.f / DM) + 1e-5f;
        }
        u32x4 g = {f2u(lane ? mean[1] : mean[0]), tag, f2u(lane ? vv[1] : vv[0]), tag};
        if (lane < 2) __builtin_amdgcn_raw_buffer_store_b128(g, rs, WS_RST + (srow + lane) * 16, 0, SC1);
    }
    ST(22);

    LAS char* wb[2]; LAS char* xb[2][4];
#pragma unroll
    for (int s = 0; s < 2; ++s) {
        wb[s] = lds + s * 65536 + hh * 2048 + WN * 512 + q4 * 64 + blk * 32 + p4 * 8;
        PIN(wb[s]);
        const int row = 32 * wm + r32;
#pragma unroll
        for (int k = 0; k < 4; ++k) { xb[s][k] = lds + s * 65536 + XOFF + row * 128 + (((2 * k + hh) ^ ((row >> 1) & 7)) * 16); PIN(xb[s][k]); }
    }
    const int csbase = QKV ? nt * 192 : 1536 + nt * 224;

    f32x16 acc[2];
#pragma unroll
    for (int t = 0; t < 2; ++t)
#pragma unroll
        for (int i = 0; i < 16; ++i) acc[t][i] = 0.f;
    bf16x8 xf[32], wf[3][2];
    auto read_x = [&](int g) { const int sl = ((g >> 2) + 1) % 3; xf[g] = *(LAS bf16x8*)(xb[sl >> 1][g & 3] + (sl & 1) * SLOT); };
    auto read_w = [&](int g, int t) {
        const int sl = ((g >> 2) + 1) % 3;
        LAS char* p = wb[sl >> 1] + (sl & 1) * SLOT + (g & 3) * 4096 + t * 1024;
        wf[g % 3][t] = cat44(tr_read(p), tr_read(p + 256));
    };
    u32x4 gst, gcs; u32x2 gv2;
    const int mytile = wm < NG ? (wm < 2 ? 2 * wm + WN : (QKV ? 4 + WN : 2 + WN + wm)) : WN;
    const unsigned gvo_st = (unsigned)(mt * 128 + 32 * wm + r32) * 16;
    const unsigned gvo_v2 = (unsigned)(G.mt2 * 64 + (wave & 1) * 32 + r32) * 16;
    const unsigned gvo_cs = (unsigned)(csbase + mytile * 32 + r32) * 32 + hh * 16;
    auto gather = [&]() {
        ld16c<WS_RST>(gst, gvo_st, rs);
        ld8c<WS_RST + 8>(gv2, gvo_v2, rs);
        ld16c<WS_CSG>(gcs, gvo_cs, rs);
    };
    float vpe = 1.f, r_t = 1.f;
    bf16x8 fixT, fixF[NG];
    auto gather_check_and_fix = [&]() {
        for (unsigned spin = 0;; ++spin) {
            const unsigned bad = (gst[1] ^ tag) | (gst[3] ^ tag) | (gv2[1] ^ tag) | (gcs[1] ^ tag) | (gcs[3] ^ tag);
            if (__all(bad == 0u) || spin > SPIN_CAP) break;
            __builtin_amdgcn_s_sleep(1);
            gather();
            gather_wait<0>(gst, gv2, gcs);
        }
        vpe = u2f(gst[2]);
        vp2 = u2f(gv2[0]);
        r_t = __builtin_amdgcn_rsqf(vpe);
        unsigned s0, s1, s2;
        split3(hh ? vpe * r_t : -u2f(gst[0]), s0, s1, s2);
        u32x4 u;
        u[0] = hi2(s0, s1);
        u[1] = hh ? (s2 >> 16) : u[0];
        u[2] = hh ? 0u : hi2(s2, s0);
        u[3] = 0u;
        fixT = asbf(u);
        if (QKV) *(LAS float*)(lds + L_RR + (wave * 32 + r32) * 4) = r_t;
        unsigned c0, c1, c2;
        split3(half_sum(u2f(gcs[0]) + u2f(gcs[2])), c0, c1, c2);
        unsigned bb = 0;
        if (!QKV) bb = *(LAS unsigned short*)(lds + L_AUX + (ptile(mytile) * 32 + r32) * 2);
        u32x4 w;
        w[0] = hh ? (bb | (bb << 16)) : hi2(c0, c0);
        w[1] = hh ? bb : hi2(c1, c1);
        w[2] = hh ? 0u : hi2(c0, c2);
        w[3] = 0u;
        *(LAS u32x4*)(lds + L_FIX + wave * 1024 + lane * 16) = w;
    };

    wait_vm<WN == 0 ? 8 : 1>();
    wait_lgkm0();
    ST(23);
    barrier();
    ST(1);
#pragma unroll
    for (int g = 0; g < 2; ++g) { read_x(g); read_w(g, 0); read_w(g, 1); }
    if constexpr (WN == 1) {
        const int idx = 16 * wm + (lane & 15);
        LAS float* p = (LAS float*)(lds + L_CSP) + (idx >> 5) * 128 + (idx & 31);
        u32x2 gq = {f2u((p[0] + p[32]) + (p[64] + p[96])), tag};
        const int uc = 2 * (mt >> 2) + (wm >> 1);
        if (lane < 16 && uc < NU / 4) __builtin_amdgcn_raw_buffer_store_b64(gq, rs, WS_CSG + (csbase + uc * 32 + (idx & 31)) * 32 + (mt & 3) * 8, 0, SC1);
        if (wave == 4) {
            if (QKV) {
                const srd_t rp = mk_srd(a.wphi, NH * 256 * 2);
                dma16<L_AUX, 0>((unsigned)(nt * 2048 + lane * 16), rp, ldsb);
                dma16<L_AUX + 1024, 1024>((unsigned)(nt * 2048 + lane * 16), rp, ldsb);
            } else {
                const srd_t rb = mk_srd(a.b1, FF * 2);
                dma16<L_AUX, 0>((unsigned)(nt * 448 + lane * 16), rb, ldsb);
            }
        }
    }
    static_for<0, 32>([&](auto GI) {
        constexpr int g = GI.value, j = g >> 2;
        SCHED0();
        if constexpr ((g & 3) == 2) {
            constexpr int JG = WN == 0 ? 4 : 5;
            if constexpr (j == JG + 1) { PIN(acc[0]); PIN(acc[1]); }
            if constexpr (WN == 0) {
                constexpr int n = j <= 6 ? 8 : 63;
                if constexpr (j == JG + 1) gather_wait<n>(gst, gv2, gcs);
                else wait_vm<n>();
            } else if constexpr (j == 4) wait_vm<0>();
            else if constexpr (j == JG + 1) gather_wait<CP>(gst, gv2, gcs);
            else if constexpr (j == 7) wait_vm<CP>();
            wait_lgkm0();
            barrier();
            ST(2 + j);
            if constexpr (WN == 1 && j == 1) {
                const int gw = G.bid * 4 + wm;
                const char* p = lane < 14 ? (const char*)a.w2 + (size_t)(gw * 14 + lane) * 128 : (const char*)a.wo + (size_t)(gw * 4 + (lane & 3)) * 128;
                touch_ptr<L_DUMMY>(p, ldsb);
            }
            if constexpr (j == JG) gather();
            if constexpr (WN == 1 && j == 7) static_for<0, CP>([&](auto I) { dma_chunk(IC(3), I); });
            if constexpr (j == JG + 1) gather_check_and_fix();
            if constexpr (j == 7) {
#pragma unroll
                for (int t = 0; t < NG; ++t) fixF[t] = *(LAS bf16x8*)(lds + L_FIX + (WN * 4 + t) * 1024 + lane * 16);
            }
            SCHED0();
        }
        constexpr int jb = (g - 2) >> 2, q = (g - 2) & 3;
        static_for<0, 2>([&](auto TI) {
            constexpr int t = TI.value;
            if (QKV && t == 1) acc[t] = MFMA32(xf[g], wf[g % 3][t], acc[t]);
            else acc[t] = MFMA32(wf[g % 3][t], xf[g], acc[t]);
            if constexpr (g + 2 < 32) {
                if (t == 0) read_x(g + 2);
                read_w(g + 2, t);
            }
            constexpr int i = q * 2 + t;
            if constexpr (WN == 0) {
                if constexpr (g < 2) { dma_pair(IC(2), IC(4 * g + 2 * t)); dma_pair(IC(2), IC(4 * g + 2 * t + 1)); }
                else if constexpr (jb <= 4) dma_pair(IC(jb + 3), IC(i));
                else if constexpr (jb == 5) dma_w2(IC(i));
                else if constexpr (jb == 6 && i < 2) dma_w2(IC(8 + i));
            } else if constexpr (g >= 2 && jb >= 4 && jb <= 6 && i < CP) dma_chunk(IC(jb - 4), IC(i));
            SCHED0();
        });
    });
    ST(10);
    const int tt = mt * 4 + wm;
    f32x16 zero16;
#pragma unroll
    for (int i = 0; i < 16; ++i) zero16[i] = 0.f;
    auto store_h = [&](f32x16& c, int tile) {
#pragma unroll
        for (int i = 0; i < 16; ++i) { float v = fmx(c[i], 0.f); c[i] = v * v; }
        const unsigned off = WS_H + (unsigned)((tt * 112 + nt * 14 + ptile(tile) * 2) * 64 + lane) * 16;
        __builtin_amdgcn_raw_buffer_store_b128(pack8u<0>(c), rs, off, 0, SC1);
        __builtin_amdgcn_raw_buffer_store_b128(pack8u<1>(c), rs, off + 1024, 0, SC1);
    };
    bf16x8 wfr[2];
    const float kscale = r_t * LOG2E;
    auto feature_map2 = [&](f32x16 (&p)[2], u32x4 (&F)[2][2]) {
        float m[2][2], sm[2][2];
#pragma unroll
        for (int X = 0; X < 2; ++X)
#pragma unroll
            for (int h = 0; h < 2; ++h) {
                const int o = 8 * h;
                m[X][h] = fmx(fmx(fmx(fmx(p[X][o], p[X][o + 1]), p[X][o + 2]), fmx(fmx(p[X][o + 3], p[X][o + 4]), p[X][o + 5])), fmx(p[X][o + 6], p[X][o + 7]));
            }
#pragma unroll
        for (int X = 0; X < 2; ++X) {
            auto s = __builtin_amdgcn_permlane32_swap(f2u(m[X][0]), f2u(m[X][1]), false, false);
            float c = -kscale * fmx(u2f(s[0]), u2f(s[1]));
            auto s2 = __builtin_amdgcn_permlane32_swap(f2u(c), f2u(c), false, false);
            m[X][0] = u2f(s2[0]); m[X][1] = u2f(s2[1]);
        }
#pragma unroll
        for (int i = 0; i < 16; ++i)
#pragma unroll
            for (int X = 0; X < 2; ++X) p[X][i] = __builtin_amdgcn_exp2f(__builtin_fmaf(p[X][i], kscale, m[X][i >> 3]));
#pragma unroll
        for (int X = 0; X < 2; ++X)
#pragma unroll
            for (int h = 0; h < 2; ++h) {
                const int o = 8 * h;
                sm[X][h] = ((p[X][o] + p[X][o + 1]) + (p[X][o + 2] + p[X][o + 3])) + ((p[X][o + 4] + p[X][o + 5]) + (p[X][o + 6] + p[X][o + 7]));
            }
#pragma unroll
        for (int X = 0; X < 2; ++X) {
            auto s = __builtin_amdgcn_permlane32_swap(f2u(sm[X][0]), f2u(sm[X][1]), false, false);
            float c = __builtin_amdgcn_rcpf(u2f(s[0]) + u2f(s[1]));
            auto s2 = __builtin_amdgcn_permlane32_swap(f2u(c), f2u(c), false, false);
            sm[X][0] = u2f(s2[0]); sm[X][1] = u2f(s2[1]);
        }
#pragma unroll
        for (int X = 0; X < 2; ++X) {
#pragma unroll
            for (int i = 0; i < 16; ++i) p[X][i] *= sm[X][i >> 3];
            F[X][0] = pack8u<0>(p[X]); F[X][1] = pack8u<1>(p[X]);
        }
    };
    if constexpr (!QKV) {
        acc[0] = MFMA32(fixF[0], fixT, acc[0]);
        acc[1] = MFMA32(fixF[1], fixT, acc[1]);
        store_h(acc[0], WN);
        store_h(acc[1], 2 + WN);
    } else {
        acc[0] = MFMA32(fixF[0], fixT, acc[0]);
        acc[1] = MFMA32(fixT, fixF[1], acc[1]);
        bf16x8 idf[2];
#pragma unroll
        for (int s = 0; s < 2; ++s) {
            u32x4 u;
#pragma unroll
            for (int i = 0; i < 4; ++i) {
                int f0 = 16 * s + 8 * (i >> 1) + 4 * hh + 2 * (i & 1);
                u[i] = (r32 == f0 ? 0x3F80u : 0u) | (r32 == f0 + 1 ? 0x3F800000u : 0u);
            }
            idf[s] = asbf(u);
        }
#pragma unroll
        for (int X = 0; X < 2; ++X) {
            LAS char* p = lds + L_AUX + (2 * WN + X) * 512 + (4 * hh + q4) * 32 + p4 * 8;
            u32x4 u = __builtin_bit_cast(u32x4, cat44(tr_read(p), tr_read(p + 256)));
            const unsigned sg = blk ? 0x80008000u : 0u;
#pragma unroll
            for (int i = 0; i < 4; ++i) u[i] ^= sg;
            wfr[X] = asbf(u);
        }
#pragma unroll
        for (int g = 0; g < 4; ++g) {
            f32x4 rr = *(LAS f32x4*)(lds + L_RR + (wave * 32 + 8 * g + 4 * hh) * 4);
#pragma unroll
            for (int i = 0; i < 4; ++i) acc[1][4 * g + i] *= rr[i];
        }
        const u32x4 v0 = pack8u<0>(acc[1]), v1 = pack8u<1>(acc[1]);
        const bf16x8 kb0 = pack8<0>(acc[0]), kb1 = pack8<1>(acc[0]);
        {
            f32x16 p[2], d[2], kv[2];
            u32x4 F[2][2];
            p[0] = MFMA32(wfr[0], kb0, zero16);
            p[1] = MFMA32(wfr[1], kb1, zero16);
            feature_map2(p, F);
#pragma unroll
            for (int X = 0; X < 2; ++X) d[X] = MFMA32(asbf(F[X][0]), idf[0], zero16);
#pragma unroll
            for (int X = 0; X < 2; ++X) d[X] = MFMA32(asbf(F[X][1]), idf[1], d[X]);
            u32x4 b[2][2];
#pragma unroll
            for (int X = 0; X < 2; ++X)
#pragma unroll
                for (int i = 0; i < 4; ++i) { b[X][0][i] = (blk == X) ? v0[i] : 0x3F803F80u; b[X][1][i] = (blk == X) ? v1[i] : 0x3F803F80u; }
#pragma unroll
            for (int X = 0; X < 2; ++X) kv[X] = MFMA32(pack8<0>(d[X]), asbf(b[X][0]), zero16);
#pragma unroll
            for (int X = 0; X < 2; ++X) kv[X] = MFMA32(pack8<1>(d[X]), asbf(b[X][1]), kv[X]);
#pragma unroll
            for (int X = 0; X < 2; ++X) {
                const int row = X == 0 ? r32 : (r32 == 15 ? 16 : r32 - 16);
                const bool use = X == 0 ? (r32 <= 16) : (r32 >= 15);
                LAS char* dst = lds + (use ? L_RED + (((WN * 2 + X) * 4 + wm) * 17 + row) * 144 : L_RED + 39168 + (r32 & 7) * 144) + hh * 16;
#pragma unroll
                for (int g = 0; g < 4; ++g) {
                    f32x4 v = {kv[X][4 * g], kv[X][4 * g + 1], kv[X][4 * g + 2], kv[X][4 * g + 3]};
                    *(LAS f32x4*)(dst + g * 32) = v;
                }
            }
        }
        wait_lgkm0();
        ST(26);
        barrier();
        if constexpr (WN == 1) {
#pragma unroll
            for (int it = 0; it < 5; ++it) {
                const int o = it * 64 + lane;
                if (it < 4 || lane < 16) {
                    LAS char* src = lds + L_RED + (wm * 4 * 17 + (o >> 4)) * 144 + (o & 15) * 8;
                    f32x2 v = (*(LAS f32x2*)(src) + *(LAS f32x2*)(src + 2448)) + (*(LAS f32x2*)(src + 2 * 2448) + *(LAS f32x2*)(src + 3 * 2448));
                    u32x4 g0 = {f2u(v[0]), tag, f2u(v[1]), tag};
                    __builtin_amdgcn_raw_buffer_store_b128(g0, rs, WS_KVP + (unsigned)((mt * 32 + nt * 4 + wm) * 272 + o) * 16, 0, SC1);
                }
            }
        }
    }
    ST(27);
    f32x16 bc[NB2];
#pragma unroll
    for (int t = 0; t < NB2; ++t) bc[t] = MFMA32(fixF[2 + t], fixT, zero16);
    {
        constexpr int CB0 = QKV ? 81920 : 40960;
        LAS char* q0 = lds + hh * (NTB * 512) + WN * 512 + q4 * 64 + blk * 32 + p4 * 8 + CB0;
        LAS char* q1 = q0 + (98304 - CB0);
        PIN(q0); PIN(q1);
        bf16x8 qf[4][NB2];
        auto read_q = [&](int g, int t) {
            const int cb = chunk_base(QKV, g >> 3);
            LAS char* p = (cb >= 98304 ? q1 + (cb - 98304) : q0 + (cb - CB0)) + (g & 7) * (NTB * 1024) + t * 512;
            qf[g & 3][t] = cat44(tr_read(p), tr_read(p + 256));
        };
#pragma unroll
        for (int g = 0; g < 3; ++g)
#pragma unroll
            for (int t = 0; t < NB2; ++t) read_q(g, t);
        static_for<0, 32>([&](auto GI) {
            constexpr int g = GI.value;
            SCHED0();
            if constexpr (g == 8 || g == 16 || g == 24) {
                if constexpr (g == 8 && WN == 1) wait_vm<QKV ? 5 : 4>();
                wait_lgkm0(); barrier();
            }
            static_for<0, NB2>([&](auto TI) {
                constexpr int t = TI.value;
                bc[t] = MFMA32(qf[g & 3][t], xf[g], bc[t]);
                if constexpr (g + 3 < 32) read_q(g + 3, t);
                if constexpr (WN == 0) {
                    if constexpr (QKV && g >= 8 && g < 18) dma_w2(IC(10 + g - 8));
                    if constexpr (!QKV && g >= 24 && g < 30) dma_w2(IC(10 + g - 24));
                } else if constexpr (!QKV && g >= 16 && g < 24) dma_wo(IC(2 * (g - 16) + t));
                else if constexpr (QKV && g >= 9 && g < 25) { if (wm >= 2) dma_wo(IC(g - 9)); }
                SCHED0();
            });
        });
    }
    ST(11);
    auto w2_to_regs = [&]() {
        LAS char* rdA = lds + wm * 2048 + trl;
#pragma unroll
        for (int s = 0; s < NR; ++s) {
            LAS char* p = rdA + s * 8192;
            ph[2 * s] = __builtin_bit_cast(u32x4, cat44(tr_read(p), tr_read(p + 1024)));
            ph[2 * s + 1] = __builtin_bit_cast(u32x4, cat44(tr_read(p + 512), tr_read(p + 1536)));
        }
        wait_lgkm0();
    };
    if constexpr (!QKV) {
#pragma unroll
        for (int t = 0; t < NB2; ++t) store_h(bc[t], 4 + WN + t);
        ST(12);
        if constexpr (WN == 0) {
            wait_vm<2>();
            w2_to_regs();
            static_for<2 * NB, 2 * NB + 8>([&](auto P) { dma_w2(P); });
            wait_vm<8>();
            barrier();
        } else {
            if (wm >= 2) {
                const int sl = (mt * 8 + nt) * 2 + (wm & 1);
                const unsigned vo = (unsigned)(((sl >> 3) * 136 + (sl & 7) * 17 + (lane < 34 ? (lane >> 1) : 0)) * 32 + (lane & 1) * 16);
                static_for<0, 16>([&](auto M) { ld16c<WS_KVP + M.value * 32 * 136 * 32>(ph[M.value], vo, rs); });
                wait_vm<16>();
                barrier();
                wait16(ph);
                ph[0][0] = kv_reduce_publish(a.ws, ph, sl, lane, tag) ? 1u : 0u;
            } else {
                if (wave == 4 && lane < 16) *(LAS unsigned*)(lds + L2_CNT + lane * 4) = 0u;
                wait_vm<0>();
                wait_lgkm0();
                barrier();
                if (wave == 5 && lane == 0)
                    __hip_atomic_store((GAS unsigned*)(a.ws + WS_FLAG + (mt * 16 + 8 + nt) * 64), tag, RLX_AGENT);
            }
        }
    } else {
        const bf16x8 qb0 = pack8<0>(bc[0]), qb1 = pack8<1>(bc[0]);
        if constexpr (WN == 0) {
            wait_vm<0>(); w2_to_regs();
            static_for<2 * NB, 2 * NB + 8>([&](auto P) { dma_w2(P); });
        }
        {
            f32x16 p[2];
            u32x4 F[2][2];
            p[0] = MFMA32(wfr[0], qb0, zero16);
            p[1] = MFMA32(wfr[1], qb1, zero16);
            feature_map2(p, F);
#pragma unroll
            for (int X = 0; X < 2; ++X) {
                LAS char* dst = lds + L_PHI + ((2 * WN + X) * 4 + wm) * 2048 + lane * 16;
                *(LAS u32x4*)(dst) = F[X][0];
                *(LAS u32x4*)(dst + 1024) = F[X][1];
            }
        }
        ST(12);
        wait_lgkm0();
        barrier();
        if constexpr (WN == 1) {
            if (wave == 4 && lane < 16) *(LAS unsigned*)(lds + L2_CNT + lane * 4) = 0u;
#pragma unroll
            for (int i = 0; i < 8; ++i) ph[i] = *(LAS u32x4*)(lds + L_PHI + wm * 8192 + i * 1024 + lane * 16);
            wait_lgkm0();
        }
        barrier();
    }
    ST(13);
    return tag;
}

#define HPV 6
constexpr int HP = HPV;
constexpr int w_at(int i) { return i >= NR && i + NB <= 27 ? i + NB : -1; }
constexpr int p2_pos(bool isw, int idx) {
    int pos = 0;
    for (int s = 0; s < HP; ++s) { pos += 2; if (!isw && idx == s) return pos; }
    for (int i = 1; i < 28; ++i) {
        if (i - 1 + HP <= 27) { pos += 2; if (!isw && idx == i - 1 + HP) return pos; }
        if (w_at(i - 1) >= 0) { pos += 2; if (isw && idx == w_at(i - 1)) return pos; }
    }
    return 0;
}
constexpr int p2_issued(int step) {
    int pos = 2 * HP;
    for (int i = 1; i < step; ++i) { if (i - 1 + HP <= 27) pos += 2; if (w_at(i - 1) >= 0) pos += 2; }
    return pos;
}
constexpr int p2_wait(int i) {
    int need = p2_pos(false, i);
    if (i + 1 >= NR + NB && i + 1 <= 27) { int w = p2_pos(true, i + 1); if (w > need) need = w; }
    return p2_issued(i) - need;
}
template <int SLEEP> DI void poll_words(srd_t rs, unsigned voff, unsigned tag) {
    for (unsigned spin = 0; spin <= SPIN_CAP; ++spin) {
        unsigned v;
        ld4c<0>(v, voff, rs);
        asm volatile("s_waitcnt vmcnt(0) ; PINW %0" : "+v"(v) :: "memory");
        if (__all(v == tag)) break;
        __builtin_amdgcn_s_sleep(SLEEP);
    }
}
DI void lds_wait_word(LAS char* p) {
    for (unsigned spin = 0; spin <= 16 * SPIN_CAP; ++spin) {
        if (__hip_atomic_load((LAS unsigned*)p, __ATOMIC_RELAXED, __HIP_MEMORY_SCOPE_WORKGROUP) != 0u) break;
        __builtin_amdgcn_s_sleep(1);
    }
    CFENCE();
}
DI void lds_set_word(LAS char* p) { __hip_atomic_store((LAS unsigned*)p, 1u, __ATOMIC_RELAXED, __HIP_MEMORY_SCOPE_WORKGROUP); }

struct Fin { f32x16 acc[2][2]; u32x2 xr0, xr1, br0, br1; };
template <int PART>
DI void phase2(const Args& a, LAS char* lds, const Geo& G, unsigned tag, u32x4 (&ph)[16], float vp2, Fin& F, unsigned long long* stamps) {
    const int lane = G.lane, wave = G.wave, mt = G.mt2, nt = G.nt2;
    const unsigned ldsb = (unsigned)(size_t)lds;
    const int hh = lane >> 5, r32 = lane & 31, blk = (lane >> 4) & 1, q4 = (lane & 15) >> 2, p4 = lane & 3;
    const int kq = wave & 3, hf = wave >> 2;
    const srd_t rs = mk_srd(a.ws, WS_END);
    const srd_t rw2 = mk_srd(a.w2, FF * DM * 2);
    const unsigned wvo = (unsigned)((((lane >> 2) & 7) * DM + nt * 64 + hh * 32 + (lane & 3) * 8) * 2);
    const unsigned trl = (4 * hh + q4) * 64 + blk * 32 + p4 * 8;
    const unsigned l16 = lane * 16;
    unsigned w2q[4];
#pragma unroll
    for (int q = 0; q < 4; ++q) w2q[q] = wvo + (unsigned)((kq * 448 + ((q + G.rq) & 3) * 112) * DM * 2);
    const int token = mt * 64 + (kq & 1) * 32 + r32;
    const int col0 = nt * 64 + (kq >> 1) * 32 + 16 * hf + 4 * hh;
    u32x2 &xr0 = F.xr0, &xr1 = F.xr1, &br0 = F.br0, &br1 = F.br1;
    auto final_loads = [&]() {
        const srd_t rx = mk_srd(a.x, SEQ * DM * 2), rb = mk_srd(a.b2, DM * 2);
        ld8<0>(xr0, (unsigned)(token * DM + col0) * 2, rx); ld8<16>(xr1, (unsigned)(token * DM + col0) * 2, rx);
        ld8<0>(br0, (unsigned)col0 * 2, rb); ld8<16>(br1, (unsigned)col0 * 2, rb);
    };
    f32x16 (&acc)[2][2] = F.acc;
    if constexpr (PART < 2) {
#pragma unroll
        for (int i = 0; i < 2; ++i)
#pragma unroll
            for (int j = 0; j < 2; ++j)
#pragma unroll
                for (int e = 0; e < 16; ++e) acc[i][j][e] = 0.f;
    }
    LAS char* upper = lds + L2_A + kq * 16384;
    if constexpr (PART == 0) {
        const unsigned lb = ldsb + kq * 2048;
        unsigned hq[4];
#pragma unroll
        for (int q = 0; q < 4; ++q) hq[q] = (unsigned)(((2 * mt) * 112 + 28 * kq + ((q + G.rq) & 3) * 7) * 1024) + l16;
        auto issue_w = [&](auto SI) {
            constexpr int s = SI.value;
            dma16<(s % NB) * 8192, (s % 7) * 16 * DM * 2>(w2q[s / 7], rw2, lb);
            dma16<(s % NB) * 8192 + 1024, ((s % 7) * 16 + 8) * DM * 2>(w2q[s / 7], rw2, lb);
        };
        u32x4 hb[HP][2];
        if (!G.qkv) { issue_w(IC(8)); issue_w(IC(9)); }
        issue_w(IC(10));
        static_for<NB + 4, NR + NB>([&](auto SI) { issue_w(SI); });
        final_loads();
        LAS char* rdA = lds + kq * 2048 + trl;
        LAS char* rdB = rdA + 6 * 8192;
        PIN(rdB);
        bf16x8 wf[2][2];
        auto rd = [&](int s, bf16x8 (&d)[2]) {
            LAS char* p = (s % NB) < 6 ? rdA + (s % NB) * 8192 : rdB + (s % NB - 6) * 8192;
            d[0] = cat44(tr_read(p), tr_read(p + 1024));
            d[1] = cat44(tr_read(p + 512), tr_read(p + 1536));
        };
        ST(24);
        lds_wait_word(lds + L2_CNT + 16);
        ST(14);
        static_for<0, HP>([&](auto SI) {
            constexpr int s = SI.value;
            ld16c<WS_H + (s % 7) * 1024>(hb[s % HP][0], hq[s / 7], rs);
            ld16c<WS_H + (112 + s % 7) * 1024>(hb[s % HP][1], hq[s / 7], rs);
        });
        static_for<0, 28>([&](auto SI) {
            constexpr int s = SI.value;
            SCHED0();
            asm volatile("s_waitcnt vmcnt(%2) ; PINW %0 %1" : "+v"(hb[s % HP][0]), "+v"(hb[s % HP][1]) : "i"(p2_wait(s)) : "memory");
            if constexpr (s == 0) ST(15);
            static_for<0, 4>([&](auto MI) {
                constexpr int m = MI.value, i = m >> 1, j = m & 1;
                acc[i][j] = MFMA32(s < NR ? asbf(ph[s < NR ? 2 * s + i : 0]) : wf[s & 1][i], asbf(hb[s % HP][j]), acc[i][j]);
                SCHED0();
                if constexpr (m == 0 && s + 1 >= NR && s + 1 < 28) rd(s + 1, wf[(s + 1) & 1]);
                constexpr int hs = s - 1 + HP;
                constexpr int ws = s >= 1 ? w_at(s - 1) : -1, wz = ws >= 0 ? ws : 0;
                if constexpr (s >= 1 && hs <= 27 && m < 2) ld16c<WS_H + (m * 112 + hs % 7) * 1024>(hb[hs % HP][m], hq[hs / 7], rs);
                if constexpr (ws >= 0 && m >= 2) dma16<(wz % NB) * 8192 + (m - 2) * 1024, ((wz % 7) * 16 + (m - 2) * 8) * DM * 2>(w2q[wz / 7], rw2, lb);
                SCHED0();
            });
        });
        SCHED0();
        ST(16);
        wait_lgkm0();
#pragma unroll
        for (int p = 0; p < 16; ++p) {
            const f32x16& c = acc[p >> 3][(p >> 2) & 1];
            f32x4 v = {c[4 * (p & 3)], c[4 * (p & 3) + 1], c[4 * (p & 3) + 2], c[4 * (p & 3) + 3]};
            *(LAS f32x4*)(lds + (p >> 1) * 8192 + kq * 2048 + (p & 1) * 1024 + l16) = v;
        }
        asm volatile("s_waitcnt vmcnt(0) ; PINW %0 %1 %2 %3" : "+v"(xr0), "+v"(xr1), "+v"(br0), "+v"(br1) :: "memory");
    } else if constexpr (PART == 1) {
        const srd_t rwo = mk_srd(a.wo, DM * DM * 2);
        __builtin_amdgcn_s_setprio(3);
        bf16x8 wv[8][2];
        auto wo_on = [&]() {
#pragma unroll
            for (int h = 0; h < 8; ++h) {
                LAS char* p = upper + h * 2048 + trl;
                wv[h][0] = cat44(tr_read(p), tr_read(p + 1024));
                wv[h][1] = cat44(tr_read(p + 512), tr_read(p + 1536));
            }
        };
        auto poll_h = [&]() {
            poll_words<1>(rs, WS_FLAG + (unsigned)((mt >> 1) * 16 + 8 + (lane & 7)) * 64, tag);
            if (lane == 0) lds_set_word(lds + L2_CNT + 16);
        };
        if (!G.qkv) {
            if (kq == 0) poll_h();
            if (kq >= 2 && __builtin_amdgcn_readfirstlane((int)ph[0][0]) == 0) {
                const int sl = (G.mt * 8 + G.nt) * 2 + (kq & 1);
                const unsigned vo = (unsigned)(((sl >> 3) * 136 + (sl & 7) * 17 + (lane < 34 ? (lane >> 1) : 0)) * 32 + (lane & 1) * 16);
                for (unsigned spin = 0; spin <= SPIN_CAP; ++spin) {
                    static_for<0, 16>([&](auto M) { ld16c<WS_KVP + M.value * 32 * 136 * 32>(ph[M.value], vo, rs); });
                    wait16(ph);
                    if (kv_reduce_publish(a.ws, ph, sl, lane, tag)) break;
                    __builtin_amdgcn_s_sleep(2);
                }
            }
            ST(24);
        } else {
            const unsigned kvo = (unsigned)((4 * G.nt + kq) * 2176 + hh * 272 + (r32 < 16 ? r32 : 16) * 16);
            const unsigned hfo = WS_FLAG + (unsigned)((mt >> 1) * 16 + 8 + (lane & 7)) * 64;
            const unsigned wovo = wvo + kq * (128 * DM * 2);
            u32x4 raw[4]; unsigned fl;
            bool hdone = kq != 2;
            auto request = [&]() { static_for<0, 4>([&](auto I) { ld16c<WS_KVR + I.value * 544>(raw[I.value], kvo, rs); }); ld4c<0>(fl, hfo, rs); };
            if (kq < 2) static_for<0, 16>([&](auto P) { dma16<L2_A + P.value * 1024, P.value * 8 * DM * 2>(wovo, rwo, ldsb + kq * 16384); });
            request();
            asm volatile("s_waitcnt vmcnt(0) ; PINW %0 %1 %2 %3 %4" : "+v"(raw[0]), "+v"(raw[1]), "+v"(raw[2]), "+v"(raw[3]), "+v"(fl) :: "memory");
            for (unsigned spin = 0; spin <= SPIN_CAP; ++spin) {
                if (!hdone && __all(fl == tag)) { hdone = true; if (lane == 0) lds_set_word(lds + L2_CNT + 16); }
                unsigned bad = 0;
#pragma unroll
                for (int i = 0; i < 4; ++i) bad |= (raw[i][1] ^ tag) | (raw[i][3] ^ tag);
                if (__all(bad == 0u)) break;
                __builtin_amdgcn_s_sleep(1);
                request();
                asm volatile("s_waitcnt vmcnt(0) ; PINW %0 %1 %2 %3 %4" : "+v"(raw[0]), "+v"(raw[1]), "+v"(raw[2]), "+v"(raw[3]), "+v"(fl) :: "memory");
            }
            ST(24);
            f32x16 zero16;
#pragma unroll
            for (int i = 0; i < 16; ++i) zero16[i] = 0.f;
            const u32x4 k0 = {raw[0][0], raw[0][2], raw[1][0], raw[1][2]}, k1 = {raw[2][0], raw[2][2], raw[3][0], raw[3][2]};
#pragma unroll
            for (int w = 0; w < 4; ++w) {
                f32x16 nd = MFMA32(asbf(k0), asbf(ph[2 * w]), zero16);
                nd = MFMA32(asbf(k1), asbf(ph[2 * w + 1]), nd);
                const float inv = __builtin_amdgcn_rcpf(nd[8] + 1e-6f);
#pragma unroll
                for (int e = 0; e < 8; ++e) nd[e] *= inv;
                __builtin_amdgcn_raw_buffer_store_b128(pack8u<0>(nd), rs, WS_O + (unsigned)(((G.mt * 4 + w) * 32 + G.nt * 4 + kq) * 1024) + l16, 0, SC1);
            }
            wait_vm<0>();
            unsigned old = 0;
            if (lane == 0) old = __hip_atomic_fetch_add((LAS unsigned*)(lds + L2_CNT), 1u, __ATOMIC_RELAXED, __HIP_MEMORY_SCOPE_WORKGROUP);
            if (lane == 0 && old == 3u) __hip_atomic_store((GAS unsigned*)(a.ws + WS_FLAG + (G.mt * 16 + G.nt) * 64), tag, RLX_AGENT);
            ST(25);
            if (!hdone) poll_h();
            __builtin_amdgcn_s_setprio(0);
        }
        __builtin_amdgcn_s_setprio(0);
        final_loads();
        poll_words<4>(rs, WS_FLAG + (unsigned)((mt >> 1) * 16 + 2 * kq + (lane & 1)) * 64, tag);
        __builtin_amdgcn_s_setprio(OPRIO);
        ST(14);
        const unsigned ovo = (unsigned)((2 * mt) * 32 + 8 * kq) * 1024 + l16;
        static_for<0, 16>([&](auto I) { constexpr int i = I.value; ld16c<WS_O + (i & 1) * 32768 + (i >> 1) * 1024>(ph[i], ovo, rs); });
        wo_on();
        wait16(ph);
        ST(15);
#pragma unroll
        for (int h = 0; h < 8; ++h)
#pragma unroll
            for (int i = 0; i < 2; ++i)
#pragma unroll
                for (int j = 0; j < 2; ++j) acc[i][j] = MFMA32(wv[h][i], asbf(ph[2 * h + j]), acc[i][j]);
        ST(16);
        wait_lgkm0();
#pragma unroll
        for (int p = 0; p < 16; ++p) {
            const f32x16& c = acc[p >> 3][(p >> 2) & 1];
            f32x4 v = {c[4 * (p & 3)], c[4 * (p & 3) + 1], c[4 * (p & 3) + 2], c[4 * (p & 3) + 3]};
            *(LAS f32x4*)(upper + p * 1024 + l16) = v;
        }
        asm volatile("s_waitcnt vmcnt(0) ; PINW %0 %1 %2 %3" : "+v"(xr0), "+v"(xr1), "+v"(br0), "+v"(br1) :: "memory");
    } else {
    wait_lgkm0();
    barrier();
    ST(17);
    const float r2 = __builtin_amdgcn_rcpf(vp2);
#pragma unroll
    for (int g = 0; g < 2; ++g) {
        LAS char* pa = lds + L2_A + (kq * 4 + 2 * hf + g) * 1024 + l16;
        LAS char* pf = lds + (kq * 2 + hf) * 8192 + g * 1024 + l16;
        f32x4 ss = (*(LAS f32x4*)(pa) + *(LAS f32x4*)(pa + 16384)) + (*(LAS f32x4*)(pa + 2 * 16384) + *(LAS f32x4*)(pa + 3 * 16384));
        f32x4 so = (*(LAS f32x4*)(pf) + *(LAS f32x4*)(pf + 2048)) + (*(LAS f32x4*)(pf + 4096) + *(LAS f32x4*)(pf + 6144));
        const u32x2 xr = g ? xr1 : xr0, br = g ? br1 : br0;
        float res[4];
#pragma unroll
        for (int i = 0; i < 4; ++i) {
            const float xv = u2f((i & 1) ? (xr[i >> 1] & 0xFFFF0000u) : (xr[i >> 1] << 16));
            const float bv = u2f((i & 1) ? (br[i >> 1] & 0xFFFF0000u) : (br[i >> 1] << 16));
            const float o = so[i] * r2 + bv;
            const float gt = o * __builtin_amdgcn_rcpf(1.f + __builtin_amdgcn_exp2f(-o * LOG2E));
            res[i] = xv + ss[i] * gt;
        }
        u32x2 ov = {pk2(res[0], res[1]), pk2(res[2], res[3])};
        *(GAS u32x2*)(a.out + (size_t)token * DM + col0 + 8 * g) = ov;
    }
    ST(18);
    if (G.bid == 0 && wave == 7) {
        for (unsigned spin = 0; spin <= SPIN_CAP; ++spin) {
            unsigned f0, f1, f2, f3;
            ld4c<WS_FLAG>(f0, (unsigned)lane * 64, rs); ld4c<WS_FLAG + 4096>(f1, (unsigned)lane * 64, rs);
            ld4c<WS_FLAG + 8192>(f2, (unsigned)lane * 64, rs); ld4c<WS_FLAG + 12288>(f3, (unsigned)lane * 64, rs);
            asm volatile("s_waitcnt vmcnt(0) ; PINW %0 %1 %2 %3" : "+v"(f0), "+v"(f1), "+v"(f2), "+v"(f3) :: "memory");
            if (__all(f0 == tag && f1 == tag && f2 == tag && f3 == tag)) break;
            __builtin_amdgcn_s_sleep(1);
        }
        if (lane == 0) __hip_atomic_store((GAS unsigned*)(a.ws + WS_CTRL), tag, RLX_AGENT);
    }
    }
}

__global__ void __launch_bounds__(512, 2) fused_kernel(Args a) {
    extern __shared__ __attribute__((aligned(16))) char smem[];
    LAS char* lds = (LAS char*)smem;
    unsigned long long stamps[28];
    ST(0);
    Geo G;
    G.bid = blockIdx.x;
    G.wave = __builtin_amdgcn_readfirstlane(threadIdx.x >> 6);
    G.lane = threadIdx.x & 63;
    const int xcd = G.bid & 7, loc = G.bid >> 3;
    const int sel = loc >> 2;
    G.mt = (xcd >> 1) * 4 + (loc & 3);
    G.nt = (xcd & 1) * 4 + (sel & 3);
    G.i16 = (xcd & 1) * 8 + sel;
    G.mt2 = G.mt * 2 + (xcd & 1);
    G.nt2 = sel;
    G.qkv = sel < 4;
#define RQ (sel & 3)
    G.rq = RQ;
    if (G.wave == 3) {
        unsigned long long pc;
        asm volatile("s_getpc_b64 %0" : "=s"(pc));
        const unsigned line = (unsigned)(loc + G.lane * 32);
        touch_ptr<L_DUMMY>((const char*)(pc & ~127ull) + (size_t)(line < CODE_LINES ? line : 0) * 128, (unsigned)(size_t)lds);
    }
    unsigned wvo0[4], xvo0[4], epoch;
    u32x4 rowv[2] = {u32x4{0u, 0u, 0u, 0u}, u32x4{0u, 0u, 0u, 0u}};
    {
        const int lane = G.lane, wm = G.wave & 3, hh = lane >> 5;
        const unsigned ldsb = (unsigned)(size_t)lds;
        const int LDW = G.qkv ? 1536 : 1792;
        const srd_t rx = mk_srd(a.x, SEQ * DM * 2);
        const srd_t rw = mk_srd(G.qkv ? a.wqkv : a.w1, DM * LDW * 2);
        ld4c<WS_CTRL>(epoch, 0u, mk_srd(a.ws, WS_END));
        if (G.wave >= 4) {
            const unsigned ro = (unsigned)((G.mt * 128 + G.i16 * 8 + 2 * wm) * DM * 2 + lane * 16);
            ld16(rowv[0], ro, rx); ld16(rowv[1], ro + 1024, rx);
        } else {
            const int c = 2 * (G.mt >> 2) + hh, pc = c + (G.nt & 1);
            const int colb = G.qkv ? ((((c >> 1) + 1) % 3) * 512 + 64 * G.nt + 32 * hh) : (224 * G.nt + 32 * (pc >= 7 ? pc - 7 : pc));
            const unsigned uo = c < (G.qkv ? 6 : 7) ? (unsigned)((((G.mt & 3) * 128 + 32 * wm + ((lane >> 2) & 7)) * LDW + colb + (lane & 3) * 8) * 2) : 0x7F000000u;
#pragma unroll
            for (int i = 0; i < 4; ++i) dma16r(uo + (unsigned)(i * 8 * LDW * 2), rw, ldsb + L_UNIT + wm * 4096 + i * 1024);
        }
#pragma unroll
        for (int i = 0; i < 4; ++i) {
            const int row = 8 * (4 * wm + i) + (lane >> 3);
            xvo0[i] = (unsigned)(((G.mt * 128 + row) * DM + ((lane & 7) ^ ((row >> 1) & 7)) * 8) * 2);
            const int k8 = 2 * wm + (i >> 1), c = 2 * (i & 1) + hh;
            const int colb = G.qkv ? (((c >> 1) + 1) * 512 + 64 * G.nt + 32 * (c & 1)) : (224 * G.nt + 32 * (c + (G.nt & 1)));
            wvo0[i] = (unsigned)(((k8 * 8 + ((lane >> 2) & 7)) * LDW + colb + (lane & 3) * 8) * 2);
        }
        if (G.wave >= 4) {
            static_for<0, 4>([&](auto I) { dma16<SLOT + XOFF + I.value * 1024, 0>(xvo0[I.value], rx, ldsb + wm * 4096); });
        } else {
            static_for<0, 4>([&](auto I) { dma16<SLOT + I.value * 1024, 0>(wvo0[I.value], rw, ldsb + wm * 4096); });
        }
        ST(21);
        asm volatile("s_waitcnt vmcnt(4) ; PINW %0 %1 %2" : "+v"(epoch), "+v"(rowv[0]), "+v"(rowv[1]) :: "memory");
    }
    unsigned tag;
    float vp2 = 1.f;
    Fin F;
    if (G.wave < 4) {
        u32x4 ph[16];
        if (sel < 4) tag = phase1<true, 0>(a, lds, G, wvo0, xvo0, epoch, rowv, ph, vp2, stamps);
        else tag = phase1<false, 0>(a, lds, G, wvo0, xvo0, epoch, rowv, ph, vp2, stamps);
        phase2<0>(a, lds, G, tag, ph, vp2, F, stamps);
    } else {
        u32x4 ph[16];
#pragma unroll
        for (int i = 0; i < 16; ++i) ph[i] = u32x4{0u, 0u, 0u, 0u};
        if (sel < 4) tag = phase1<true, 1>(a, lds, G, wvo0, xvo0, epoch, rowv, ph, vp2, stamps);
        else tag = phase1<false, 1>(a, lds, G, wvo0, xvo0, epoch, rowv, ph, vp2, stamps);
        phase2<1>(a, lds, G, tag, ph, vp2, F, stamps);
    }
    {
        u32x4 ph[16];
        phase2<2>(a, lds, G, tag, ph, vp2, F, stamps);
    }
}

extern "C" void kernel_launch(void* const* d_in, const int* in_sizes, int n_in, void* d_out, int out_size, void* d_ws,
                              size_t ws_size, hipStream_t stream) {
    Args a;
    a.x = (const __bf16*)d_in[0]; a.wqkv = (const __bf16*)d_in[1]; a.wphi = (const __bf16*)d_in[2]; a.wo = (const __bf16*)d_in[3];
    a.w1 = (const __bf16*)d_in[5]; a.b1 = (const __bf16*)d_in[6]; a.w2 = (const __bf16*)d_in[7]; a.b2 = (const __bf16*)d_in[8];
    a.ws = (char*)d_ws; a.out = (__bf16*)d_out;
    (void)hipFuncSetAttribute((const void*)fused_kernel, hipFuncAttributeMaxDynamicSharedMemorySize, L_TOTAL);
    fused_kernel<<<256, 512, L_TOTAL, stream>>>(a);
}
```

```cpp
#include <hip/hip_runtime.h>
#include <hip/hip_bf16.h>
#include <stdint.h>
#include <type_traits>

typedef __attribute__((ext_vector_type(8))) __bf16 bf16x8;
typedef __attribute__((ext_vector_type(4))) __bf16 bf16x4;
typedef __attribute__((ext_vector_type(2))) __bf16 bf16x2;
typedef __attribute__((ext_vector_type(16))) float f32x16;
typedef __attribute__((ext_vector_type(4))) float f32x4;
typedef __attribute__((ext_vector_type(2))) float f32x2;
typedef __attribute__((ext_vector_type(4))) unsigned u32x4;
typedef __attribute__((ext_vector_type(2))) unsigned u32x2;
typedef __amdgpu_buffer_rsrc_t srd_t;
#define LAS __attribute__((address_space(3)))
#define GAS __attribute__((address_space(1)))
#define DI __device__ __forceinline__
#define MFMA32(a, b, c) __builtin_amdgcn_mfma_f32_32x32x16_bf16((a), (b), (c), 0, 0, 0)
#define SCHED0() __builtin_amdgcn_sched_barrier(0)
#define CFENCE() asm volatile("" ::: "memory")
#define RLX_AGENT __ATOMIC_RELAXED, __HIP_MEMORY_SCOPE_AGENT

constexpr int SEQ = 2048, DM = 512, NH = 32, FF = 1792;
constexpr float LOG2E = 1.4426950408889634f;
constexpr int SC1 = 16;
constexpr unsigned SPIN_CAP = 1u << 16;
constexpr unsigned CODE_LINES = 380;

constexpr unsigned WS_CTRL = 0;
constexpr unsigned WS_FLAG = 1024;
constexpr unsigned WS_RST  = WS_FLAG + 256 * 256;
constexpr unsigned WS_CSG  = WS_RST + 2048 * 16;
constexpr unsigned WS_KVR  = WS_CSG + 3328 * 32;
constexpr unsigned WS_KVP  = WS_KVR + 32 * 2176;
constexpr unsigned WS_H    = (WS_KVP + 16u * 32 * 136 * 32 + 4095) & ~4095u;
constexpr unsigned WS_O    = WS_H + 64u * 112 * 2048;
constexpr unsigned WS_END  = WS_O + 64u * 32 * 2048;

#define ST(i)

constexpr int SLOT = 32768, XOFF = 16384;
constexpr int chunk_base(bool qkv, int c) { return qkv ? (c == 0 ? 131072 : (c == 3 ? 81920 : 98304 + (c - 1) * 16384)) : (c == 0 ? 98304 : (c == 1 ? 122880 : (c == 2 ? 40960 : 65536))); }
constexpr int L_UNIT = 131072;
constexpr int L_CSP = 147456;
constexpr int L_RR = L_CSP + 1024;
constexpr int L_AUX = L_RR + 1024;
constexpr int L_DUMMY = L_AUX + 2048;
constexpr int L_FIX = L_DUMMY + 256;
constexpr int L_RED = 40960;
constexpr int L_PHI = 98304;
constexpr int NR = 8;
constexpr int NB = 11;
constexpr int L2_CNT = NB * 8192;
constexpr int L2_A = 98304;
constexpr int L_TOTAL = 163840;
static_assert(L_FIX + 8192 <= L_TOTAL, "lds");

template <int LOFF, int SOFF> DI void dma16(unsigned voff, srd_t srd, unsigned lbase) {
    unsigned t;
    lbase = (unsigned)__builtin_amdgcn_readfirstlane((int)lbase);
    asm volatile("s_add_i32 m0, %3, %4\n\ts_mov_b32 %0, %5\n\tbuffer_load_dwordx4 %1, %2, %0 offen lds"
                 : "=&s"(t) : "v"(voff), "s"(srd), "s"(lbase), "i"(LOFF), "i"(SOFF) : "memory", "scc");
}
template <int LOFF> DI void touch_ptr(const void* p, unsigned lbase) {
    asm volatile("s_add_i32 m0, %1, %2\n\ts_nop 0\n\tglobal_load_lds_dword %0, off" :: "v"(p), "s"(lbase), "i"(LOFF) : "memory", "scc");
}
template <int LOFF> DI void touch_c(unsigned voff, srd_t srd, unsigned lbase) {
    asm volatile("s_add_i32 m0, %2, %3\n\ts_nop 0\n\tbuffer_load_dword %0, %1, 0 offen sc1 lds" :: "v"(voff), "s"(srd), "s"(lbase), "i"(LOFF) : "memory", "scc");
}
template <int LOFF> DI void touch(unsigned voff, srd_t srd, unsigned lbase) {
    asm volatile("s_add_i32 m0, %2, %3\n\ts_nop 0\n\tbuffer_load_dword %0, %1, 0 offen lds" :: "v"(voff), "s"(srd), "s"(lbase), "i"(LOFF) : "memory", "scc");
}
template <int OFF> DI void ld16c(u32x4& d, unsigned voff, srd_t srd) {
    asm volatile("buffer_load_dwordx4 %0, %1, %2, %3 offen offset:%4 sc1" : "=v"(d) : "v"(voff), "s"(srd), "s"(OFF & ~4095), "i"(OFF & 4095) : "memory");
}
template <int OFF> DI void ld4c(unsigned& d, unsigned voff, srd_t srd) {
    asm volatile("buffer_load_dword %0, %1, %2, %3 offen offset:%4 sc1" : "=v"(d) : "v"(voff), "s"(srd), "s"(OFF & ~4095), "i"(OFF & 4095) : "memory");
}
template <int OFF> DI void ld8c(u32x2& d, unsigned voff, srd_t srd) {
    asm volatile("buffer_load_dwordx2 %0, %1, %2, %3 offen offset:%4 sc1" : "=v"(d) : "v"(voff), "s"(srd), "s"(OFF & ~4095), "i"(OFF & 4095) : "memory");
}
DI void dma16r(unsigned voff, srd_t srd, unsigned lds_dst) {
    asm volatile("s_mov_b32 m0, %2\n\ts_nop 0\n\tbuffer_load_dwordx4 %0, %1, 0 offen lds" :: "v"(voff), "s"(srd), "s"(lds_dst) : "memory");
}
DI void ld16(u32x4& d, unsigned voff, srd_t srd) { asm volatile("buffer_load_dwordx4 %0, %1, %2, 0 offen" : "=v"(d) : "v"(voff), "s"(srd) : "memory"); }
template <int OFF> DI void ld8(u32x2& d, unsigned voff, srd_t srd) { asm volatile("buffer_load_dwordx2 %0, %1, %2, 0 offen offset:%3" : "=v"(d) : "v"(voff), "s"(srd), "i"(OFF) : "memory"); }
DI float fmx(float a, float b) { return __builtin_elementwise_maximum(a, b); }
DI void wait_lgkm0() { __builtin_amdgcn_s_waitcnt(0xC07F); CFENCE(); }
template <int N> DI void wait_vm() { asm volatile("s_waitcnt vmcnt(%0)" ::"i"(N) : "memory"); }
DI void barrier() { __builtin_amdgcn_s_barrier(); CFENCE(); }
#define PIN(x) asm volatile("" : "+v"(x))
DI srd_t mk_srd(const void* p, unsigned bytes) { return __builtin_amdgcn_make_buffer_rsrc((void*)p, 0, (int)bytes, 0x00020000); }

DI bf16x4 tr_read(LAS char* p) { return __builtin_amdgcn_ds_read_tr16_b64_v4bf16((LAS bf16x4*)p); }
DI bf16x8 cat44(bf16x4 a, bf16x4 b) { return __builtin_shufflevector(a, b, 0, 1, 2, 3, 4, 5, 6, 7); }
DI float dot2(unsigned a, unsigned b, float c) {
    return __builtin_amdgcn_fdot2_f32_bf16(__builtin_bit_cast(bf16x2, a), __builtin_bit_cast(bf16x2, b), c, false);
}
DI unsigned pk2(float a, float b) { f32x2 v = {a, b}; return __builtin_bit_cast(unsigned, __builtin_convertvector(v, bf16x2)); }
template <int S> DI u32x4 pack8u(const f32x16& x) {
    u32x4 p = {pk2(x[8 * S], x[8 * S + 1]), pk2(x[8 * S + 2], x[8 * S + 3]), pk2(x[8 * S + 4], x[8 * S + 5]), pk2(x[8 * S + 6], x[8 * S + 7])};
    return p;
}
template <int S> DI bf16x8 pack8(const f32x16& x) { return __builtin_bit_cast(bf16x8, pack8u<S>(x)); }
DI bf16x8 asbf(u32x4 u) { return __builtin_bit_cast(bf16x8, u); }
DI float u2f(unsigned u) { return __builtin_bit_cast(float, u); }
DI unsigned f2u(float f) { return __builtin_bit_cast(unsigned, f); }
DI unsigned hi2(unsigned lo, unsigned hi) { return __builtin_amdgcn_perm(hi, lo, 0x07060302u); }
DI void split3(float v, unsigned& s0, unsigned& s1, unsigned& s2) {
    s0 = f2u(v) & 0xFFFF0000u;
    float r = v - u2f(s0);
    s1 = f2u(r) & 0xFFFF0000u;
    s2 = f2u(r - u2f(s1));
}
DI float half_sum(float v) { auto s = __builtin_amdgcn_permlane32_swap(f2u(v), f2u(v), false, false); return u2f(s[0]) + u2f(s[1]); }
DI float wave_sum(float v) {
#define DPPADD(ctrl, rm) v += u2f((unsigned)__builtin_amdgcn_update_dpp(0, (int)f2u(v), ctrl, rm, 0xf, true))
    DPPADD(0x111, 0xf); DPPADD(0x112, 0xf); DPPADD(0x114, 0xf); DPPADD(0x118, 0xf); DPPADD(0x142, 0xa); DPPADD(0x143, 0xc);
#undef DPPADD
    return u2f((unsigned)__builtin_amdgcn_readlane((int)f2u(v), 63));
}
template <int C, int N, class F> DI void static_for(F&& f) { if constexpr (C < N) { f(std::integral_constant<int, C>{}); static_for<C + 1, N>(f); } }

#define NO_L2W
#define R3_AT 3
#define OPRIO 0
#define TOUCH1 2
struct Args {
    const __bf16* x; const __bf16* wqkv; const __bf16* wphi; const __bf16* wo;
    const __bf16* w1; const __bf16* b1; const __bf16* w2; const __bf16* b2;
    char* ws; __bf16* out;
};
struct Geo { int bid, wave, lane, mt, nt, i16, mt2, nt2, qkv, rq; };
#define IC(v) std::integral_constant<int, (v)>{}

template <int N> DI void gather_wait(u32x4& gst, u32x2& gv2, u32x4& gcs) {
    asm volatile("s_waitcnt vmcnt(%3) ; PINW %0 %1 %2" : "+v"(gst), "+v"(gv2), "+v"(gcs) : "i"(N) : "memory");
}
DI void wait16(u32x4 (&v)[16]) {
    asm volatile("s_waitcnt vmcnt(0) ; PINW %0 %1 %2 %3 %4 %5 %6 %7 %8 %9 %10 %11 %12 %13 %14 %15"
                 : "+v"(v[0]), "+v"(v[1]), "+v"(v[2]), "+v"(v[3]), "+v"(v[4]), "+v"(v[5]), "+v"(v[6]), "+v"(v[7]),
                   "+v"(v[8]), "+v"(v[9]), "+v"(v[10]), "+v"(v[11]), "+v"(v[12]), "+v"(v[13]), "+v"(v[14]), "+v"(v[15]) :: "memory");
}

DI bool kv_reduce_publish(char* ws, u32x4 (&v)[16], int sl, int lane, unsigned tag) {
    unsigned bad = 0;
#pragma unroll
    for (int m = 0; m < 16; ++m) bad |= (v[m][1] ^ tag) | (v[m][3] ^ tag);
    if (!__all(bad == 0u)) return false;
    float s0[16], s1[16];
#pragma unroll
    for (int m = 0; m < 16; ++m) { s0[m] = u2f(v[m][0]); s1[m] = u2f(v[m][2]); }
#pragma unroll
    for (int st = 8; st >= 1; st >>= 1)
#pragma unroll
        for (int m = 0; m < st; ++m) { s0[m] += s0[m + st]; s1[m] += s1[m + st]; }
    const int g = (sl & 7) * 17 + (lane < 34 ? (lane >> 1) : 0), row = g >> 3, fq = g & 7;
    const unsigned off = WS_KVR + (sl >> 3) * 2176 + fq * 272 + row * 16 + (lane & 1) * 8;
    if (lane < 34)
        __hip_atomic_store((GAS unsigned long long*)(ws + off), (unsigned long long)pk2(s0[0], s1[0]) | ((unsigned long long)tag << 32), RLX_AGENT);
    return true;
}
template <bool QKV, int WN>
DI unsigned phase1(const Args& a, LAS char* lds, const Geo& G, const unsigned (&wvo0)[4], const unsigned (&xvo0)[4],
                   unsigned epoch, const u32x4 (&rowv)[2], u32x4 (&ph)[16], float& vp2, unsigned long long* stamps) {
    constexpr int NTB = QKV ? 2 : 3;
    constexpr int NB2 = QKV ? 1 : (WN == 0 ? 1 : 2);
    constexpr int NG = 2 + NB2;
    constexpr int NU = QKV ? 24 : 28;
    constexpr int LDW = QKV ? 1536 : 1792;
    constexpr int CP = NTB * 2;
    const int lane = G.lane, wave = G.wave, wm = wave & 3, mt = G.mt, nt = G.nt;
    const unsigned ldsb = (unsigned)(size_t)lds;
    const int hh = lane >> 5, r32 = lane & 31, blk = (lane >> 4) & 1, q4 = (lane & 15) >> 2, p4 = lane & 3;
    const srd_t rs = mk_srd(a.ws, WS_END);
    const srd_t rx = mk_srd(a.x, SEQ * DM * 2);
    const srd_t rw = mk_srd(QKV ? a.wqkv : a.w1, DM * LDW * 2);
    const srd_t rw2 = mk_srd(a.w2, FF * DM * 2);
    const srd_t rwo = mk_srd(a.wo, DM * DM * 2);
    auto ptile = [&](int c) { const int p = c + (nt & 1); return p >= 7 ? p - 7 : p; };
    auto colb_of = [&](int c) { return QKV ? ((((c >> 1) + 1) % 3) * 512 + 64 * nt + 32 * (c & 1)) : (224 * nt + 32 * ptile(c)); };
    auto tile_of = [&](int t) { return t < 2 ? 2 * t + WN : (QKV ? 4 + WN : 4 + WN + (t - 2)); };
    auto dma_pair = [&](auto JI, auto II) {
        constexpr int j = JI.value, i = II.value, sb = ((j + 1) % 3) * SLOT;
        if constexpr (i < 4) dma16<sb + i * 1024, j * 64 * LDW * 2>(wvo0[i], rw, ldsb + wm * 4096);
        else dma16<sb + XOFF + (i - 4) * 1024, j * 128>(xvo0[i - 4], rx, ldsb + wm * 4096);
    };
    const unsigned w2vo = (unsigned)((((lane >> 2) & 7) * DM + G.nt2 * 64 + hh * 32 + (lane & 3) * 8) * 2);
    unsigned w2q[3];
#pragma unroll
    for (int q = 0; q < 3; ++q) w2q[q] = w2vo + (unsigned)((wm * 448 + ((q + G.rq) & 3) * 112) * DM * 2);
    auto dma_w2 = [&](auto PI) {
        constexpr int r = PI.value >> 1, t = PI.value & 1;
        dma16<(r % NB) * 8192 + t * 1024, ((r % 7) * 16 + t * 8) * DM * 2>(w2q[r / 7], rw2, ldsb + wm * 2048);
    };
    const unsigned wovo = w2vo + wm * (128 * DM * 2);
    auto dma_wo = [&](auto PI) { dma16<L2_A + PI.value * 1024, PI.value * 8 * DM * 2>(wovo, rwo, ldsb + wm * 16384); };
    unsigned cvo[WN == 1 ? NTB : 1];
    if constexpr (WN == 1) {
#pragma unroll
        for (int i = 0; i < NTB; ++i) {
            const int st = 2 * i + hh, k8 = 4 * wm + (st >= NTB ? 1 : 0), c = st >= NTB ? st - NTB : st;
            cvo[i] = (unsigned)(((k8 * 8 + ((lane >> 2) & 7)) * LDW + colb_of(4 + c) + (lane & 3) * 8) * 2);
        }
    }
    auto dma_chunk = [&](auto CI, auto II) {
        constexpr int c = CI.value, i = II.value;
        dma16<chunk_base(QKV, c) + i * 1024, (c * 128 + (i / NTB) * 16) * LDW * 2>(cvo[i % NTB], rw, ldsb + wm * (CP * 1024));
    };
    const int srow = mt * 128 + G.i16 * 8 + 2 * wm;
    const unsigned tag = (unsigned)__builtin_amdgcn_readfirstlane((int)epoch) + 1u;
    const unsigned trl = (4 * hh + q4) * 64 + blk * 32 + p4 * 8;
    ST(20);
    if constexpr (WN == 0) {
        LAS char* p = lds + L_UNIT + wm * 4096 + trl;
        const unsigned one = 0x3F803F80u;
        float su[2];
#pragma unroll
        for (int u = 0; u < 2; ++u) {
            float s = 0.f;
#pragma unroll
            for (int i = 0; i < 4; ++i) { u32x2 w = __builtin_bit_cast(u32x2, tr_read(p + u * 512 + i * 1024)); s = dot2(w[1], one, dot2(w[0], one, s)); }
            su[u] = s;
        }
        auto sw = __builtin_amdgcn_permlane32_swap(f2u(su[0]), f2u(su[1]), false, false);
        *(LAS float*)(lds + L_CSP + ((hh * 4 + wm) * 32 + r32) * 4) = u2f(sw[0]) + u2f(sw[1]);
        static_for<0, 8>([&](auto I) { dma_pair(IC(1), I); });
    } else {
        float mean[2], vv[2];
#pragma unroll
        for (int r = 0; r < 2; ++r) {
            float e[8];
#pragma unroll
            for (int i = 0; i < 4; ++i) { e[2 * i] = u2f(rowv[r][i] << 16); e[2 * i + 1] = u2f(rowv[r][i] & 0xFFFF0000u); }
            mean[r] = wave_sum(((e[0] + e[1]) + (e[2] + e[3])) + ((e[4] + e[5]) + (e[6] + e[7]))) * (1.f / DM);
            float q = 0.f;
#pragma unroll
            for (int i = 0; i < 8; ++i) { float d = e[i] - mean[r]; q = __builtin_fmaf(d, d, q); }
            vv[r] = wave_sum(q) * (1.f / DM) + 1e-5f;
        }
        u32x4 g = {f2u(lane ? mean[1] : mean[0]), tag, f2u(lane ? vv[1] : vv[0]), tag};
        if (lane < 2) __builtin_amdgcn_raw_buffer_store_b128(g, rs, WS_RST + (srow + lane) * 16, 0, SC1);
    }
    ST(22);

    LAS char* wb[2]; LAS char* xb[2][4];
#pragma unroll
    for (int s = 0; s < 2; ++s) {
        wb[s] = lds + s * 65536 + hh * 2048 + WN * 512 + q4 * 64 + blk * 32 + p4 * 8;
        PIN(wb[s]);
        const int row = 32 * wm + r32;
#pragma unroll
        for (int k = 0; k < 4; ++k) { xb[s][k] = lds + s * 65536 + XOFF + row * 128 + (((2 * k + hh) ^ ((row >> 1) & 7)) * 16); PIN(xb[s][k]); }
    }
    const int csbase = QKV ? nt * 192 : 1536 + nt * 224;

    f32x16 acc[2];
#pragma unroll
    for (int t = 0; t < 2; ++t)
#pragma unroll
        for (int i = 0; i < 16; ++i) acc[t][i] = 0.f;
    bf16x8 xf[32], wf[3][2];
    auto read_x = [&](int g) { const int sl = ((g >> 2) + 1) % 3; xf[g] = *(LAS bf16x8*)(xb[sl >> 1][g & 3] + (sl & 1) * SLOT); };
    auto read_w = [&](int g, int t) {
        const int sl = ((g >> 2) + 1) % 3;
        LAS char* p = wb[sl >> 1] + (sl & 1) * SLOT + (g & 3) * 4096 + t * 1024;
        wf[g % 3][t] = cat44(tr_read(p), tr_read(p + 256));
    };
    u32x4 gst, gcs; u32x2 gv2;
    const int mytile = wm < NG ? (wm < 2 ? 2 * wm + WN : (QKV ? 4 + WN : 2 + WN + wm)) : WN;
    const unsigned gvo_st = (unsigned)(mt * 128 + 32 * wm + r32) * 16;
    const unsigned gvo_v2 = (unsigned)(G.mt2 * 64 + wave * 8 + (lane >> 3)) * 16;
    const unsigned gvo_cs = (unsigned)(csbase + mytile * 32 + r32) * 32 + hh * 16;
    auto gather = [&]() {
        ld16c<WS_RST>(gst, gvo_st, rs);
        ld8c<WS_RST + 8>(gv2, gvo_v2, rs);
        ld16c<WS_CSG>(gcs, gvo_cs, rs);
    };
    float vpe = 1.f, r_t = 1.f;
    bf16x8 fixT, fixF[NG];
    auto gather_check_and_fix = [&]() {
        for (unsigned spin = 0;; ++spin) {
            const unsigned bad = (gst[1] ^ tag) | (gst[3] ^ tag) | (gv2[1] ^ tag) | (gcs[1] ^ tag) | (gcs[3] ^ tag);
            if (__all(bad == 0u) || spin > SPIN_CAP) break;
            __builtin_amdgcn_s_sleep(1);
            gather();
            gather_wait<0>(gst, gv2, gcs);
        }
        vpe = u2f(gst[2]);
        vp2 = u2f(gv2[0]);
        r_t = __builtin_amdgcn_rsqf(vpe);
        unsigned s0, s1, s2;
        split3(hh ? vpe * r_t : -u2f(gst[0]), s0, s1, s2);
        u32x4 u;
        u[0] = hi2(s0, s1);
        u[1] = hh ? (s2 >> 16) : u[0];
        u[2] = hh ? 0u : hi2(s2, s0);
        u[3] = 0u;
        fixT = asbf(u);
        if (QKV) *(LAS float*)(lds + L_RR + (wave * 32 + r32) * 4) = r_t;
        unsigned c0, c1, c2;
        split3(half_sum(u2f(gcs[0]) + u2f(gcs[2])), c0, c1, c2);
        unsigned bb = 0;
        if (!QKV) bb = *(LAS unsigned short*)(lds + L_AUX + (ptile(mytile) * 32 + r32) * 2);
        u32x4 w;
        w[0] = hh ? (bb | (bb << 16)) : hi2(c0, c0);
        w[1] = hh ? bb : hi2(c1, c1);
        w[2] = hh ? 0u : hi2(c0, c2);
        w[3] = 0u;
        *(LAS u32x4*)(lds + L_FIX + wave * 1024 + lane * 16) = w;
    };

    wait_vm<WN == 0 ? 8 : 1>();
    wait_lgkm0();
    ST(23);
    barrier();
    ST(1);
#pragma unroll
    for (int g = 0; g < 2; ++g) { read_x(g); read_w(g, 0); read_w(g, 1); }
    if constexpr (WN == 1) {
        const int idx = 16 * wm + (lane & 15);
        LAS float* p = (LAS float*)(lds + L_CSP) + (idx >> 5) * 128 + (idx & 31);
        u32x2 gq = {f2u((p[0] + p[32]) + (p[64] + p[96])), tag};
        const int uc = 2 * (mt >> 2) + (wm >> 1);
        if (lane < 16 && uc < NU / 4) __builtin_amdgcn_raw_buffer_store_b64(gq, rs, WS_CSG + (csbase + uc * 32 + (idx & 31)) * 32 + (mt & 3) * 8, 0, SC1);
        if (wave == 4) {
            if (QKV) {
                const srd_t rp = mk_srd(a.wphi, NH * 256 * 2);
                dma16<L_AUX, 0>((unsigned)(nt * 2048 + lane * 16), rp, ldsb);
                dma16<L_AUX + 1024, 1024>((unsigned)(nt * 2048 + lane * 16), rp, ldsb);
            } else {
                const srd_t rb = mk_srd(a.b1, FF * 2);
                dma16<L_AUX, 0>((unsigned)(nt * 448 + lane * 16), rb, ldsb);
            }
        }
    }
    static_for<0, 32>([&](auto GI) {
        constexpr int g = GI.value, j = g >> 2;
        SCHED0();
        if constexpr ((g & 3) == 2) {
            constexpr int JG = WN == 0 ? 4 : 5;
            if constexpr (j == JG + 1) { PIN(acc[0]); PIN(acc[1]); }
            if constexpr (WN == 0) {
                constexpr int n = j <= 6 ? 8 : 63;
                if constexpr (j == JG + 1) gather_wait<n>(gst, gv2, gcs);
                else wait_vm<n>();
            } else if constexpr (j == 4) wait_vm<1>();
            else if constexpr (j == JG + 1) gather_wait<CP>(gst, gv2, gcs);
            else if constexpr (j == 7) wait_vm<CP>();
            wait_lgkm0();
            barrier();
            ST(2 + j);
            if constexpr (WN == 1 && j == 1) {
                const int gw = G.bid * 4 + wm;
                const char* p = lane < 14 ? (const char*)a.w2 + (size_t)(gw * 14 + lane) * 128 : (const char*)a.wo + (size_t)(gw * 4 + (lane & 3)) * 128;
                touch_ptr<L_DUMMY>(p, ldsb);
            }
            if constexpr (WN == 1 && j == R3_AT) {
                PIN(acc[0]); PIN(acc[1]);
                const int loc = G.bid >> 3;
                const unsigned c2 = (3u - (unsigned)((size_t)a.w2 >> 7)) & 7u, co = (3u - (unsigned)((size_t)a.wo >> 7)) & 7u;
                const char* p = lane < 14 ? (const char*)a.w2 + (size_t)((loc * 4 + wm) * 14 + lane) * 1024 + c2 * 128
                                          : (const char*)a.wo + (size_t)((loc * 4 + wm) * 4 + (lane & 3)) * 1024 + co * 128;
                touch_ptr<L_DUMMY>(p, ldsb);
            }
            if constexpr (j == JG) gather();
            if constexpr (WN == 1 && j == 7) static_for<0, CP>([&](auto I) { dma_chunk(IC(3), I); });
            if constexpr (j == JG + 1) gather_check_and_fix();
            if constexpr (j == 7) {
#pragma unroll
                for (int t = 0; t < NG; ++t) fixF[t] = *(LAS bf16x8*)(lds + L_FIX + (WN * 4 + t) * 1024 + lane * 16);
            }
            SCHED0();
        }
        constexpr int jb = (g - 2) >> 2, q = (g - 2) & 3;
        static_for<0, 2>([&](auto TI) {
            constexpr int t = TI.value;
            if (QKV && t == 1) acc[t] = MFMA32(xf[g], wf[g % 3][t], acc[t]);
            else acc[t] = MFMA32(wf[g % 3][t], xf[g], acc[t]);
            if constexpr (g + 2 < 32) {
                if (t == 0) read_x(g + 2);
                read_w(g + 2, t);
            }
            constexpr int i = q * 2 + t;
            if constexpr (WN == 0) {
                if constexpr (g < 2) { dma_pair(IC(2), IC(4 * g + 2 * t)); dma_pair(IC(2), IC(4 * g + 2 * t + 1)); }
                else if constexpr (jb <= 4) dma_pair(IC(jb + 3), IC(i));
                else if constexpr (jb == 5) dma_w2(IC(i));
                else if constexpr (jb == 6 && i < 2) dma_w2(IC(8 + i));
            } else if constexpr (g >= 2 && jb >= 4 && jb <= 6 && i < CP) dma_chunk(IC(jb - 4), IC(i));
            SCHED0();
        });
    });
    ST(10);
    const int tt = mt * 4 + wm;
    f32x16 zero16;
#pragma unroll
    for (int i = 0; i < 16; ++i) zero16[i] = 0.f;
    auto store_h = [&](f32x16& c, int tile) {
#pragma unroll
        for (int i = 0; i < 16; ++i) { float v = fmx(c[i], 0.f); c[i] = v * v; }
        const unsigned off = WS_H + (unsigned)((tt * 112 + nt * 14 + ptile(tile) * 2) * 2048 + lane * 16 + (lane >= 24 ? 128 : 0));
        __builtin_amdgcn_raw_buffer_store_b128(pack8u<0>(c), rs, off, 0, SC1);
        __builtin_amdgcn_raw_buffer_store_b128(pack8u<1>(c), rs, off + 2048, 0, SC1);
    };
    bf16x8 wfr[2];
    const float kscale = r_t * LOG2E;
    auto feature_map2 = [&](f32x16 (&p)[2], u32x4 (&F)[2][2]) {
        float m[2][2], sm[2][2];
#pragma unroll
        for (int X = 0; X < 2; ++X)
#pragma unroll
            for (int h = 0; h < 2; ++h) {
                const int o = 8 * h;
                m[X][h] = fmx(fmx(fmx(fmx(p[X][o], p[X][o + 1]), p[X][o + 2]), fmx(fmx(p[X][o + 3], p[X][o + 4]), p[X][o + 5])), fmx(p[X][o + 6], p[X][o + 7]));
            }
#pragma unroll
        for (int X = 0; X < 2; ++X) {
            auto s = __builtin_amdgcn_permlane32_swap(f2u(m[X][0]), f2u(m[X][1]), false, false);
            float c = -kscale * fmx(u2f(s[0]), u2f(s[1]));
            auto s2 = __builtin_amdgcn_permlane32_swap(f2u(c), f2u(c), false, false);
            m[X][0] = u2f(s2[0]); m[X][1] = u2f(s2[1]);
        }
#pragma unroll
        for (int i = 0; i < 16; ++i)
#pragma unroll
            for (int X = 0; X < 2; ++X) p[X][i] = __builtin_amdgcn_exp2f(__builtin_fmaf(p[X][i], kscale, m[X][i >> 3]));
#pragma unroll
        for (int X = 0; X < 2; ++X)
#pragma unroll
            for (int h = 0; h < 2; ++h) {
                const int o = 8 * h;
                sm[X][h] = ((p[X][o] + p[X][o + 1]) + (p[X][o + 2] + p[X][o + 3])) + ((p[X][o + 4] + p[X][o + 5]) + (p[X][o + 6] + p[X][o + 7]));
            }
#pragma unroll
        for (int X = 0; X < 2; ++X) {
            auto s = __builtin_amdgcn_permlane32_swap(f2u(sm[X][0]), f2u(sm[X][1]), false, false);
            float c = __builtin_amdgcn_rcpf(u2f(s[0]) + u2f(s[1]));
            auto s2 = __builtin_amdgcn_permlane32_swap(f2u(c), f2u(c), false, false);
            sm[X][0] = u2f(s2[0]); sm[X][1] = u2f(s2[1]);
        }
#pragma unroll
        for (int X = 0; X < 2; ++X) {
#pragma unroll
            for (int i = 0; i < 16; ++i) p[X][i] *= sm[X][i >> 3];
            F[X][0] = pack8u<0>(p[X]); F[X][1] = pack8u<1>(p[X]);
        }
    };
    if constexpr (!QKV) {
        acc[0] = MFMA32(fixF[0], fixT, acc[0]);
        acc[1] = MFMA32(fixF[1], fixT, acc[1]);
        store_h(acc[0], WN);
        store_h(acc[1], 2 + WN);
    } else {
        acc[0] = MFMA32(fixF[0], fixT, acc[0]);
        acc[1] = MFMA32(fixT, fixF[1], acc[1]);
        bf16x8 idf[2];
#pragma unroll
        for (int s = 0; s < 2; ++s) {
            u32x4 u;
#pragma unroll
            for (int i = 0; i < 4; ++i) {
                int f0 = 16 * s + 8 * (i >> 1) + 4 * hh + 2 * (i & 1);
                u[i] = (r32 == f0 ? 0x3F80u : 0u) | (r32 == f0 + 1 ? 0x3F800000u : 0u);
            }
            idf[s] = asbf(u);
        }
#pragma unroll
        for (int X = 0; X < 2; ++X) {
            LAS char* p = lds + L_AUX + (2 * WN + X) * 512 + (4 * hh + q4) * 32 + p4 * 8;
            u32x4 u = __builtin_bit_cast(u32x4, cat44(tr_read(p), tr_read(p + 256)));
            const unsigned sg = blk ? 0x80008000u : 0u;
#pragma unroll
            for (int i = 0; i < 4; ++i) u[i] ^= sg;
            wfr[X] = asbf(u);
        }
#pragma unroll
        for (int g = 0; g < 4; ++g) {
            f32x4 rr = *(LAS f32x4*)(lds + L_RR + (wave * 32 + 8 * g + 4 * hh) * 4);
#pragma unroll
            for (int i = 0; i < 4; ++i) acc[1][4 * g + i] *= rr[i];
        }
        const u32x4 v0 = pack8u<0>(acc[1]), v1 = pack8u<1>(acc[1]);
        const bf16x8 kb0 = pack8<0>(acc[0]), kb1 = pack8<1>(acc[0]);
        {
            f32x16 p[2], d[2], kv[2];
            u32x4 F[2][2];
            p[0] = MFMA32(wfr[0], kb0, zero16);
            p[1] = MFMA32(wfr[1], kb1, zero16);
            feature_map2(p, F);
#pragma unroll
            for (int X = 0; X < 2; ++X) d[X] = MFMA32(asbf(F[X][0]), idf[0], zero16);
#pragma unroll
            for (int X = 0; X < 2; ++X) d[X] = MFMA32(asbf(F[X][1]), idf[1], d[X]);
            u32x4 b[2][2];
#pragma unroll
            for (int X = 0; X < 2; ++X)
#pragma unroll
                for (int i = 0; i < 4; ++i) { b[X][0][i] = (blk == X) ? v0[i] : 0x3F803F80u; b[X][1][i] = (blk == X) ? v1[i] : 0x3F803F80u; }
#pragma unroll
            for (int X = 0; X < 2; ++X) kv[X] = MFMA32(pack8<0>(d[X]), asbf(b[X][0]), zero16);
#pragma unroll
            for (int X = 0; X < 2; ++X) kv[X] = MFMA32(pack8<1>(d[X]), asbf(b[X][1]), kv[X]);
#pragma unroll
            for (int X = 0; X < 2; ++X) {
                const int row = X == 0 ? r32 : (r32 == 15 ? 16 : r32 - 16);
                const bool use = X == 0 ? (r32 <= 16) : (r32 >= 15);
                LAS char* dst = lds + (use ? L_RED + (((WN * 2 + X) * 4 + wm) * 17 + row) * 144 : L_RED + 39168 + (r32 & 7) * 144) + hh * 16;
#pragma unroll
                for (int g = 0; g < 4; ++g) {
                    f32x4 v = {kv[X][4 * g], kv[X][4 * g + 1], kv[X][4 * g + 2], kv[X][4 * g + 3]};
                    *(LAS f32x4*)(dst + g * 32) = v;
                }
            }
        }
        wait_lgkm0();
        ST(26);
        barrier();
        if constexpr (WN == 1) {
#pragma unroll
            for (int it = 0; it < 5; ++it) {
                const int o = it * 64 + lane;
                if (it < 4 || lane < 16) {
                    LAS char* src = lds + L_RED + (wm * 4 * 17 + (o >> 4)) * 144 + (o & 15) * 8;
                    f32x2 v = (*(LAS f32x2*)(src) + *(LAS f32x2*)(src + 2448)) + (*(LAS f32x2*)(src + 2 * 2448) + *(LAS f32x2*)(src + 3 * 2448));
                    u32x4 g0 = {f2u(v[0]), tag, f2u(v[1]), tag};
                    __builtin_amdgcn_raw_buffer_store_b128(g0, rs, WS_KVP + (unsigned)((mt * 32 + nt * 4 + wm) * 272 + o) * 16, 0, SC1);
                }
            }
        }
    }
    ST(27);
    f32x16 bc[NB2];
#pragma unroll
    for (int t = 0; t < NB2; ++t) bc[t] = MFMA32(fixF[2 + t], fixT, zero16);
    {
        constexpr int CB0 = QKV ? 81920 : 40960;
        LAS char* q0 = lds + hh * (NTB * 512) + WN * 512 + q4 * 64 + blk * 32 + p4 * 8 + CB0;
        LAS char* q1 = q0 + (98304 - CB0);
        PIN(q0); PIN(q1);
        bf16x8 qf[4][NB2];
        auto read_q = [&](int g, int t) {
            const int cb = chunk_base(QKV, g >> 3);
            LAS char* p = (cb >= 98304 ? q1 + (cb - 98304) : q0 + (cb - CB0)) + (g & 7) * (NTB * 1024) + t * 512;
            qf[g & 3][t] = cat44(tr_read(p), tr_read(p + 256));
        };
#pragma unroll
        for (int g = 0; g < 3; ++g)
#pragma unroll
            for (int t = 0; t < NB2; ++t) read_q(g, t);
        static_for<0, 32>([&](auto GI) {
            constexpr int g = GI.value;
            SCHED0();
            if constexpr (g == 8 || g == 16 || g == 24) {
                if constexpr (g == 8 && WN == 1) wait_vm<QKV ? 5 : 4>();
                wait_lgkm0(); barrier();
            }
            static_for<0, NB2>([&](auto TI) {
                constexpr int t = TI.value;
                bc[t] = MFMA32(qf[g & 3][t], xf[g], bc[t]);
                if constexpr (g + 3 < 32) read_q(g + 3, t);
                if constexpr (WN == 0) {
                    if constexpr (QKV && g >= 8 && g < 18) dma_w2(IC(10 + g - 8));
                    if constexpr (!QKV && g >= 24 && g < 30) dma_w2(IC(10 + g - 24));
                } else if constexpr (!QKV && g >= 16 && g < 24) dma_wo(IC(2 * (g - 16) + t));
                else if constexpr (QKV && g >= 9 && g < 25) { if (wm >= 2) dma_wo(IC(g - 9)); }
                SCHED0();
            });
        });
    }
    ST(11);
    auto w2_to_regs = [&]() {
        LAS char* rdA = lds + wm * 2048 + trl;
#pragma unroll
        for (int s = 0; s < NR; ++s) {
            LAS char* p = rdA + s * 8192;
            ph[2 * s] = __builtin_bit_cast(u32x4, cat44(tr_read(p), tr_read(p + 1024)));
            ph[2 * s + 1] = __builtin_bit_cast(u32x4, cat44(tr_read(p + 512), tr_read(p + 1536)));
        }
        wait_lgkm0();
    };
    if constexpr (!QKV) {
#pragma unroll
        for (int t = 0; t < NB2; ++t) store_h(bc[t], 4 + WN + t);
        ST(12);
        if constexpr (WN == 0) {
            wait_vm<2>();
            w2_to_regs();
            static_for<2 * NB, 2 * NB + 8>([&](auto P) { dma_w2(P); });
            wait_vm<8>();
            barrier();
        } else {
            if (wm >= 2) {
                const int sl = (mt * 8 + nt) * 2 + (wm & 1);
                const unsigned vo = (unsigned)(((sl >> 3) * 136 + (sl & 7) * 17 + (lane < 34 ? (lane >> 1) : 0)) * 32 + (lane & 1) * 16);
                static_for<0, 16>([&](auto M) { ld16c<WS_KVP + M.value * 32 * 136 * 32>(ph[M.value], vo, rs); });
                wait_vm<16>();
                barrier();
                wait16(ph);
                ph[0][0] = kv_reduce_publish(a.ws, ph, sl, lane, tag) ? 1u : 0u;
            } else {
                if (wave == 4 && lane < 16) *(LAS unsigned*)(lds + L2_CNT + lane * 4) = 0u;
                wait_vm<0>();
                wait_lgkm0();
                barrier();
                if (wave == 5 && lane == 0)
                    __hip_atomic_store((GAS unsigned*)(a.ws + WS_FLAG + (mt * 16 + 8 + nt) * 256), tag, RLX_AGENT);
            }
        }
    } else {
        const bf16x8 qb0 = pack8<0>(bc[0]), qb1 = pack8<1>(bc[0]);
        if constexpr (WN == 0) {
            wait_vm<0>(); w2_to_regs();
            static_for<2 * NB, 2 * NB + 8>([&](auto P) { dma_w2(P); });
        }
        {
            f32x16 p[2];
            u32x4 F[2][2];
            p[0] = MFMA32(wfr[0], qb0, zero16);
            p[1] = MFMA32(wfr[1], qb1, zero16);
            feature_map2(p, F);
#pragma unroll
            for (int X = 0; X < 2; ++X) {
                LAS char* dst = lds + L_PHI + ((2 * WN + X) * 4 + wm) * 2048 + lane * 16;
                *(LAS u32x4*)(dst) = F[X][0];
                *(LAS u32x4*)(dst + 1024) = F[X][1];
            }
        }
        ST(12);
        wait_lgkm0();
        barrier();
        if constexpr (WN == 1) {
            if (wave == 4 && lane < 16) *(LAS unsigned*)(lds + L2_CNT + lane * 4) = 0u;
#pragma unroll
            for (int i = 0; i < 8; ++i) ph[i] = *(LAS u32x4*)(lds + L_PHI + wm * 8192 + i * 1024 + lane * 16);
            wait_lgkm0();
        }
        barrier();
    }
    ST(13);
    return tag;
}

#define HPV 6
constexpr int HP = HPV;
constexpr int w_at(int i) { return i >= NR && i + NB <= 27 ? i + NB : -1; }
constexpr int p2_pos(bool isw, int idx) {
    int pos = 0;
    for (int s = 0; s < HP; ++s) { pos += 2; if (!isw && idx == s) return pos; }
    for (int i = 1; i < 28; ++i) {
        if (i - 1 + HP <= 27) { pos += 2; if (!isw && idx == i - 1 + HP) return pos; }
        if (w_at(i - 1) >= 0) { pos += 2; if (isw && idx == w_at(i - 1)) return pos; }
    }
    return 0;
}
constexpr int p2_issued(int step) {
    int pos = 2 * HP;
    for (int i = 1; i < step; ++i) { if (i - 1 + HP <= 27) pos += 2; if (w_at(i - 1) >= 0) pos += 2; }
    return pos;
}
constexpr int p2_wait(int i) {
    int need = p2_pos(false, i);
    if (i + 1 >= NR + NB && i + 1 <= 27) { int w = p2_pos(true, i + 1); if (w > need) need = w; }
    return p2_issued(i) - need;
}
template <int SLEEP> DI void poll_words(srd_t rs, unsigned voff, unsigned tag) {
    for (unsigned spin = 0; spin <= SPIN_CAP; ++spin) {
        unsigned v;
        ld4c<0>(v, voff, rs);
        asm volatile("s_waitcnt vmcnt(0) ; PINW %0" : "+v"(v) :: "memory");
        if (__all(v == tag)) break;
        __builtin_amdgcn_s_sleep(SLEEP);
    }
}
DI void lds_wait_word(LAS char* p) {
    for (unsigned spin = 0; spin <= 16 * SPIN_CAP; ++spin) {
        if (__hip_atomic_load((LAS unsigned*)p, __ATOMIC_RELAXED, __HIP_MEMORY_SCOPE_WORKGROUP) != 0u) break;
        __builtin_amdgcn_s_sleep(1);
    }
    CFENCE();
}
DI void lds_set_word(LAS char* p) { __hip_atomic_store((LAS unsigned*)p, 1u, __ATOMIC_RELAXED, __HIP_MEMORY_SCOPE_WORKGROUP); }

struct Fin { f32x16 acc[2][2]; u32x4 xr, br; };
template <int PART>
DI void phase2(const Args& a, LAS char* lds, const Geo& G, unsigned tag, u32x4 (&ph)[16], float vp2, Fin& F, unsigned long long* stamps) {
    const int lane = G.lane, wave = G.wave, mt = G.mt2, nt = G.nt2;
    const unsigned ldsb = (unsigned)(size_t)lds;
    const int hh = lane >> 5, r32 = lane & 31, blk = (lane >> 4) & 1, q4 = (lane & 15) >> 2, p4 = lane & 3;
    const int kq = wave & 3, hf = wave >> 2;
    const srd_t rs = mk_srd(a.ws, WS_END);
    const srd_t rw2 = mk_srd(a.w2, FF * DM * 2);
    const unsigned wvo = (unsigned)((((lane >> 2) & 7) * DM + nt * 64 + hh * 32 + (lane & 3) * 8) * 2);
    const unsigned trl = (4 * hh + q4) * 64 + blk * 32 + p4 * 8;
    const unsigned l16 = lane * 16;
    const unsigned l16s = l16 + (lane >= 24 ? 128 : 0);
    unsigned w2q[4];
#pragma unroll
    for (int q = 0; q < 4; ++q) w2q[q] = wvo + (unsigned)((kq * 448 + ((q + G.rq) & 3) * 112) * DM * 2);
    const int tl = wave * 8 + (lane >> 3), cg = lane & 7;
    const unsigned ofo = (unsigned)((mt * 64 + tl) * DM + nt * 64 + cg * 8) * 2;
    u32x4 &xr = F.xr, &br = F.br;
    auto final_loads = [&]() {
        ld16(xr, ofo, mk_srd(a.x, SEQ * DM * 2));
        ld16(br, (unsigned)(nt * 64 + cg * 8) * 2, mk_srd(a.b2, DM * 2));
    };
    f32x16 (&acc)[2][2] = F.acc;
    if constexpr (PART < 2) {
#pragma unroll
        for (int i = 0; i < 2; ++i)
#pragma unroll
            for (int j = 0; j < 2; ++j)
#pragma unroll
                for (int e = 0; e < 16; ++e) acc[i][j][e] = 0.f;
    }
    LAS char* upper = lds + L2_A + kq * 16384;
    if constexpr (PART == 0) {
        const unsigned lb = ldsb + kq * 2048;
        unsigned hq[4];
#pragma unroll
        for (int q = 0; q < 4; ++q) hq[q] = (unsigned)(((2 * mt) * 112 + 28 * kq + ((q + G.rq) & 3) * 7) * 2048) + l16s;
        auto issue_w = [&](auto SI) {
            constexpr int s = SI.value;
            dma16<(s % NB) * 8192, (s % 7) * 16 * DM * 2>(w2q[s / 7], rw2, lb);
            dma16<(s % NB) * 8192 + 1024, ((s % 7) * 16 + 8) * DM * 2>(w2q[s / 7], rw2, lb);
        };
        u32x4 hb[HP][2];
        if (!G.qkv) { issue_w(IC(8)); issue_w(IC(9)); }
        issue_w(IC(10));
        static_for<NB + 4, NR + NB>([&](auto SI) { issue_w(SI); });
        final_loads();
        LAS char* rdA = lds + kq * 2048 + trl;
        LAS char* rdB = rdA + 6 * 8192;
        PIN(rdB);
        bf16x8 wf[2][2];
        auto rd = [&](int s, bf16x8 (&d)[2]) {
            LAS char* p = (s % NB) < 6 ? rdA + (s % NB) * 8192 : rdB + (s % NB - 6) * 8192;
            d[0] = cat44(tr_read(p), tr_read(p + 1024));
            d[1] = cat44(tr_read(p + 512), tr_read(p + 1536));
        };
        ST(24);
        lds_wait_word(lds + L2_CNT + 16);
        ST(14);
        static_for<0, HP>([&](auto SI) {
            constexpr int s = SI.value;
            ld16c<WS_H + (s % 7) * 2048>(hb[s % HP][0], hq[s / 7], rs);
            ld16c<WS_H + (112 + s % 7) * 2048>(hb[s % HP][1], hq[s / 7], rs);
        });
        static_for<0, 28>([&](auto SI) {
            constexpr int s = SI.value;
            SCHED0();
            asm volatile("s_waitcnt vmcnt(%2) ; PINW %0 %1" : "+v"(hb[s % HP][0]), "+v"(hb[s % HP][1]) : "i"(p2_wait(s)) : "memory");
            if constexpr (s == 0) ST(15);
            static_for<0, 4>([&](auto MI) {
                constexpr int m = MI.value, i = m >> 1, j = m & 1;
                acc[i][j] = MFMA32(s < NR ? asbf(ph[s < NR ? 2 * s + i : 0]) : wf[s & 1][i], asbf(hb[s % HP][j]), acc[i][j]);
                SCHED0();
                if constexpr (m == 0 && s + 1 >= NR && s + 1 < 28) rd(s + 1, wf[(s + 1) & 1]);
                constexpr int hs = s - 1 + HP;
                constexpr int ws = s >= 1 ? w_at(s - 1) : -1, wz = ws >= 0 ? ws : 0;
                if constexpr (s >= 1 && hs <= 27 && m < 2) ld16c<WS_H + (m * 112 + hs % 7) * 2048>(hb[hs % HP][m], hq[hs / 7], rs);
                if constexpr (ws >= 0 && m >= 2) dma16<(wz % NB) * 8192 + (m - 2) * 1024, ((wz % 7) * 16 + (m - 2) * 8) * DM * 2>(w2q[wz / 7], rw2, lb);
                SCHED0();
            });
        });
        SCHED0();
        ST(16);
        wait_lgkm0();
#pragma unroll
        for (int p = 0; p < 16; ++p) {
            const f32x16& c = acc[p >> 3][(p >> 2) & 1];
            f32x4 v = {c[4 * (p & 3)], c[4 * (p & 3) + 1], c[4 * (p & 3) + 2], c[4 * (p & 3) + 3]};
            *(LAS f32x4*)(lds + (p >> 1) * 8192 + kq * 2048 + (p & 1) * 1024 + (l16 ^ ((((p >> 3) * 4 + (p & 3)) * 2) * 16))) = v;
        }
        asm volatile("s_waitcnt vmcnt(0) ; PINW %0 %1" : "+v"(xr), "+v"(br) :: "memory");
    } else if constexpr (PART == 1) {
        const srd_t rwo = mk_srd(a.wo, DM * DM * 2);
        __builtin_amdgcn_s_setprio(3);
        bf16x8 wv[8][2];
        auto wo_on = [&]() {
#pragma unroll
            for (int h = 0; h < 8; ++h) {
                LAS char* p = upper + h * 2048 + trl;
                wv[h][0] = cat44(tr_read(p), tr_read(p + 1024));
                wv[h][1] = cat44(tr_read(p + 512), tr_read(p + 1536));
            }
        };
        auto poll_h = [&]() {
            poll_words<1>(rs, WS_FLAG + (unsigned)((mt >> 1) * 16 + 8 + (lane & 7)) * 256, tag);
            if (lane == 0) lds_set_word(lds + L2_CNT + 16);
        };
        if (!G.qkv) {
            if (kq == 0) poll_h();
            if (kq >= 2 && __builtin_amdgcn_readfirstlane((int)ph[0][0]) == 0) {
                const int sl = (G.mt * 8 + G.nt) * 2 + (kq & 1);
                const unsigned vo = (unsigned)(((sl >> 3) * 136 + (sl & 7) * 17 + (lane < 34 ? (lane >> 1) : 0)) * 32 + (lane & 1) * 16);
                for (unsigned spin = 0; spin <= SPIN_CAP; ++spin) {
                    static_for<0, 16>([&](auto M) { ld16c<WS_KVP + M.value * 32 * 136 * 32>(ph[M.value], vo, rs); });
                    wait16(ph);
                    if (kv_reduce_publish(a.ws, ph, sl, lane, tag)) break;
                    __builtin_amdgcn_s_sleep(2);
                }
            }
            ST(24);
        } else {
            const unsigned kvo = (unsigned)((4 * G.nt + kq) * 2176 + hh * 272 + (r32 < 16 ? r32 : 16) * 16);
            const unsigned hfo = WS_FLAG + (unsigned)((mt >> 1) * 16 + 8 + (lane & 7)) * 256;
            const unsigned wovo = wvo + kq * (128 * DM * 2);
            u32x4 raw[4]; unsigned fl;
            bool hdone = kq != 2;
            auto request = [&]() { static_for<0, 4>([&](auto I) { ld16c<WS_KVR + I.value * 544>(raw[I.value], kvo, rs); }); ld4c<0>(fl, hfo, rs); };
            if (kq < 2) static_for<0, 16>([&](auto P) { dma16<L2_A + P.value * 1024, P.value * 8 * DM * 2>(wovo, rwo, ldsb + kq * 16384); });
            request();
            asm volatile("s_waitcnt vmcnt(0) ; PINW %0 %1 %2 %3 %4" : "+v"(raw[0]), "+v"(raw[1]), "+v"(raw[2]), "+v"(raw[3]), "+v"(fl) :: "memory");
            for (unsigned spin = 0; spin <= SPIN_CAP; ++spin) {
                if (!hdone && __all(fl == tag)) { hdone = true; if (lane == 0) lds_set_word(lds + L2_CNT + 16); }
                unsigned bad = 0;
#pragma unroll
                for (int i = 0; i < 4; ++i) bad |= (raw[i][1] ^ tag) | (raw[i][3] ^ tag);
                if (__all(bad == 0u)) break;
                __builtin_amdgcn_s_sleep(1);
                request();
                asm volatile("s_waitcnt vmcnt(0) ; PINW %0 %1 %2 %3 %4" : "+v"(raw[0]), "+v"(raw[1]), "+v"(raw[2]), "+v"(raw[3]), "+v"(fl) :: "memory");
            }
            ST(24);
            f32x16 zero16;
#pragma unroll
            for (int i = 0; i < 16; ++i) zero16[i] = 0.f;
            const u32x4 k0 = {raw[0][0], raw[0][2], raw[1][0], raw[1][2]}, k1 = {raw[2][0], raw[2][2], raw[3][0], raw[3][2]};
#pragma unroll
            for (int w = 0; w < 4; ++w) {
                f32x16 nd = MFMA32(asbf(k0), asbf(ph[2 * w]), zero16);
                nd = MFMA32(asbf(k1), asbf(ph[2 * w + 1]), nd);
                const float inv = __builtin_amdgcn_rcpf(nd[8] + 1e-6f);
#pragma unroll
                for (int e = 0; e < 8; ++e) nd[e] *= inv;
                __builtin_amdgcn_raw_buffer_store_b128(pack8u<0>(nd), rs, WS_O + (unsigned)(((G.mt * 4 + w) * 32 + G.nt * 4 + kq) * 2048) + l16s, 0, SC1);
            }
            wait_vm<0>();
            unsigned old = 0;
            if (lane == 0) old = __hip_atomic_fetch_add((LAS unsigned*)(lds + L2_CNT), 1u, __ATOMIC_RELAXED, __HIP_MEMORY_SCOPE_WORKGROUP);
            if (lane == 0 && old == 3u) __hip_atomic_store((GAS unsigned*)(a.ws + WS_FLAG + (G.mt * 16 + G.nt) * 256), tag, RLX_AGENT);
            ST(25);
            if (!hdone) poll_h();
            __builtin_amdgcn_s_setprio(0);
        }
        __builtin_amdgcn_s_setprio(0);
        final_loads();
        poll_words<4>(rs, WS_FLAG + (unsigned)((mt >> 1) * 16 + 2 * kq + (lane & 1)) * 256, tag);
        __builtin_amdgcn_s_setprio(OPRIO);
        ST(14);
        const unsigned ovo = (unsigned)((2 * mt) * 32 + 8 * kq) * 2048 + l16s;
        static_for<0, 16>([&](auto I) { constexpr int i = I.value; ld16c<WS_O + (i & 1) * 65536 + (i >> 1) * 2048>(ph[i], ovo, rs); });
        wo_on();
        wait16(ph);
        ST(15);
#pragma unroll
        for (int h = 0; h < 8; ++h)
#pragma unroll
            for (int i = 0; i < 2; ++i)
#pragma unroll
                for (int j = 0; j < 2; ++j) acc[i][j] = MFMA32(wv[h][i], asbf(ph[2 * h + j]), acc[i][j]);
        ST(16);
        wait_lgkm0();
#pragma unroll
        for (int p = 0; p < 16; ++p) {
            const f32x16& c = acc[p >> 3][(p >> 2) & 1];
            f32x4 v = {c[4 * (p & 3)], c[4 * (p & 3) + 1], c[4 * (p & 3) + 2], c[4 * (p & 3) + 3]};
            *(LAS f32x4*)(upper + p * 1024 + (l16 ^ ((((p >> 3) * 4 + (p & 3)) * 2) * 16))) = v;
        }
        asm volatile("s_waitcnt vmcnt(0) ; PINW %0 %1" : "+v"(xr), "+v"(br) :: "memory");
    } else {
    wait_lgkm0();
    barrier();
    ST(17);
    const float r2 = __builtin_amdgcn_rcpf(vp2);
    const int p = (cg >> 2) * 8 + (tl >> 5) * 4 + (cg & 3);
    const int sl = ((tl & 31) ^ (cg * 2)) * 16;
    LAS char* pa = lds + L2_A + p * 1024 + sl;
    LAS char* pf = lds + (p >> 1) * 8192 + (p & 1) * 1024 + sl;
    u32x4 ov;
#pragma unroll
    for (int h = 0; h < 2; ++h) {
        f32x4 ss = (*(LAS f32x4*)(pa + h * 512) + *(LAS f32x4*)(pa + h * 512 + 16384)) + (*(LAS f32x4*)(pa + h * 512 + 2 * 16384) + *(LAS f32x4*)(pa + h * 512 + 3 * 16384));
        f32x4 so = (*(LAS f32x4*)(pf + h * 512) + *(LAS f32x4*)(pf + h * 512 + 2048)) + (*(LAS f32x4*)(pf + h * 512 + 4096) + *(LAS f32x4*)(pf + h * 512 + 6144));
        float res[4];
#pragma unroll
        for (int i = 0; i < 4; ++i) {
            const unsigned xu = xr[2 * h + (i >> 1)], bu = br[2 * h + (i >> 1)];
            const float xv = u2f((i & 1) ? (xu & 0xFFFF0000u) : (xu << 16));
            const float bv = u2f((i & 1) ? (bu & 0xFFFF0000u) : (bu << 16));
            const float o = so[i] * r2 + bv;
            const float gt = o * __builtin_amdgcn_rcpf(1.f + __builtin_amdgcn_exp2f(-o * LOG2E));
            res[i] = xv + ss[i] * gt;
        }
        ov[2 * h] = pk2(res[0], res[1]); ov[2 * h + 1] = pk2(res[2], res[3]);
    }
    *(GAS u32x4*)((char*)a.out + ofo) = ov;
    ST(18);
    if (G.bid == 0 && wave == 7) {
        for (unsigned spin = 0; spin <= SPIN_CAP; ++spin) {
            unsigned f0, f1, f2, f3;
            ld4c<WS_FLAG>(f0, (unsigned)lane * 256, rs); ld4c<WS_FLAG + 16384>(f1, (unsigned)lane * 256, rs);
            ld4c<WS_FLAG + 32768>(f2, (unsigned)lane * 256, rs); ld4c<WS_FLAG + 49152>(f3, (unsigned)lane * 256, rs);
            asm volatile("s_waitcnt vmcnt(0) ; PINW %0 %1 %2 %3" : "+v"(f0), "+v"(f1), "+v"(f2), "+v"(f3) :: "memory");
            if (__all(f0 == tag && f1 == tag && f2 == tag && f3 == tag)) break;
            __builtin_amdgcn_s_sleep(1);
        }
        if (lane == 0) __hip_atomic_store((GAS unsigned*)(a.ws + WS_CTRL), tag, RLX_AGENT);
    }
    }
}

__global__ void __launch_bounds__(512, 2) fused_kernel(Args a) {
    extern __shared__ __attribute__((aligned(16))) char smem[];
    LAS char* lds = (LAS char*)smem;
    unsigned long long stamps[28];
    ST(0);
    Geo G;
    G.bid = blockIdx.x;
    G.wave = __builtin_amdgcn_readfirstlane(threadIdx.x >> 6);
    G.lane = threadIdx.x & 63;
    const int xcd = G.bid & 7, loc = G.bid >> 3;
    const int sel = loc >> 2;
    G.mt = (xcd >> 1) * 4 + (loc & 3);
    G.nt = (xcd & 1) * 4 + (sel & 3);
    G.i16 = (xcd & 1) * 8 + sel;
    G.mt2 = G.mt * 2 + (xcd & 1);
    G.nt2 = sel;
    G.qkv = sel < 4;
#define RQ (sel & 3)
    G.rq = RQ;
    if (G.wave == 3) {
        unsigned long long pc;
        asm volatile("s_getpc_b64 %0" : "=s"(pc));
        const unsigned line = (unsigned)(loc + G.lane * 32);
        touch_ptr<L_DUMMY>((const char*)(pc & ~127ull) + (size_t)(line < CODE_LINES ? line : 0) * 128, (unsigned)(size_t)lds);
    }
    if (G.wave == 2) {
        const unsigned cx = (3u - (unsigned)((size_t)a.x >> 7)) & 7u, cq = (3u - (unsigned)((size_t)a.wqkv >> 7)) & 7u;
        const char* px = (const char*)a.x + (size_t)(G.mt * 128 + sel * 16 + (G.lane & 15)) * 1024 + cx * 128;
        const char* pq = (const char*)a.wqkv + (size_t)(loc * 16 + (G.lane & 15)) * 3072 + ((G.lane >> 4) % 3) * 1024 + cq * 128;
        touch_ptr<L_DUMMY>((G.lane < 16 || (int)(cq >> 2) != (xcd & 1)) ? px : pq, (unsigned)(size_t)lds);
    }
    unsigned wvo0[4], xvo0[4], epoch;
    u32x4 rowv[2] = {u32x4{0u, 0u, 0u, 0u}, u32x4{0u, 0u, 0u, 0u}};
    {
        const int lane = G.lane, wm = G.wave & 3, hh = lane >> 5;
        const unsigned ldsb = (unsigned)(size_t)lds;
        const int LDW = G.qkv ? 1536 : 1792;
        const srd_t rx = mk_srd(a.x, SEQ * DM * 2);
        const srd_t rw = mk_srd(G.qkv ? a.wqkv : a.w1, DM * LDW * 2);
        ld4c<WS_CTRL>(epoch, 0u, mk_srd(a.ws, WS_END));
        if (G.wave >= 4) {
            const unsigned ro = (unsigned)((G.mt * 128 + G.i16 * 8 + 2 * wm) * DM * 2 + lane * 16);
            ld16(rowv[0], ro, rx); ld16(rowv[1], ro + 1024, rx);
        } else {
            const int c = 2 * (G.mt >> 2) + hh, pc = c + (G.nt & 1);
            const int colb = G.qkv ? ((((c >> 1) + 1) % 3) * 512 + 64 * G.nt + 32 * hh) : (224 * G.nt + 32 * (pc >= 7 ? pc - 7 : pc));
            const unsigned uo = c < (G.qkv ? 6 : 7) ? (unsigned)((((G.mt & 3) * 128 + 32 * wm + ((lane >> 2) & 7)) * LDW + colb + (lane & 3) * 8) * 2) : 0x7F000000u;
#pragma unroll
            for (int i = 0; i < 4; ++i) dma16r(uo + (unsigned)(i * 8 * LDW * 2), rw, ldsb + L_UNIT + wm * 4096 + i * 1024);
        }
#pragma unroll
        for (int i = 0; i < 4; ++i) {
            const int row = 8 * (4 * wm + i) + (lane >> 3);
            xvo0[i] = (unsigned)(((G.mt * 128 + row) * DM + ((lane & 7) ^ ((row >> 1) & 7)) * 8) * 2);
            const int k8 = 2 * wm + (i >> 1), c = 2 * (i & 1) + hh;
            const int colb = G.qkv ? (((c >> 1) + 1) * 512 + 64 * G.nt + 32 * (c & 1)) : (224 * G.nt + 32 * (c + (G.nt & 1)));
            wvo0[i] = (unsigned)(((k8 * 8 + ((lane >> 2) & 7)) * LDW + colb + (lane & 3) * 8) * 2);
        }
        if (G.wave >= 4) {
            static_for<0, 4>([&](auto I) { dma16<SLOT + XOFF + I.value * 1024, 0>(xvo0[I.value], rx, ldsb + wm * 4096); });
        } else {
            static_for<0, 4>([&](auto I) { dma16<SLOT + I.value * 1024, 0>(wvo0[I.value], rw, ldsb + wm * 4096); });
        }
        ST(21);
        asm volatile("s_waitcnt vmcnt(4) ; PINW %0 %1 %2" : "+v"(epoch), "+v"(rowv[0]), "+v"(rowv[1]) :: "memory");
    }
    unsigned tag;
    float vp2 = 1.f;
    Fin F;
    if (G.wave < 4) {
        u32x4 ph[16];
        if (sel < 4) tag = phase1<true, 0>(a, lds, G, wvo0, xvo0, epoch, rowv, ph, vp2, stamps);
        else tag = phase1<false, 0>(a, lds, G, wvo0, xvo0, epoch, rowv, ph, vp2, stamps);
        phase2<0>(a, lds, G, tag, ph, vp2, F, stamps);
    } else {
        u32x4 ph[16];
#pragma unroll
        for (int i = 0; i < 16; ++i) ph[i] = u32x4{0u, 0u, 0u, 0u};
        if (sel < 4) tag = phase1<true, 1>(a, lds, G, wvo0, xvo0, epoch, rowv, ph, vp2, stamps);
        else tag = phase1<false, 1>(a, lds, G, wvo0, xvo0, epoch, rowv, ph, vp2, stamps);
        phase2<1>(a, lds, G, tag, ph, vp2, F, stamps);
    }
    {
        u32x4 ph[16];
        phase2<2>(a, lds, G, tag, ph, vp2, F, stamps);
    }
}

extern "C" void kernel_launch(void* const* d_in, const int* in_sizes, int n_in, void* d_out, int out_size, void* d_ws,
                              size_t ws_size, hipStream_t stream) {
    Args a;
    a.x = (const __bf16*)d_in[0]; a.wqkv = (const __bf16*)d_in[1]; a.wphi = (const __bf16*)d_in[2]; a.wo = (const __bf16*)d_in[3];
    a.w1 = (const __bf16*)d_in[5]; a.b1 = (const __bf16*)d_in[6]; a.w2 = (const __bf16*)d_in[7]; a.b2 = (const __bf16*)d_in[8];
    a.ws = (char*)d_ws; a.out = (__bf16*)d_out;
    (void)hipFuncSetAttribute((const void*)fused_kernel, hipFuncAttributeMaxDynamicSharedMemorySize, L_TOTAL);
    fused_kernel<<<256, 512, L_TOTAL, stream>>>(a);
}
```

```cpp
#include <hip/hip_runtime.h>
#include <hip/hip_bf16.h>
#include <stdint.h>
#include <type_traits>

typedef __attribute__((ext_vector_type(8))) __bf16 bf16x8;
typedef __attribute__((ext_vector_type(4))) __bf16 bf16x4;
typedef __attribute__((ext_vector_type(2))) __bf16 bf16x2;
typedef __attribute__((ext_vector_type(16))) float f32x16;
typedef __attribute__((ext_vector_type(4))) float f32x4;
typedef __attribute__((ext_vector_type(2))) float f32x2;
typedef __attribute__((ext_vector_type(4))) unsigned u32x4;
typedef __attribute__((ext_vector_type(2))) unsigned u32x2;
typedef __amdgpu_buffer_rsrc_t srd_t;
#define LAS __attribute__((address_space(3)))
#define GAS __attribute__((address_space(1)))
#define DI __device__ __forceinline__
#define MFMA32(a, b, c) __builtin_amdgcn_mfma_f32_32x32x16_bf16((a), (b), (c), 0, 0, 0)
#define SCHED0() __builtin_amdgcn_sched_barrier(0)
#define CFENCE() asm volatile("" ::: "memory")
#define RLX_AGENT __ATOMIC_RELAXED, __HIP_MEMORY_SCOPE_AGENT

constexpr int SEQ = 2048, DM = 512, NH = 32, FF = 1792;
constexpr float LOG2E = 1.4426950408889634f;
constexpr int SC1 = 16;
constexpr unsigned SPIN_CAP = 1u << 16;
constexpr unsigned CODE_LINES = 388;

constexpr unsigned WS_CTRL = 0;
constexpr unsigned WS_FLAG = 1024;
constexpr unsigned WS_RST  = WS_FLAG + 256 * 256;
constexpr unsigned WS_CSG  = WS_RST + 2048 * 16;
constexpr unsigned WS_KVR  = WS_CSG + 3328 * 32;
constexpr unsigned WS_KVP  = WS_KVR + 32 * 2176;
constexpr unsigned WS_H    = (WS_KVP + 16u * 32 * 136 * 32 + 4095) & ~4095u;
constexpr unsigned WS_O    = WS_H + 64u * 112 * 2048;
constexpr unsigned WS_END  = WS_O + 64u * 32 * 2048;

constexpr int SLOT = 32768, XOFF = 16384;
constexpr int chunk_base(bool qkv, int c) { return qkv ? (c == 0 ? 131072 : (c == 3 ? 81920 : 98304 + (c - 1) * 16384)) : (c == 0 ? 98304 : (c == 1 ? 122880 : (c == 2 ? 40960 : 65536))); }
constexpr int L_UNIT = 131072;
constexpr int L_CSP = 147456;
constexpr int L_RR = L_CSP + 1024;
constexpr int L_AUX = L_RR + 1024;
constexpr int L_DUMMY = L_AUX + 2048;
constexpr int L_FIX = L_DUMMY + 256;
constexpr int L_RED = 40960;
constexpr int L_PHI = 98304;
constexpr int NR = 8;
constexpr int NB = 11;
constexpr int L2_CNT = NB * 8192;
constexpr int L2_A = 98304;
constexpr int L_TOTAL = 163840;
static_assert(L_FIX + 8192 <= L_TOTAL, "lds");

template <int LOFF, int SOFF> DI void dma16(unsigned voff, srd_t srd, unsigned lbase) {
    unsigned t;
    lbase = (unsigned)__builtin_amdgcn_readfirstlane((int)lbase);
    asm volatile("s_add_i32 m0, %3, %4\n\ts_mov_b32 %0, %5\n\tbuffer_load_dwordx4 %1, %2, %0 offen lds"
                 : "=&s"(t) : "v"(voff), "s"(srd), "s"(lbase), "i"(LOFF), "i"(SOFF) : "memory", "scc");
}
template <int LOFF> DI void touch_ptr(const void* p, unsigned lbase) {
    asm volatile("s_add_i32 m0, %1, %2\n\ts_nop 0\n\tglobal_load_lds_dword %0, off" :: "v"(p), "s"(lbase), "i"(LOFF) : "memory", "scc");
}
template <int OFF> DI void ld16c(u32x4& d, unsigned voff, srd_t srd) {
    asm volatile("buffer_load_dwordx4 %0, %1, %2, %3 offen offset:%4 sc1" : "=v"(d) : "v"(voff), "s"(srd), "s"(OFF & ~4095), "i"(OFF & 4095) : "memory");
}
template <int OFF> DI void ld4c(unsigned& d, unsigned voff, srd_t srd) {
    asm volatile("buffer_load_dword %0, %1, %2, %3 offen offset:%4 sc1" : "=v"(d) : "v"(voff), "s"(srd), "s"(OFF & ~4095), "i"(OFF & 4095) : "memory");
}
template <int OFF> DI void ld8c(u32x2& d, unsigned voff, srd_t srd) {
    asm volatile("buffer_load_dwordx2 %0, %1, %2, %3 offen offset:%4 sc1" : "=v"(d) : "v"(voff), "s"(srd), "s"(OFF & ~4095), "i"(OFF & 4095) : "memory");
}
DI void dma16r(unsigned voff, srd_t srd, unsigned lds_dst) {
    asm volatile("s_mov_b32 m0, %2\n\ts_nop 0\n\tbuffer_load_dwordx4 %0, %1, 0 offen lds" :: "v"(voff), "s"(srd), "s"(lds_dst) : "memory");
}
DI void ld16(u32x4& d, unsigned voff, srd_t srd) { asm volatile("buffer_load_dwordx4 %0, %1, %2, 0 offen" : "=v"(d) : "v"(voff), "s"(srd) : "memory"); }
DI float fmx(float a, float b) { return __builtin_elementwise_maximum(a, b); }
DI void wait_lgkm0() { __builtin_amdgcn_s_waitcnt(0xC07F); CFENCE(); }
template <int N> DI void wait_vm() { asm volatile("s_waitcnt vmcnt(%0)" ::"i"(N) : "memory"); }
DI void barrier() { __builtin_amdgcn_s_barrier(); CFENCE(); }
#define PIN(x) asm volatile("" : "+v"(x))
DI srd_t mk_srd(const void* p, unsigned bytes) { return __builtin_amdgcn_make_buffer_rsrc((void*)p, 0, (int)bytes, 0x00020000); }

DI bf16x4 tr_read(LAS char* p) { return __builtin_amdgcn_ds_read_tr16_b64_v4bf16((LAS bf16x4*)p); }
DI bf16x8 cat44(bf16x4 a, bf16x4 b) { return __builtin_shufflevector(a, b, 0, 1, 2, 3, 4, 5, 6, 7); }
DI float dot2(unsigned a, unsigned b, float c) {
    return __builtin_amdgcn_fdot2_f32_bf16(__builtin_bit_cast(bf16x2, a), __builtin_bit_cast(bf16x2, b), c, false);
}
DI unsigned pk2(float a, float b) { f32x2 v = {a, b}; return __builtin_bit_cast(unsigned, __builtin_convertvector(v, bf16x2)); }
template <int S> DI u32x4 pack8u(const f32x16& x) {
    u32x4 p = {pk2(x[8 * S], x[8 * S + 1]), pk2(x[8 * S + 2], x[8 * S + 3]), pk2(x[8 * S + 4], x[8 * S + 5]), pk2(x[8 * S + 6], x[8 * S + 7])};
    return p;
}
template <int S> DI bf16x8 pack8(const f32x16& x) { return __builtin_bit_cast(bf16x8, pack8u<S>(x)); }
DI bf16x8 asbf(u32x4 u) { return __builtin_bit_cast(bf16x8, u); }
DI float u2f(unsigned u) { return __builtin_bit_cast(float, u); }
DI unsigned f2u(float f) { return __builtin_bit_cast(unsigned, f); }
DI unsigned hi2(unsigned lo, unsigned hi) { return __builtin_amdgcn_perm(hi, lo, 0x07060302u); }
DI void split3(float v, unsigned& s0, unsigned& s1, unsigned& s2) {
    s0 = f2u(v) & 0xFFFF0000u;
    float r = v - u2f(s0);
    s1 = f2u(r) & 0xFFFF0000u;
    s2 = f2u(r - u2f(s1));
}
DI float half_sum(float v) { auto s = __builtin_amdgcn_permlane32_swap(f2u(v), f2u(v), false, false); return u2f(s[0]) + u2f(s[1]); }
DI float wave_sum(float v) {
#define DPPADD(ctrl, rm) v += u2f((unsigned)__builtin_amdgcn_update_dpp(0, (int)f2u(v), ctrl, rm, 0xf, true))
    DPPADD(0x111, 0xf); DPPADD(0x112, 0xf); DPPADD(0x114, 0xf); DPPADD(0x118, 0xf); DPPADD(0x142, 0xa); DPPADD(0x143, 0xc);
#undef DPPADD
    return u2f((unsigned)__builtin_amdgcn_readlane((int)f2u(v), 63));
}
template <int C, int N, class F> DI void static_for(F&& f) { if constexpr (C < N) { f(std::integral_constant<int, C>{}); static_for<C + 1, N>(f); } }

struct Args {
    const __bf16* x; const __bf16* wqkv; const __bf16* wphi; const __bf16* wo;
    const __bf16* w1; const __bf16* b1; const __bf16* w2; const __bf16* b2;
    char* ws; __bf16* out;
};
struct Geo { int bid, wave, lane, mt, nt, i16, mt2, nt2, qkv, rq; };
#define IC(v) std::integral_constant<int, (v)>{}

template <int N> DI void gather_wait(u32x4& gst, u32x2& gv2, u32x4& gcs) {
    asm volatile("s_waitcnt vmcnt(%3) ; PINW %0 %1 %2" : "+v"(gst), "+v"(gv2), "+v"(gcs) : "i"(N) : "memory");
}
DI void wait16(u32x4 (&v)[16]) {
    asm volatile("s_waitcnt vmcnt(0) ; PINW %0 %1 %2 %3 %4 %5 %6 %7 %8 %9 %10 %11 %12 %13 %14 %15"
                 : "+v"(v[0]), "+v"(v[1]), "+v"(v[2]), "+v"(v[3]), "+v"(v[4]), "+v"(v[5]), "+v"(v[6]), "+v"(v[7]),
                   "+v"(v[8]), "+v"(v[9]), "+v"(v[10]), "+v"(v[11]), "+v"(v[12]), "+v"(v[13]), "+v"(v[14]), "+v"(v[15]) :: "memory");
}

DI bool kv_reduce_publish(char* ws, u32x4 (&v)[16], int sl, int lane, unsigned tag) {
    unsigned bad = 0;
#pragma unroll
    for (int m = 0; m < 16; ++m) bad |= (v[m][1] ^ tag) | (v[m][3] ^ tag);
    if (!__all(bad == 0u)) return false;
    float s0[16], s1[16];
#pragma unroll
    for (int m = 0; m < 16; ++m) { s0[m] = u2f(v[m][0]); s1[m] = u2f(v[m][2]); }
#pragma unroll
    for (int st = 8; st >= 1; st >>= 1)
#pragma unroll
        for (int m = 0; m < st; ++m) { s0[m] += s0[m + st]; s1[m] += s1[m + st]; }
    const int g = (sl & 7) * 17 + (lane < 34 ? (lane >> 1) : 0), row = g >> 3, fq = g & 7;
    const unsigned off = WS_KVR + (sl >> 3) * 2176 + fq * 272 + row * 16 + (lane & 1) * 8;
    if (lane < 34)
        __hip_atomic_store((GAS unsigned long long*)(ws + off), (unsigned long long)pk2(s0[0], s1[0]) | ((unsigned long long)tag << 32), RLX_AGENT);
    return true;
}
template <bool QKV, int WN>
DI unsigned phase1(const Args& a, LAS char* lds, const Geo& G, const unsigned (&wvo0)[4], const unsigned (&xvo0)[4],
                   unsigned epoch, const u32x4 (&rowv)[2], u32x4 (&ph)[16], float& vp2) {
    constexpr int NTB = QKV ? 2 : 3;
    constexpr int NB2 = QKV ? 1 : (WN == 0 ? 1 : 2);
    constexpr int NG = 2 + NB2;
    constexpr int NU = QKV ? 24 : 28;
    constexpr int LDW = QKV ? 1536 : 1792;
    constexpr int CP = NTB * 2;
    const int lane = G.lane, wave = G.wave, wm = wave & 3, mt = G.mt, nt = G.nt;
    const unsigned ldsb = (unsigned)(size_t)lds;
    const int hh = lane >> 5, r32 = lane & 31, blk = (lane >> 4) & 1, q4 = (lane & 15) >> 2, p4 = lane & 3;
    const srd_t rs = mk_srd(a.ws, WS_END);
    const srd_t rx = mk_srd(a.x, SEQ * DM * 2);
    const srd_t rw = mk_srd(QKV ? a.wqkv : a.w1, DM * LDW * 2);
    const srd_t rw2 = mk_srd(a.w2, FF * DM * 2);
    const srd_t rwo = mk_srd(a.wo, DM * DM * 2);
    auto ptile = [&](int c) { const int p = c + (nt & 1); return p >= 7 ? p - 7 : p; };
    auto colb_of = [&](int c) { return QKV ? ((((c >> 1) + 1) % 3) * 512 + 64 * nt + 32 * (c & 1)) : (224 * nt + 32 * ptile(c)); };
    auto tile_of = [&](int t) { return t < 2 ? 2 * t + WN : (QKV ? 4 + WN : 4 + WN + (t - 2)); };
    auto dma_pair = [&](auto JI, auto II) {
        constexpr int j = JI.value, i = II.value, sb = ((j + 1) % 3) * SLOT;
        if constexpr (i < 4) dma16<sb + i * 1024, j * 64 * LDW * 2>(wvo0[i], rw, ldsb + wm * 4096);
        else dma16<sb + XOFF + (i - 4) * 1024, j * 128>(xvo0[i - 4], rx, ldsb + wm * 4096);
    };
    const unsigned w2vo = (unsigned)((((lane >> 2) & 7) * DM + G.nt2 * 64 + hh * 32 + (lane & 3) * 8) * 2);
    unsigned w2q[3];
#pragma unroll
    for (int q = 0; q < 3; ++q) w2q[q] = w2vo + (unsigned)((wm * 448 + ((q + G.rq) & 3) * 112) * DM * 2);
    auto dma_w2 = [&](auto PI) {
        constexpr int r = PI.value >> 1, t = PI.value & 1;
        dma16<(r % NB) * 8192 + t * 1024, ((r % 7) * 16 + t * 8) * DM * 2>(w2q[r / 7], rw2, ldsb + wm * 2048);
    };
    const unsigned wovo = w2vo + wm * (128 * DM * 2);
    auto dma_wo = [&](auto PI) { dma16<L2_A + PI.value * 1024, PI.value * 8 * DM * 2>(wovo, rwo, ldsb + wm * 16384); };
    unsigned cvo[WN == 1 ? NTB : 1];
    if constexpr (WN == 1) {
#pragma unroll
        for (int i = 0; i < NTB; ++i) {
            const int st = 2 * i + hh, k8 = 4 * wm + (st >= NTB ? 1 : 0), c = st >= NTB ? st - NTB : st;
            cvo[i] = (unsigned)(((k8 * 8 + ((lane >> 2) & 7)) * LDW + colb_of(4 + c) + (lane & 3) * 8) * 2);
        }
    }
    auto dma_chunk = [&](auto CI, auto II) {
        constexpr int c = CI.value, i = II.value;
        dma16<chunk_base(QKV, c) + i * 1024, (c * 128 + (i / NTB) * 16) * LDW * 2>(cvo[i % NTB], rw, ldsb + wm * (CP * 1024));
    };
    const int srow = mt * 128 + G.i16 * 8 + 2 * wm;
    const unsigned tag = (unsigned)__builtin_amdgcn_readfirstlane((int)epoch) + 1u;
    const unsigned trl = (4 * hh + q4) * 64 + blk * 32 + p4 * 8;
    if constexpr (WN == 0) {
        LAS char* p = lds + L_UNIT + wm * 4096 + trl;
        const unsigned one = 0x3F803F80u;
        float su[2];
#pragma unroll
        for (int u = 0; u < 2; ++u) {
            float s = 0.f;
#pragma unroll
            for (int i = 0; i < 4; ++i) { u32x2 w = __builtin_bit_cast(u32x2, tr_read(p + u * 512 + i * 1024)); s = dot2(w[1], one, dot2(w[0], one, s)); }
            su[u] = s;
        }
        auto sw = __builtin_amdgcn_permlane32_swap(f2u(su[0]), f2u(su[1]), false, false);
        *(LAS float*)(lds + L_CSP + ((hh * 4 + wm) * 32 + r32) * 4) = u2f(sw[0]) + u2f(sw[1]);
        static_for<0, 8>([&](auto I) { dma_pair(IC(1), I); });
    } else {
        float mean[2], vv[2];
#pragma unroll
        for (int r = 0; r < 2; ++r) {
            float e[8];
#pragma unroll
            for (int i = 0; i < 4; ++i) { e[2 * i] = u2f(rowv[r][i] << 16); e[2 * i + 1] = u2f(rowv[r][i] & 0xFFFF0000u); }
            mean[r] = wave_sum(((e[0] + e[1]) + (e[2] + e[3])) + ((e[4] + e[5]) + (e[6] + e[7]))) * (1.f / DM);
            float q = 0.f;
#pragma unroll
            for (int i = 0; i < 8; ++i) { float d = e[i] - mean[r]; q = __builtin_fmaf(d, d, q); }
            vv[r] = wave_sum(q) * (1.f / DM) + 1e-5f;
        }
        u32x4 g = {f2u(lane ? mean[1] : mean[0]), tag, f2u(lane ? vv[1] : vv[0]), tag};
        if (lane < 2) __builtin_amdgcn_raw_buffer_store_b128(g, rs, WS_RST + (srow + lane) * 16, 0, SC1);
    }

    LAS char* wb[2]; LAS char* xb[2][4];
#pragma unroll
    for (int s = 0; s < 2; ++s) {
        wb[s] = lds + s * 65536 + hh * 2048 + WN * 512 + q4 * 64 + blk * 32 + p4 * 8;
        PIN(wb[s]);
        const int row = 32 * wm + r32;
#pragma unroll
        for (int k = 0; k < 4; ++k) { xb[s][k] = lds + s * 65536 + XOFF + row * 128 + (((2 * k + hh) ^ ((row >> 1) & 7)) * 16); PIN(xb[s][k]); }
    }
    const int csbase = QKV ? nt * 192 : 1536 + nt * 224;

    f32x16 acc[2];
#pragma unroll
    for (int t = 0; t < 2; ++t)
#pragma unroll
        for (int i = 0; i < 16; ++i) acc[t][i] = 0.f;
    bf16x8 xf[32], wf[3][2];
    auto read_x = [&](int g) { const int sl = ((g >> 2) + 1) % 3; xf[g] = *(LAS bf16x8*)(xb[sl >> 1][g & 3] + (sl & 1) * SLOT); };
    auto read_w = [&](int g, int t) {
        const int sl = ((g >> 2) + 1) % 3;
        LAS char* p = wb[sl >> 1] + (sl & 1) * SLOT + (g & 3) * 4096 + t * 1024;
        wf[g % 3][t] = cat44(tr_read(p), tr_read(p + 256));
    };
    u32x4 gst, gcs; u32x2 gv2;
    const int mytile = wm < NG ? (wm < 2 ? 2 * wm + WN : (QKV ? 4 + WN : 2 + WN + wm)) : WN;
    const unsigned gvo_st = (unsigned)(mt * 128 + 32 * wm + r32) * 16;
    const unsigned gvo_v2 = (unsigned)(G.mt2 * 64 + wave * 8 + (lane >> 3)) * 16;
    const unsigned gvo_cs = (unsigned)(csbase + mytile * 32 + r32) * 32 + hh * 16;
    auto gather = [&]() {
        ld16c<WS_RST>(gst, gvo_st, rs);
        ld8c<WS_RST + 8>(gv2, gvo_v2, rs);
        ld16c<WS_CSG>(gcs, gvo_cs, rs);
    };
    float vpe = 1.f, r_t = 1.f;
    bf16x8 fixT, fixF[NG];
    auto gather_check_and_fix = [&]() {
        for (unsigned spin = 0;; ++spin) {
            const unsigned bad = (gst[1] ^ tag) | (gst[3] ^ tag) | (gv2[1] ^ tag) | (gcs[1] ^ tag) | (gcs[3] ^ tag);
            if (__all(bad == 0u) || spin > SPIN_CAP) break;
            __builtin_amdgcn_s_sleep(1);
            gather();
            gather_wait<0>(gst, gv2, gcs);
        }
        vpe = u2f(gst[2]);
        vp2 = u2f(gv2[0]);
        r_t = __builtin_amdgcn_rsqf(vpe);
        unsigned s0, s1, s2;
        split3(hh ? vpe * r_t : -u2f(gst[0]), s0, s1, s2);
        u32x4 u;
        u[0] = hi2(s0, s1);
        u[1] = hh ? (s2 >> 16) : u[0];
        u[2] = hh ? 0u : hi2(s2, s0);
        u[3] = 0u;
        fixT = asbf(u);
        if (QKV) *(LAS float*)(lds + L_RR + (wave * 32 + r32) * 4) = r_t;
        unsigned c0, c1, c2;
        split3(half_sum(u2f(gcs[0]) + u2f(gcs[2])), c0, c1, c2);
        unsigned bb = 0;
        if (!QKV) bb = *(LAS unsigned short*)(lds + L_AUX + (ptile(mytile) * 32 + r32) * 2);
        u32x4 w;
        w[0] = hh ? (bb | (bb << 16)) : hi2(c0, c0);
        w[1] = hh ? bb : hi2(c1, c1);
        w[2] = hh ? 0u : hi2(c0, c2);
        w[3] = 0u;
        *(LAS u32x4*)(lds + L_FIX + wave * 1024 + lane * 16) = w;
    };

    wait_vm<WN == 0 ? 8 : 1>();
    wait_lgkm0();
    barrier();
#pragma unroll
    for (int g = 0; g < 2; ++g) { read_x(g); read_w(g, 0); read_w(g, 1); }
    if constexpr (WN == 1) {
        const int idx = 16 * wm + (lane & 15);
        LAS float* p = (LAS float*)(lds + L_CSP) + (idx >> 5) * 128 + (idx & 31);
        u32x2 gq = {f2u((p[0] + p[32]) + (p[64] + p[96])), tag};
        const int uc = 2 * (mt >> 2) + (wm >> 1);
        if (lane < 16 && uc < NU / 4) __builtin_amdgcn_raw_buffer_store_b64(gq, rs, WS_CSG + (csbase + uc * 32 + (idx & 31)) * 32 + (mt & 3) * 8, 0, SC1);
        if (wave == 4) {
            if (QKV) {
                const srd_t rp = mk_srd(a.wphi, NH * 256 * 2);
                dma16<L_AUX, 0>((unsigned)(nt * 2048 + lane * 16), rp, ldsb);
                dma16<L_AUX + 1024, 1024>((unsigned)(nt * 2048 + lane * 16), rp, ldsb);
            } else {
                const srd_t rb = mk_srd(a.b1, FF * 2);
                dma16<L_AUX, 0>((unsigned)(nt * 448 + lane * 16), rb, ldsb);
            }
        }
    }
    static_for<0, 32>([&](auto GI) {
        constexpr int g = GI.value, j = g >> 2;
        SCHED0();
        if constexpr ((g & 3) == 2) {
            constexpr int JG = WN == 0 ? 4 : 5;
            if constexpr (j == JG + 1) { PIN(acc[0]); PIN(acc[1]); }
            if constexpr (WN == 0) {
                constexpr int n = j <= 6 ? 8 : 63;
                if constexpr (j == JG + 1) gather_wait<n>(gst, gv2, gcs);
                else wait_vm<n>();
            } else if constexpr (j == 4) wait_vm<1>();
            else if constexpr (j == JG + 1) gather_wait<CP>(gst, gv2, gcs);
            else if constexpr (j == 7) wait_vm<CP>();
            wait_lgkm0();
            barrier();
            if constexpr (WN == 1 && j == 1) {
                const int gw = G.bid * 4 + wm;
                const char* p = lane < 14 ? (const char*)a.w2 + (size_t)(gw * 14 + lane) * 128 : (const char*)a.wo + (size_t)(gw * 4 + (lane & 3)) * 128;
                touch_ptr<L_DUMMY>(p, ldsb);
            }
            if constexpr (WN == 1 && j == 3) {
                PIN(acc[0]); PIN(acc[1]);
                const int loc = G.bid >> 3;
                const unsigned c2 = (3u - (unsigned)((size_t)a.w2 >> 7)) & 7u, co = (3u - (unsigned)((size_t)a.wo >> 7)) & 7u;
                const char* p = lane < 14 ? (const char*)a.w2 + (size_t)((loc * 4 + wm) * 14 + lane) * 1024 + c2 * 128
                                          : (const char*)a.wo + (size_t)((loc * 4 + wm) * 4 + (lane & 3)) * 1024 + co * 128;
                touch_ptr<L_DUMMY>(p, ldsb);
            }
            if constexpr (j == JG) gather();
            if constexpr (WN == 1 && j == 7) static_for<0, CP>([&](auto I) { dma_chunk(IC(3), I); });
            if constexpr (j == JG + 1) gather_check_and_fix();
            if constexpr (j == 7) {
#pragma unroll
                for (int t = 0; t < NG; ++t) fixF[t] = *(LAS bf16x8*)(lds + L_FIX + (WN * 4 + t) * 1024 + lane * 16);
            }
            SCHED0();
        }
        constexpr int jb = (g - 2) >> 2, q = (g - 2) & 3;
        static_for<0, 2>([&](auto TI) {
            constexpr int t = TI.value;
            if (QKV && t == 1) acc[t] = MFMA32(xf[g], wf[g % 3][t], acc[t]);
            else acc[t] = MFMA32(wf[g % 3][t], xf[g], acc[t]);
            if constexpr (g + 2 < 32) {
                if (t == 0) read_x(g + 2);
                read_w(g + 2, t);
            }
            constexpr int i = q * 2 + t;
            if constexpr (WN == 0) {
                if constexpr (g < 2) { dma_pair(IC(2), IC(4 * g + 2 * t)); dma_pair(IC(2), IC(4 * g + 2 * t + 1)); }
                else if constexpr (jb <= 4) dma_pair(IC(jb + 3), IC(i));
                else if constexpr (jb == 5) dma_w2(IC(i));
                else if constexpr (jb == 6 && i < 2) dma_w2(IC(8 + i));
            } else if constexpr (g >= 2 && jb >= 4 && jb <= 6 && i < CP) dma_chunk(IC(jb - 4), IC(i));
            SCHED0();
        });
    });
    const int tt = mt * 4 + wm;
    f32x16 zero16;
#pragma unroll
    for (int i = 0; i < 16; ++i) zero16[i] = 0.f;
    auto store_h = [&](f32x16& c, int tile) {
#pragma unroll
        for (int i = 0; i < 16; ++i) { float v = fmx(c[i], 0.f); c[i] = v * v; }
        const unsigned off = WS_H + (unsigned)((tt * 112 + nt * 14 + ptile(tile) * 2) * 2048 + lane * 16 + (lane >= 24 ? 128 : 0));
        __builtin_amdgcn_raw_buffer_store_b128(pack8u<0>(c), rs, off, 0, SC1);
        __builtin_amdgcn_raw_buffer_store_b128(pack8u<1>(c), rs, off + 2048, 0, SC1);
    };
    bf16x8 wfr[2];
    const float kscale = r_t * LOG2E;
    auto feature_map2 = [&](f32x16 (&p)[2], u32x4 (&F)[2][2]) {
        float m[2][2], sm[2][2];
#pragma unroll
        for (int X = 0; X < 2; ++X)
#pragma unroll
            for (int h = 0; h < 2; ++h) {
                const int o = 8 * h;
                m[X][h] = fmx(fmx(fmx(fmx(p[X][o], p[X][o + 1]), p[X][o + 2]), fmx(fmx(p[X][o + 3], p[X][o + 4]), p[X][o + 5])), fmx(p[X][o + 6], p[X][o + 7]));
            }
#pragma unroll
        for (int X = 0; X < 2; ++X) {
            auto s = __builtin_amdgcn_permlane32_swap(f2u(m[X][0]), f2u(m[X][1]), false, false);
            float c = -kscale * fmx(u2f(s[0]), u2f(s[1]));
            auto s2 = __builtin_amdgcn_permlane32_swap(f2u(c), f2u(c), false, false);
            m[X][0] = u2f(s2[0]); m[X][1] = u2f(s2[1]);
        }
#pragma unroll
        for (int i = 0; i < 16; ++i)
#pragma unroll
            for (int X = 0; X < 2; ++X) p[X][i] = __builtin_amdgcn_exp2f(__builtin_fmaf(p[X][i], kscale, m[X][i >> 3]));
#pragma unroll
        for (int X = 0; X < 2; ++X)
#pragma unroll
            for (int h = 0; h < 2; ++h) {
                const int o = 8 * h;
                sm[X][h] = ((p[X][o] + p[X][o + 1]) + (p[X][o + 2] + p[X][o + 3])) + ((p[X][o + 4] + p[X][o + 5]) + (p[X][o + 6] + p[X][o + 7]));
            }
#pragma unroll
        for (int X = 0; X < 2; ++X) {
            auto s = __builtin_amdgcn_permlane32_swap(f2u(sm[X][0]), f2u(sm[X][1]), false, false);
            float c = __builtin_amdgcn_rcpf(u2f(s[0]) + u2f(s[1]));
            auto s2 = __builtin_amdgcn_permlane32_swap(f2u(c), f2u(c), false, false);
            sm[X][0] = u2f(s2[0]); sm[X][1] = u2f(s2[1]);
        }
#pragma unroll
        for (int X = 0; X < 2; ++X) {
#pragma unroll
            for (int i = 0; i < 16; ++i) p[X][i] *= sm[X][i >> 3];
            F[X][0] = pack8u<0>(p[X]); F[X][1] = pack8u<1>(p[X]);
        }
    };
    if constexpr (!QKV) {
        acc[0] = MFMA32(fixF[0], fixT, acc[0]);
        acc[1] = MFMA32(fixF[1], fixT, acc[1]);
        store_h(acc[0], WN);
        store_h(acc[1], 2 + WN);
    } else {
        acc[0] = MFMA32(fixF[0], fixT, acc[0]);
        acc[1] = MFMA32(fixT, fixF[1], acc[1]);
        bf16x8 idf[2];
#pragma unroll
        for (int s = 0; s < 2; ++s) {
            u32x4 u;
#pragma unroll
            for (int i = 0; i < 4; ++i) {
                int f0 = 16 * s + 8 * (i >> 1) + 4 * hh + 2 * (i & 1);
                u[i] = (r32 == f0 ? 0x3F80u : 0u) | (r32 == f0 + 1 ? 0x3F800000u : 0u);
            }
            idf[s] = asbf(u);
        }
#pragma unroll
        for (int X = 0; X < 2; ++X) {
            LAS char* p = lds + L_AUX + (2 * WN + X) * 512 + (4 * hh + q4) * 32 + p4 * 8;
            u32x4 u = __builtin_bit_cast(u32x4, cat44(tr_read(p), tr_read(p + 256)));
            const unsigned sg = blk ? 0x80008000u : 0u;
#pragma unroll
            for (int i = 0; i < 4; ++i) u[i] ^= sg;
            wfr[X] = asbf(u);
        }
#pragma unroll
        for (int g = 0; g < 4; ++g) {
            f32x4 rr = *(LAS f32x4*)(lds + L_RR + (wave * 32 + 8 * g + 4 * hh) * 4);
#pragma unroll
            for (int i = 0; i < 4; ++i) acc[1][4 * g + i] *= rr[i];
        }
        const u32x4 v0 = pack8u<0>(acc[1]), v1 = pack8u<1>(acc[1]);
        const bf16x8 kb0 = pack8<0>(acc[0]), kb1 = pack8<1>(acc[0]);
        {
            f32x16 p[2], d[2], kv[2];
            u32x4 F[2][2];
            p[0] = MFMA32(wfr[0], kb0, zero16);
            p[1] = MFMA32(wfr[1], kb1, zero16);
            feature_map2(p, F);
#pragma unroll
            for (int X = 0; X < 2; ++X) d[X] = MFMA32(asbf(F[X][0]), idf[0], zero16);
#pragma unroll
            for (int X = 0; X < 2; ++X) d[X] = MFMA32(asbf(F[X][1]), idf[1], d[X]);
            u32x4 b[2][2];
#pragma unroll
            for (int X = 0; X < 2; ++X)
#pragma unroll
                for (int i = 0; i < 4; ++i) { b[X][0][i] = (blk == X) ? v0[i] : 0x3F803F80u; b[X][1][i] = (blk == X) ? v1[i] : 0x3F803F80u; }
#pragma unroll
            for (int X = 0; X < 2; ++X) kv[X] = MFMA32(pack8<0>(d[X]), asbf(b[X][0]), zero16);
#pragma unroll
            for (int X = 0; X < 2; ++X) kv[X] = MFMA32(pack8<1>(d[X]), asbf(b[X][1]), kv[X]);
#pragma unroll
            for (int X = 0; X < 2; ++X) {
                const int row = X == 0 ? r32 : (r32 == 15 ? 16 : r32 - 16);
                const bool use = X == 0 ? (r32 <= 16) : (r32 >= 15);
                LAS char* dst = lds + (use ? L_RED + (((WN * 2 + X) * 4 + wm) * 17 + row) * 144 : L_RED + 39168 + (r32 & 7) * 144) + hh * 16;
#pragma unroll
                for (int g = 0; g < 4; ++g) {
                    f32x4 v = {kv[X][4 * g], kv[X][4 * g + 1], kv[X][4 * g + 2], kv[X][4 * g + 3]};
                    *(LAS f32x4*)(dst + g * 32) = v;
                }
            }
        }
        wait_lgkm0();
        barrier();
        if constexpr (WN == 1) {
#pragma unroll
            for (int it = 0; it < 5; ++it) {
                const int o = it * 64 + lane;
                if (it < 4 || lane < 16) {
                    LAS char* src = lds + L_RED + (wm * 4 * 17 + (o >> 4)) * 144 + (o & 15) * 8;
                    f32x2 v = (*(LAS f32x2*)(src) + *(LAS f32x2*)(src + 2448)) + (*(LAS f32x2*)(src + 2 * 2448) + *(LAS f32x2*)(src + 3 * 2448));
                    u32x4 g0 = {f2u(v[0]), tag, f2u(v[1]), tag};
                    __builtin_amdgcn_raw_buffer_store_b128(g0, rs, WS_KVP + (unsigned)((mt * 32 + nt * 4 + wm) * 272 + o) * 16, 0, SC1);
                }
            }
        }
    }
    f32x16 bc[NB2];
#pragma unroll
    for (int t = 0; t < NB2; ++t) bc[t] = MFMA32(fixF[2 + t], fixT, zero16);
    {
        constexpr int CB0 = QKV ? 81920 : 40960;
        LAS char* q0 = lds + hh * (NTB * 512) + WN * 512 + q4 * 64 + blk * 32 + p4 * 8 + CB0;
        LAS char* q1 = q0 + (98304 - CB0);
        PIN(q0); PIN(q1);
        bf16x8 qf[4][NB2];
        auto read_q = [&](int g, int t) {
            const int cb = chunk_base(QKV, g >> 3);
            LAS char* p = (cb >= 98304 ? q1 + (cb - 98304) : q0 + (cb - CB0)) + (g & 7) * (NTB * 1024) + t * 512;
            qf[g & 3][t] = cat44(tr_read(p), tr_read(p + 256));
        };
#pragma unroll
        for (int g = 0; g < 3; ++g)
#pragma unroll
            for (int t = 0; t < NB2; ++t) read_q(g, t);
        static_for<0, 32>([&](auto GI) {
            constexpr int g = GI.value;
            SCHED0();
            if constexpr (g == 8 || g == 16 || g == 24) {
                if constexpr (g == 8 && WN == 1) wait_vm<QKV ? 5 : 4>();
                wait_lgkm0(); barrier();
            }
            static_for<0, NB2>([&](auto TI) {
                constexpr int t = TI.value;
                bc[t] = MFMA32(qf[g & 3][t], xf[g], bc[t]);
                if constexpr (g + 3 < 32) read_q(g + 3, t);
                if constexpr (WN == 0) {
                    if constexpr (QKV && g >= 8 && g < 18) dma_w2(IC(10 + g - 8));
                    if constexpr (!QKV && g >= 24 && g < 30) dma_w2(IC(10 + g - 24));
                } else if constexpr (!QKV && g >= 16 && g < 24) dma_wo(IC(2 * (g - 16) + t));
                else if constexpr (QKV && g >= 9 && g < 25) { if (wm >= 2) dma_wo(IC(g - 9)); }
                SCHED0();
            });
        });
    }
    auto w2_to_regs = [&]() {
        LAS char* rdA = lds + wm * 2048 + trl;
#pragma unroll
        for (int s = 0; s < NR; ++s) {
            LAS char* p = rdA + s * 8192;
            ph[2 * s] = __builtin_bit_cast(u32x4, cat44(tr_read(p), tr_read(p + 1024)));
            ph[2 * s + 1] = __builtin_bit_cast(u32x4, cat44(tr_read(p + 512), tr_read(p + 1536)));
        }
        wait_lgkm0();
    };
    if constexpr (!QKV) {
#pragma unroll
        for (int t = 0; t < NB2; ++t) store_h(bc[t], 4 + WN + t);
        if constexpr (WN == 0) {
            wait_vm<2>();
            w2_to_regs();
            static_for<2 * NB, 2 * NB + 8>([&](auto P) { dma_w2(P); });
            wait_vm<8>();
            barrier();
        } else {
            if (wm >= 2) {
                const int sl = (mt * 8 + nt) * 2 + (wm & 1);
                const unsigned vo = (unsigned)(((sl >> 3) * 136 + (sl & 7) * 17 + (lane < 34 ? (lane >> 1) : 0)) * 32 + (lane & 1) * 16);
                static_for<0, 16>([&](auto M) { ld16c<WS_KVP + M.value * 32 * 136 * 32>(ph[M.value], vo, rs); });
                wait_vm<16>();
                barrier();
                wait16(ph);
                ph[0][0] = kv_reduce_publish(a.ws, ph, sl, lane, tag) ? 1u : 0u;
            } else {
                if (wave == 4 && lane < 16) *(LAS unsigned*)(lds + L2_CNT + lane * 4) = 0u;
                wait_vm<0>();
                wait_lgkm0();
                barrier();
                if (wave == 5 && lane == 0)
                    __hip_atomic_store((GAS unsigned*)(a.ws + WS_FLAG + (mt * 16 + 8 + nt) * 256), tag, RLX_AGENT);
            }
        }
    } else {
        const bf16x8 qb0 = pack8<0>(bc[0]), qb1 = pack8<1>(bc[0]);
        if constexpr (WN == 0) {
            wait_vm<0>(); w2_to_regs();
            static_for<2 * NB, 2 * NB + 8>([&](auto P) { dma_w2(P); });
        }
        {
            f32x16 p[2];
            u32x4 F[2][2];
            p[0] = MFMA32(wfr[0], qb0, zero16);
            p[1] = MFMA32(wfr[1], qb1, zero16);
            feature_map2(p, F);
#pragma unroll
            for (int X = 0; X < 2; ++X) {
                LAS char* dst = lds + L_PHI + ((2 * WN + X) * 4 + wm) * 2048 + lane * 16;
                *(LAS u32x4*)(dst) = F[X][0];
                *(LAS u32x4*)(dst + 1024) = F[X][1];
            }
        }
        wait_lgkm0();
        barrier();
        if constexpr (WN == 1) {
            if (wave == 4 && lane < 16) *(LAS unsigned*)(lds + L2_CNT + lane * 4) = 0u;
#pragma unroll
            for (int i = 0; i < 8; ++i) ph[i] = *(LAS u32x4*)(lds + L_PHI + wm * 8192 + i * 1024 + lane * 16);
            wait_lgkm0();
        }
        barrier();
    }
    return tag;
}

constexpr int HP = 6;
constexpr int w_at(int i) { return i >= NR && i + NB <= 27 ? i + NB : -1; }
constexpr int p2_pos(bool isw, int idx) {
    int pos = 0;
    for (int s = 0; s < HP; ++s) { pos += 2; if (!isw && idx == s) return pos; }
    for (int i = 1; i < 28; ++i) {
        if (i - 1 + HP <= 27) { pos += 2; if (!isw && idx == i - 1 + HP) return pos; }
        if (w_at(i - 1) >= 0) { pos += 2; if (isw && idx == w_at(i - 1)) return pos; }
    }
    return 0;
}
constexpr int p2_issued(int step) {
    int pos = 2 * HP;
    for (int i = 1; i < step; ++i) { if (i - 1 + HP <= 27) pos += 2; if (w_at(i - 1) >= 0) pos += 2; }
    return pos;
}
constexpr int p2_wait(int i) {
    int need = p2_pos(false, i);
    if (i + 1 >= NR + NB && i + 1 <= 27) { int w = p2_pos(true, i + 1); if (w > need) need = w; }
    return p2_issued(i) - need;
}
template <int SLEEP> DI void poll_words(srd_t rs, unsigned voff, unsigned tag) {
    for (unsigned spin = 0; spin <= SPIN_CAP; ++spin) {
        unsigned v;
        ld4c<0>(v, voff, rs);
        asm volatile("s_waitcnt vmcnt(0) ; PINW %0" : "+v"(v) :: "memory");
        if (__all(v == tag)) break;
        __builtin_amdgcn_s_sleep(SLEEP);
    }
}
DI void lds_wait_word(LAS char* p) {
    for (unsigned spin = 0; spin <= 16 * SPIN_CAP; ++spin) {
        if (__hip_atomic_load((LAS unsigned*)p, __ATOMIC_RELAXED, __HIP_MEMORY_SCOPE_WORKGROUP) != 0u) break;
        __builtin_amdgcn_s_sleep(1);
    }
    CFENCE();
}
DI void lds_set_word(LAS char* p) { __hip_atomic_store((LAS unsigned*)p, 1u, __ATOMIC_RELAXED, __HIP_MEMORY_SCOPE_WORKGROUP); }

struct Fin { f32x16 acc[2][2]; u32x4 xr, br; };
template <int PART>
DI void phase2(const Args& a, LAS char* lds, const Geo& G, unsigned tag, u32x4 (&ph)[16], float vp2, Fin& F) {
    const int lane = G.lane, wave = G.wave, mt = G.mt2, nt = G.nt2;
    const unsigned ldsb = (unsigned)(size_t)lds;
    const int hh = lane >> 5, r32 = lane & 31, blk = (lane >> 4) & 1, q4 = (lane & 15) >> 2, p4 = lane & 3;
    const int kq = wave & 3, hf = wave >> 2;
    const srd_t rs = mk_srd(a.ws, WS_END);
    const srd_t rw2 = mk_srd(a.w2, FF * DM * 2);
    const unsigned wvo = (unsigned)((((lane >> 2) & 7) * DM + nt * 64 + hh * 32 + (lane & 3) * 8) * 2);
    const unsigned trl = (4 * hh + q4) * 64 + blk * 32 + p4 * 8;
    const unsigned l16 = lane * 16;
    const unsigned l16s = l16 + (lane >= 24 ? 128 : 0);
    unsigned w2q[4];
#pragma unroll
    for (int q = 0; q < 4; ++q) w2q[q] = wvo + (unsigned)((kq * 448 + ((q + G.rq) & 3) * 112) * DM * 2);
    const int tl = wave * 8 + (lane >> 3), cg = lane & 7;
    const unsigned ofo = (unsigned)((mt * 64 + tl) * DM + nt * 64 + cg * 8) * 2;
    u32x4 &xr = F.xr, &br = F.br;
    auto final_loads = [&]() {
        ld16(xr, ofo, mk_srd(a.x, SEQ * DM * 2));
        ld16(br, (unsigned)(nt * 64 + cg * 8) * 2, mk_srd(a.b2, DM * 2));
    };
    f32x16 (&acc)[2][2] = F.acc;
    if constexpr (PART < 2) {
#pragma unroll
        for (int i = 0; i < 2; ++i)
#pragma unroll
            for (int j = 0; j < 2; ++j)
#pragma unroll
                for (int e = 0; e < 16; ++e) acc[i][j][e] = 0.f;
    }
    LAS char* upper = lds + L2_A + kq * 16384;
    if constexpr (PART == 0) {
        const unsigned lb = ldsb + kq * 2048;
        unsigned hq[4];
#pragma unroll
        for (int q = 0; q < 4; ++q) hq[q] = (unsigned)(((2 * mt) * 112 + 28 * kq + ((q + G.rq) & 3) * 7) * 2048) + l16s;
        auto issue_w = [&](auto SI) {
            constexpr int s = SI.value;
            dma16<(s % NB) * 8192, (s % 7) * 16 * DM * 2>(w2q[s / 7], rw2, lb);
            dma16<(s % NB) * 8192 + 1024, ((s % 7) * 16 + 8) * DM * 2>(w2q[s / 7], rw2, lb);
        };
        u32x4 hb[HP][2];
        if (!G.qkv) { lds_wait_word(lds + L2_CNT + 24); lds_wait_word(lds + L2_CNT + 28); issue_w(IC(8)); issue_w(IC(9)); }
        issue_w(IC(10));
        static_for<NB + 4, NR + NB>([&](auto SI) { issue_w(SI); });
        final_loads();
        LAS char* rdA = lds + kq * 2048 + trl;
        LAS char* rdB = rdA + 6 * 8192;
        PIN(rdB);
        bf16x8 wf[2][2];
        auto rd = [&](int s, bf16x8 (&d)[2]) {
            LAS char* p = (s % NB) < 6 ? rdA + (s % NB) * 8192 : rdB + (s % NB - 6) * 8192;
            d[0] = cat44(tr_read(p), tr_read(p + 1024));
            d[1] = cat44(tr_read(p + 512), tr_read(p + 1536));
        };
        lds_wait_word(lds + L2_CNT + 16);
        static_for<0, HP>([&](auto SI) {
            constexpr int s = SI.value;
            ld16c<WS_H + (s % 7) * 2048>(hb[s % HP][0], hq[s / 7], rs);
            ld16c<WS_H + (112 + s % 7) * 2048>(hb[s % HP][1], hq[s / 7], rs);
        });
        static_for<0, 28>([&](auto SI) {
            constexpr int s = SI.value;
            SCHED0();
            asm volatile("s_waitcnt vmcnt(%2) ; PINW %0 %1" : "+v"(hb[s % HP][0]), "+v"(hb[s % HP][1]) : "i"(p2_wait(s)) : "memory");
            static_for<0, 4>([&](auto MI) {
                constexpr int m = MI.value, i = m >> 1, j = m & 1;
                acc[i][j] = MFMA32(s < NR ? asbf(ph[s < NR ? 2 * s + i : 0]) : wf[s & 1][i], asbf(hb[s % HP][j]), acc[i][j]);
                SCHED0();
                if constexpr (m == 0 && s + 1 >= NR && s + 1 < 28) rd(s + 1, wf[(s + 1) & 1]);
                constexpr int hs = s - 1 + HP;
                constexpr int ws = s >= 1 ? w_at(s - 1) : -1, wz = ws >= 0 ? ws : 0;
                if constexpr (s >= 1 && hs <= 27 && m < 2) ld16c<WS_H + (m * 112 + hs % 7) * 2048>(hb[hs % HP][m], hq[hs / 7], rs);
                if constexpr (ws >= 0 && m >= 2) dma16<(wz % NB) * 8192 + (m - 2) * 1024, ((wz % 7) * 16 + (m - 2) * 8) * DM * 2>(w2q[wz / 7], rw2, lb);
                SCHED0();
            });
        });
        SCHED0();
        wait_lgkm0();
#pragma unroll
        for (int p = 0; p < 16; ++p) {
            const f32x16& c = acc[p >> 3][(p >> 2) & 1];
            f32x4 v = {c[4 * (p & 3)], c[4 * (p & 3) + 1], c[4 * (p & 3) + 2], c[4 * (p & 3) + 3]};
            *(LAS f32x4*)(lds + (p >> 1) * 8192 + kq * 2048 + (p & 1) * 1024 + (l16 ^ ((((p >> 3) * 4 + (p & 3)) * 2) * 16))) = v;
        }
        asm volatile("s_waitcnt vmcnt(0) ; PINW %0 %1" : "+v"(xr), "+v"(br) :: "memory");
    } else if constexpr (PART == 1) {
        const srd_t rwo = mk_srd(a.wo, DM * DM * 2);
        __builtin_amdgcn_s_setprio(3);
        bf16x8 wv[8][2];
        auto wo_on = [&]() {
#pragma unroll
            for (int h = 0; h < 8; ++h) {
                LAS char* p = upper + h * 2048 + trl;
                wv[h][0] = cat44(tr_read(p), tr_read(p + 1024));
                wv[h][1] = cat44(tr_read(p + 512), tr_read(p + 1536));
            }
        };
        auto poll_h = [&]() {
            poll_words<1>(rs, WS_FLAG + (unsigned)((mt >> 1) * 16 + 8 + (lane & 7)) * 256, tag);
            if (lane == 0) lds_set_word(lds + L2_CNT + 16);
        };
        if (!G.qkv) {
            if (kq == 0) poll_h();
            if (kq >= 2 && __builtin_amdgcn_readfirstlane((int)ph[0][0]) == 0) {
                const int sl = (G.mt * 8 + G.nt) * 2 + (kq & 1);
                const unsigned vo = (unsigned)(((sl >> 3) * 136 + (sl & 7) * 17 + (lane < 34 ? (lane >> 1) : 0)) * 32 + (lane & 1) * 16);
                for (unsigned spin = 0; spin <= SPIN_CAP; ++spin) {
                    static_for<0, 16>([&](auto M) { ld16c<WS_KVP + M.value * 32 * 136 * 32>(ph[M.value], vo, rs); });
                    wait16(ph);
                    if (kv_reduce_publish(a.ws, ph, sl, lane, tag)) break;
                    __builtin_amdgcn_s_sleep(2);
                }
            }
            if (kq >= 2 && lane == 0) lds_set_word(lds + L2_CNT + 16 + kq * 4);
        } else {
            const unsigned kvo = (unsigned)((4 * G.nt + kq) * 2176 + hh * 272 + (r32 < 16 ? r32 : 16) * 16);
            const unsigned hfo = WS_FLAG + (unsigned)((mt >> 1) * 16 + 8 + (lane & 7)) * 256;
            const unsigned wovo = wvo + kq * (128 * DM * 2);
            u32x4 raw[4]; unsigned fl;
            bool hdone = kq != 2;
            auto request = [&]() { static_for<0, 4>([&](auto I) { ld16c<WS_KVR + I.value * 544>(raw[I.value], kvo, rs); }); ld4c<0>(fl, hfo, rs); };
            if (kq < 2) static_for<0, 16>([&](auto P) { dma16<L2_A + P.value * 1024, P.value * 8 * DM * 2>(wovo, rwo, ldsb + kq * 16384); });
            request();
            asm volatile("s_waitcnt vmcnt(0) ; PINW %0 %1 %2 %3 %4" : "+v"(raw[0]), "+v"(raw[1]), "+v"(raw[2]), "+v"(raw[3]), "+v"(fl) :: "memory");
            for (unsigned spin = 0; spin <= SPIN_CAP; ++spin) {
                if (!hdone && __all(fl == tag)) { hdone = true; if (lane == 0) lds_set_word(lds + L2_CNT + 16); }
                unsigned bad = 0;
#pragma unroll
                for (int i = 0; i < 4; ++i) bad |= (raw[i][1] ^ tag) | (raw[i][3] ^ tag);
                if (__all(bad == 0u)) break;
                __builtin_amdgcn_s_sleep(1);
                request();
                asm volatile("s_waitcnt vmcnt(0) ; PINW %0 %1 %2 %3 %4" : "+v"(raw[0]), "+v"(raw[1]), "+v"(raw[2]), "+v"(raw[3]), "+v"(fl) :: "memory");
            }
            f32x16 zero16;
#pragma unroll
            for (int i = 0; i < 16; ++i) zero16[i] = 0.f;
            const u32x4 k0 = {raw[0][0], raw[0][2], raw[1][0], raw[1][2]}, k1 = {raw[2][0], raw[2][2], raw[3][0], raw[3][2]};
#pragma unroll
            for (int w = 0; w < 4; ++w) {
                f32x16 nd = MFMA32(asbf(k0), asbf(ph[2 * w]), zero16);
                nd = MFMA32(asbf(k1), asbf(ph[2 * w + 1]), nd);
                const float inv = __builtin_amdgcn_rcpf(nd[8] + 1e-6f);
#pragma unroll
                for (int e = 0; e < 8; ++e) nd[e] *= inv;
                __builtin_amdgcn_raw_buffer_store_b128(pack8u<0>(nd), rs, WS_O + (unsigned)(((G.mt * 4 + w) * 32 + G.nt * 4 + kq) * 2048) + l16s, 0, SC1);
            }
            wait_vm<0>();
            unsigned old = 0;
            if (lane == 0) old = __hip_atomic_fetch_add((LAS unsigned*)(lds + L2_CNT), 1u, __ATOMIC_RELAXED, __HIP_MEMORY_SCOPE_WORKGROUP);
            if (lane == 0 && old == 3u) __hip_atomic_store((GAS unsigned*)(a.ws + WS_FLAG + (G.mt * 16 + G.nt) * 256), tag, RLX_AGENT);
            if (!hdone) poll_h();
            __builtin_amdgcn_s_setprio(0);
        }
        __builtin_amdgcn_s_setprio(0);
        final_loads();
        wait_vm<2>();
        wo_on();
#pragma unroll
        for (int h = 0; h < 8; ++h) { PIN(wv[h][0]); PIN(wv[h][1]); }
        poll_words<4>(rs, WS_FLAG + (unsigned)((mt >> 1) * 16 + 2 * kq + (lane & 1)) * 256, tag);
        __builtin_amdgcn_s_setprio(3);
        const unsigned ovo = (unsigned)((2 * mt) * 32 + 8 * kq) * 2048 + l16s;
        static_for<0, 16>([&](auto I) { constexpr int i = I.value; ld16c<WS_O + (i & 1) * 65536 + (i >> 1) * 2048>(ph[i], ovo, rs); });
        static_for<0, 8>([&](auto HI) {
            constexpr int h = HI.value;
            SCHED0();
            asm volatile("s_waitcnt vmcnt(%2) ; PINW %0 %1" : "+v"(ph[2 * h]), "+v"(ph[2 * h + 1]) : "i"(14 - 2 * h) : "memory");
#pragma unroll
            for (int i = 0; i < 2; ++i)
#pragma unroll
                for (int j = 0; j < 2; ++j) acc[i][j] = MFMA32(wv[h][i], asbf(ph[2 * h + j]), acc[i][j]);
        });
        SCHED0();
        wait_lgkm0();
#pragma unroll
        for (int p = 0; p < 16; ++p) {
            const f32x16& c = acc[p >> 3][(p >> 2) & 1];
            f32x4 v = {c[4 * (p & 3)], c[4 * (p & 3) + 1], c[4 * (p & 3) + 2], c[4 * (p & 3) + 3]};
            *(LAS f32x4*)(upper + p * 1024 + (l16 ^ ((((p >> 3) * 4 + (p & 3)) * 2) * 16))) = v;
        }
        asm volatile("s_waitcnt vmcnt(0) ; PINW %0 %1" : "+v"(xr), "+v"(br) :: "memory");
    } else {
    wait_lgkm0();
    barrier();
    const float r2 = __builtin_amdgcn_rcpf(vp2);
    const int p = (cg >> 2) * 8 + (tl >> 5) * 4 + (cg & 3);
    const int sl = ((tl & 31) ^ (cg * 2)) * 16;
    LAS char* pa = lds + L2_A + p * 1024 + sl;
    LAS char* pf = lds + (p >> 1) * 8192 + (p & 1) * 1024 + sl;
    u32x4 ov;
#pragma unroll
    for (int h = 0; h < 2; ++h) {
        f32x4 ss = (*(LAS f32x4*)(pa + h * 512) + *(LAS f32x4*)(pa + h * 512 + 16384)) + (*(LAS f32x4*)(pa + h * 512 + 2 * 16384) + *(LAS f32x4*)(pa + h * 512 + 3 * 16384));
        f32x4 so = (*(LAS f32x4*)(pf + h * 512) + *(LAS f32x4*)(pf + h * 512 + 2048)) + (*(LAS f32x4*)(pf + h * 512 + 4096) + *(LAS f32x4*)(pf + h * 512 + 6144));
        float res[4];
#pragma unroll
        for (int i = 0; i < 4; ++i) {
            const unsigned xu = xr[2 * h + (i >> 1)], bu = br[2 * h + (i >> 1)];
            const float xv = u2f((i & 1) ? (xu & 0xFFFF0000u) : (xu << 16));
            const float bv = u2f((i & 1) ? (bu & 0xFFFF0000u) : (bu << 16));
            const float o = so[i] * r2 + bv;
            const float gt = o * __builtin_amdgcn_rcpf(1.f + __builtin_amdgcn_exp2f(-o * LOG2E));
            res[i] = xv + ss[i] * gt;
        }
        ov[2 * h] = pk2(res[0], res[1]); ov[2 * h + 1] = pk2(res[2], res[3]);
    }
    *(GAS u32x4*)((char*)a.out + ofo) = ov;
    if (G.bid == 0 && wave == 7) {
        for (unsigned spin = 0; spin <= SPIN_CAP; ++spin) {
            unsigned f0, f1, f2, f3;
            ld4c<WS_FLAG>(f0, (unsigned)lane * 256, rs); ld4c<WS_FLAG + 16384>(f1, (unsigned)lane * 256, rs);
            ld4c<WS_FLAG + 32768>(f2, (unsigned)lane * 256, rs); ld4c<WS_FLAG + 49152>(f3, (unsigned)lane * 256, rs);
            asm volatile("s_waitcnt vmcnt(0) ; PINW %0 %1 %2 %3" : "+v"(f0), "+v"(f1), "+v"(f2), "+v"(f3) :: "memory");
            if (__all(f0 == tag && f1 == tag && f2 == tag && f3 == tag)) break;
            __builtin_amdgcn_s_sleep(1);
        }
        if (lane == 0) __hip_atomic_store((GAS unsigned*)(a.ws + WS_CTRL), tag, RLX_AGENT);
    }
    }
}

__global__ void __launch_bounds__(512, 2) fused_kernel(Args a) {
    extern __shared__ __attribute__((aligned(16))) char smem[];
    LAS char* lds = (LAS char*)smem;
    Geo G;
    G.bid = blockIdx.x;
    G.wave = __builtin_amdgcn_readfirstlane(threadIdx.x >> 6);
    G.lane = threadIdx.x & 63;
    const int xcd = G.bid & 7, loc = G.bid >> 3;
    const int sel = loc >> 2;
    G.mt = (xcd >> 1) * 4 + (loc & 3);
    G.nt = (xcd & 1) * 4 + (sel & 3);
    G.i16 = (xcd & 1) * 8 + sel;
    G.mt2 = G.mt * 2 + (xcd & 1);
    G.nt2 = sel ^ 4;
    G.qkv = sel < 4;
    G.rq = (sel & 3);
    if (G.wave == 3) {
        unsigned long long pc;
        asm volatile("s_getpc_b64 %0" : "=s"(pc));
        const unsigned line = (unsigned)(loc + G.lane * 32);
        touch_ptr<L_DUMMY>((const char*)(pc & ~127ull) + (size_t)(line < CODE_LINES ? line : 0) * 128, (unsigned)(size_t)lds);
    }
    if (G.wave == 2) {
        const unsigned cx = (3u - (unsigned)((size_t)a.x >> 7)) & 7u, cq = (3u - (unsigned)((size_t)a.wqkv >> 7)) & 7u;
        const char* px = (const char*)a.x + (size_t)(G.mt * 128 + sel * 16 + (G.lane & 15)) * 1024 + cx * 128;
        const char* pq = (const char*)a.wqkv + (size_t)(loc * 16 + (G.lane & 15)) * 3072 + ((G.lane >> 4) % 3) * 1024 + cq * 128;
        touch_ptr<L_DUMMY>((G.lane < 16 || (int)(cq >> 2) != (xcd & 1)) ? px : pq, (unsigned)(size_t)lds);
    }
    unsigned wvo0[4], xvo0[4], epoch;
    u32x4 rowv[2] = {u32x4{0u, 0u, 0u, 0u}, u32x4{0u, 0u, 0u, 0u}};
    {
        const int lane = G.lane, wm = G.wave & 3, hh = lane >> 5;
        const unsigned ldsb = (unsigned)(size_t)lds;
        const int LDW = G.qkv ? 1536 : 1792;
        const srd_t rx = mk_srd(a.x, SEQ * DM * 2);
        const srd_t rw = mk_srd(G.qkv ? a.wqkv : a.w1, DM * LDW * 2);
        ld4c<WS_CTRL>(epoch, 0u, mk_srd(a.ws, WS_END));
        if (G.wave >= 4) {
            const unsigned ro = (unsigned)((G.mt * 128 + G.i16 * 8 + 2 * wm) * DM * 2 + lane * 16);
            ld16(rowv[0], ro, rx); ld16(rowv[1], ro + 1024, rx);
        } else {
            const int c = 2 * (G.mt >> 2) + hh, pc = c + (G.nt & 1);
            const int colb = G.qkv ? ((((c >> 1) + 1) % 3) * 512 + 64 * G.nt + 32 * hh) : (224 * G.nt + 32 * (pc >= 7 ? pc - 7 : pc));
            const unsigned uo = c < (G.qkv ? 6 : 7) ? (unsigned)((((G.mt & 3) * 128 + 32 * wm + ((lane >> 2) & 7)) * LDW + colb + (lane & 3) * 8) * 2) : 0x7F000000u;
#pragma unroll
            for (int i = 0; i < 4; ++i) dma16r(uo + (unsigned)(i * 8 * LDW * 2), rw, ldsb + L_UNIT + wm * 4096 + i * 1024);
        }
#pragma unroll
        for (int i = 0; i < 4; ++i) {
            const int row = 8 * (4 * wm + i) + (lane >> 3);
            xvo0[i] = (unsigned)(((G.mt * 128 + row) * DM + ((lane & 7) ^ ((row >> 1) & 7)) * 8) * 2);
            const int k8 = 2 * wm + (i >> 1), c = 2 * (i & 1) + hh;
            const int colb = G.qkv ? (((c >> 1) + 1) * 512 + 64 * G.nt + 32 * (c & 1)) : (224 * G.nt + 32 * (c + (G.nt & 1)));
            wvo0[i] = (unsigned)(((k8 * 8 + ((lane >> 2) & 7)) * LDW + colb + (lane & 3) * 8) * 2);
        }
        if (G.wave >= 4) {
            static_for<0, 4>([&](auto I) { dma16<SLOT + XOFF + I.value * 1024, 0>(xvo0[I.value], rx, ldsb + wm * 4096); });
        } else {
            static_for<0, 4>([&](auto I) { dma16<SLOT + I.value * 1024, 0>(wvo0[I.value], rw, ldsb + wm * 4096); });
        }
        asm volatile("s_waitcnt vmcnt(4) ; PINW %0 %1 %2" : "+v"(epoch), "+v"(rowv[0]), "+v"(rowv[1]) :: "memory");
    }
    unsigned tag;
    float vp2 = 1.f;
    Fin F;
    if (G.wave < 4) {
        u32x4 ph[16];
        if (sel < 4) tag = phase1<true, 0>(a, lds, G, wvo0, xvo0, epoch, rowv, ph, vp2);
        else tag = phase1<false, 0>(a, lds, G, wvo0, xvo0, epoch, rowv, ph, vp2);
        phase2<0>(a, lds, G, tag, ph, vp2, F);
    } else {
        u32x4 ph[16];
#pragma unroll
        for (int i = 0; i < 16; ++i) ph[i] = u32x4{0u, 0u, 0u, 0u};
        if (sel < 4) tag = phase1<true, 1>(a, lds, G, wvo0, xvo0, epoch, rowv, ph, vp2);
        else tag = phase1<false, 1>(a, lds, G, wvo0, xvo0, epoch, rowv, ph, vp2);
        phase2<1>(a, lds, G, tag, ph, vp2, F);
    }
    {
        u32x4 ph[16];
        phase2<2>(a, lds, G, tag, ph, vp2, F);
    }
}

extern "C" void kernel_launch(void* const* d_in, const int* in_sizes, int n_in, void* d_out, int out_size, void* d_ws,
                              size_t ws_size, hipStream_t stream) {
    Args a;
    a.x = (const __bf16*)d_in[0]; a.wqkv = (const __bf16*)d_in[1]; a.wphi = (const __bf16*)d_in[2]; a.wo = (const __bf16*)d_in[3];
    a.w1 = (const __bf16*)d_in[5]; a.b1 = (const __bf16*)d_in[6]; a.w2 = (const __bf16*)d_in[7]; a.b2 = (const __bf16*)d_in[8];
    a.ws = (char*)d_ws; a.out = (__bf16*)d_out;
    (void)hipFuncSetAttribute((const void*)fused_kernel, hipFuncAttributeMaxDynamicSharedMemorySize, L_TOTAL);
    fused_kernel<<<256, 512, L_TOTAL, stream>>>(a);
}
```

```cpp
#include <hip/hip_runtime.h>
#include <hip/hip_bf16.h>
#include <stdint.h>
#include <type_traits>

typedef __attribute__((ext_vector_type(8))) __bf16 bf16x8;
typedef __attribute__((ext_vector_type(4))) __bf16 bf16x4;
typedef __attribute__((ext_vector_type(2))) __bf16 bf16x2;
typedef __attribute__((ext_vector_type(16))) float f32x16;
typedef __attribute__((ext_vector_type(4))) float f32x4;
typedef __attribute__((ext_vector_type(2))) float f32x2;
typedef __attribute__((ext_vector_type(4))) unsigned u32x4;
typedef __attribute__((ext_vector_type(2))) unsigned u32x2;
typedef __amdgpu_buffer_rsrc_t srd_t;
#define LAS __attribute__((address_space(3)))
#define GAS __attribute__((address_space(1)))
#define DI __device__ __forceinline__
#define MFMA32(a, b, c) __builtin_amdgcn_mfma_f32_32x32x16_bf16((a), (b), (c), 0, 0, 0)
#define SCHED0() __builtin_amdgcn_sched_barrier(0)
#define CFENCE() asm volatile("" ::: "memory")
#define RLX_AGENT __ATOMIC_RELAXED, __HIP_MEMORY_SCOPE_AGENT

constexpr int SEQ = 2048, DM = 512, NH = 32, FF = 1792;
constexpr float LOG2E = 1.4426950408889634f;
constexpr int SC1 = 16;
constexpr unsigned SPIN_CAP = 1u << 16;
constexpr unsigned CODE_LINES = 392;

constexpr unsigned WS_CTRL = 0;
constexpr unsigned WS_FLAG = 1024;
constexpr unsigned WS_MARK = WS_FLAG + 256 * 256;
constexpr unsigned WS_RST  = WS_MARK + 1024;
constexpr unsigned WS_CSG  = WS_RST + 2048 * 16;
constexpr unsigned WS_KVR  = WS_CSG + 3328 * 32;
constexpr unsigned WS_KVP  = WS_KVR + 32 * 2176;
constexpr unsigned WS_H    = (WS_KVP + 16u * 32 * 136 * 32 + 4095) & ~4095u;
constexpr unsigned WS_O    = WS_H + 64u * 112 * 2048;
constexpr unsigned WS_END  = WS_O + 64u * 32 * 2048;

constexpr int SLOT = 32768, XOFF = 16384;
constexpr int chunk_base(bool qkv, int c) { return qkv ? (c == 0 ? 131072 : (c == 3 ? 81920 : 98304 + (c - 1) * 16384)) : (c == 0 ? 98304 : (c == 1 ? 122880 : (c == 2 ? 40960 : 65536))); }
constexpr int L_UNIT = 131072;
constexpr int L_CSP = 147456;
constexpr int L_RR = L_CSP + 1024;
constexpr int L_AUX = L_RR + 1024;
constexpr int L_DUMMY = L_AUX + 2048;
constexpr int L_FIX = L_DUMMY + 256;
constexpr int L_RED = 40960;
constexpr int L_PHI = 98304;
constexpr int NR = 8;
constexpr int NB = 11;
constexpr int L2_CNT = NB * 8192;
constexpr int L2_A = 98304;
constexpr int L_TOTAL = 163840;
static_assert(L_FIX + 8192 <= L_TOTAL, "lds");

template <int LOFF, int SOFF> DI void dma16(unsigned voff, srd_t srd, unsigned lbase) {
    unsigned t;
    lbase = (unsigned)__builtin_amdgcn_readfirstlane((int)lbase);
    asm volatile("s_add_i32 m0, %3, %4\n\ts_mov_b32 %0, %5\n\tbuffer_load_dwordx4 %1, %2, %0 offen lds"
                 : "=&s"(t) : "v"(voff), "s"(srd), "s"(lbase), "i"(LOFF), "i"(SOFF) : "memory", "scc");
}
template <int LOFF> DI void touch_ptr(const void* p, unsigned lbase) {
    asm volatile("s_add_i32 m0, %1, %2\n\ts_nop 0\n\tglobal_load_lds_dword %0, off" :: "v"(p), "s"(lbase), "i"(LOFF) : "memory", "scc");
}
template <int OFF> DI void ld16c(u32x4& d, unsigned voff, srd_t srd) {
    asm volatile("buffer_load_dwordx4 %0, %1, %2, %3 offen offset:%4 sc1" : "=v"(d) : "v"(voff), "s"(srd), "s"(OFF & ~4095), "i"(OFF & 4095) : "memory");
}
template <int OFF> DI void ld4c(unsigned& d, unsigned voff, srd_t srd) {
    asm volatile("buffer_load_dword %0, %1, %2, %3 offen offset:%4 sc1" : "=v"(d) : "v"(voff), "s"(srd), "s"(OFF & ~4095), "i"(OFF & 4095) : "memory");
}
template <int OFF> DI void ld8c(u32x2& d, unsigned voff, srd_t srd) {
    asm volatile("buffer_load_dwordx2 %0, %1, %2, %3 offen offset:%4 sc1" : "=v"(d) : "v"(voff), "s"(srd), "s"(OFF & ~4095), "i"(OFF & 4095) : "memory");
}
DI void dma16r(unsigned voff, srd_t srd, unsigned lds_dst) {
    asm volatile("s_mov_b32 m0, %2\n\ts_nop 0\n\tbuffer_load_dwordx4 %0, %1, 0 offen lds" :: "v"(voff), "s"(srd), "s"(lds_dst) : "memory");
}
DI void ld16(u32x4& d, unsigned voff, srd_t srd) { asm volatile("buffer_load_dwordx4 %0, %1, %2, 0 offen" : "=v"(d) : "v"(voff), "s"(srd) : "memory"); }
DI float fmx(float a, float b) { return __builtin_elementwise_maximum(a, b); }
DI void wait_lgkm0() { __builtin_amdgcn_s_waitcnt(0xC07F); CFENCE(); }
template <int N> DI void wait_vm() { asm volatile("s_waitcnt vmcnt(%0)" ::"i"(N) : "memory"); }
DI void barrier() { __builtin_amdgcn_s_barrier(); CFENCE(); }
#define PIN(x) asm volatile("" : "+v"(x))
DI srd_t mk_srd(const void* p, unsigned bytes) { return __builtin_amdgcn_make_buffer_rsrc((void*)p, 0, (int)bytes, 0x00020000); }

DI bf16x4 tr_read(LAS char* p) { return __builtin_amdgcn_ds_read_tr16_b64_v4bf16((LAS bf16x4*)p); }
DI bf16x8 cat44(bf16x4 a, bf16x4 b) { return __builtin_shufflevector(a, b, 0, 1, 2, 3, 4, 5, 6, 7); }
DI float dot2(unsigned a, unsigned b, float c) {
    return __builtin_amdgcn_fdot2_f32_bf16(__builtin_bit_cast(bf16x2, a), __builtin_bit_cast(bf16x2, b), c, false);
}
DI unsigned pk2(float a, float b) { f32x2 v = {a, b}; return __builtin_bit_cast(unsigned, __builtin_convertvector(v, bf16x2)); }
template <int S> DI u32x4 pack8u(const f32x16& x) {
    u32x4 p = {pk2(x[8 * S], x[8 * S + 1]), pk2(x[8 * S + 2], x[8 * S + 3]), pk2(x[8 * S + 4], x[8 * S + 5]), pk2(x[8 * S + 6], x[8 * S + 7])};
    return p;
}
template <int S> DI bf16x8 pack8(const f32x16& x) { return __builtin_bit_cast(bf16x8, pack8u<S>(x)); }
DI bf16x8 asbf(u32x4 u) { return __builtin_bit_cast(bf16x8, u); }
DI float u2f(unsigned u) { return __builtin_bit_cast(float, u); }
DI unsigned f2u(float f) { return __builtin_bit_cast(unsigned, f); }
DI unsigned hi2(unsigned lo, unsigned hi) { return __builtin_amdgcn_perm(hi, lo, 0x07060302u); }
DI void split3(float v, unsigned& s0, unsigned& s1, unsigned& s2) {
    s0 = f2u(v) & 0xFFFF0000u;
    float r = v - u2f(s0);
    s1 = f2u(r) & 0xFFFF0000u;
    s2 = f2u(r - u2f(s1));
}
DI float half_sum(float v) { auto s = __builtin_amdgcn_permlane32_swap(f2u(v), f2u(v), false, false); return u2f(s[0]) + u2f(s[1]); }
DI float wave_sum(float v) {
#define DPPADD(ctrl, rm) v += u2f((unsigned)__builtin_amdgcn_update_dpp(0, (int)f2u(v), ctrl, rm, 0xf, true))
    DPPADD(0x111, 0xf); DPPADD(0x112, 0xf); DPPADD(0x114, 0xf); DPPADD(0x118, 0xf); DPPADD(0x142, 0xa); DPPADD(0x143, 0xc);
#undef DPPADD
    return u2f((unsigned)__builtin_amdgcn_readlane((int)f2u(v), 63));
}
template <int C, int N, class F> DI void static_for(F&& f) { if constexpr (C < N) { f(std::integral_constant<int, C>{}); static_for<C + 1, N>(f); } }

struct Args {
    const __bf16* x; const __bf16* wqkv; const __bf16* wphi; const __bf16* wo;
    const __bf16* w1; const __bf16* b1; const __bf16* w2; const __bf16* b2;
    char* ws; __bf16* out;
};
struct Geo { int bid, wave, lane, mt, nt, i16, mt2, nt2, qkv, rq; };
#define IC(v) std::integral_constant<int, (v)>{}

template <int N> DI void gather_wait(u32x4& gst, u32x2& gv2, u32x4& gcs) {
    asm volatile("s_waitcnt vmcnt(%3) ; PINW %0 %1 %2" : "+v"(gst), "+v"(gv2), "+v"(gcs) : "i"(N) : "memory");
}
DI void wait16(u32x4 (&v)[16]) {
    asm volatile("s_waitcnt vmcnt(0) ; PINW %0 %1 %2 %3 %4 %5 %6 %7 %8 %9 %10 %11 %12 %13 %14 %15"
                 : "+v"(v[0]), "+v"(v[1]), "+v"(v[2]), "+v"(v[3]), "+v"(v[4]), "+v"(v[5]), "+v"(v[6]), "+v"(v[7]),
                   "+v"(v[8]), "+v"(v[9]), "+v"(v[10]), "+v"(v[11]), "+v"(v[12]), "+v"(v[13]), "+v"(v[14]), "+v"(v[15]) :: "memory");
}

DI bool kv_reduce_publish(char* ws, u32x4 (&v)[16], int sl, int lane, unsigned tag) {
    unsigned bad = 0;
#pragma unroll
    for (int m = 0; m < 16; ++m) bad |= (v[m][1] ^ tag) | (v[m][3] ^ tag);
    if (!__all(bad == 0u)) return false;
    float s0[16], s1[16];
#pragma unroll
    for (int m = 0; m < 16; ++m) { s0[m] = u2f(v[m][0]); s1[m] = u2f(v[m][2]); }
#pragma unroll
    for (int st = 8; st >= 1; st >>= 1)
#pragma unroll
        for (int m = 0; m < st; ++m) { s0[m] += s0[m + st]; s1[m] += s1[m + st]; }
    const int g = (sl & 7) * 17 + (lane < 34 ? (lane >> 1) : 0), row = g >> 3, fq = g & 7;
    const unsigned off = WS_KVR + (sl >> 3) * 2176 + fq * 272 + row * 16 + (lane & 1) * 8;
    if (lane < 34)
        __hip_atomic_store((GAS unsigned long long*)(ws + off), (unsigned long long)pk2(s0[0], s1[0]) | ((unsigned long long)tag << 32), RLX_AGENT);
    return true;
}
template <bool QKV, int WN>
DI unsigned phase1(const Args& a, LAS char* lds, const Geo& G, const unsigned (&wvo0)[4], const unsigned (&xvo0)[4],
                   unsigned epoch, const u32x4 (&rowv)[2], u32x4 (&ph)[16], float& vp2) {
    constexpr int NTB = QKV ? 2 : 3;
    constexpr int NB2 = QKV ? 1 : (WN == 0 ? 1 : 2);
    constexpr int NG = 2 + NB2;
    constexpr int NU = QKV ? 24 : 28;
    constexpr int LDW = QKV ? 1536 : 1792;
    constexpr int CP = NTB * 2;
    const int lane = G.lane, wave = G.wave, wm = wave & 3, mt = G.mt, nt = G.nt;
    const unsigned ldsb = (unsigned)(size_t)lds;
    const int hh = lane >> 5, r32 = lane & 31, blk = (lane >> 4) & 1, q4 = (lane & 15) >> 2, p4 = lane & 3;
    const srd_t rs = mk_srd(a.ws, WS_END);
    const srd_t rx = mk_srd(a.x, SEQ * DM * 2);
    const srd_t rw = mk_srd(QKV ? a.wqkv : a.w1, DM * LDW * 2);
    const srd_t rw2 = mk_srd(a.w2, FF * DM * 2);
    const srd_t rwo = mk_srd(a.wo, DM * DM * 2);
    auto ptile = [&](int c) { const int p = c + (nt & 1); return p >= 7 ? p - 7 : p; };
    auto colb_of = [&](int c) { return QKV ? ((((c >> 1) + 1) % 3) * 512 + 64 * nt + 32 * (c & 1)) : (224 * nt + 32 * ptile(c)); };
    auto tile_of = [&](int t) { return t < 2 ? 2 * t + WN : (QKV ? 4 + WN : 4 + WN + (t - 2)); };
    auto dma_pair = [&](auto JI, auto II) {
        constexpr int j = JI.value, i = II.value, sb = ((j + 1) % 3) * SLOT;
        if constexpr (i < 4) dma16<sb + i * 1024, j * 64 * LDW * 2>(wvo0[i], rw, ldsb + wm * 4096);
        else dma16<sb + XOFF + (i - 4) * 1024, j * 128>(xvo0[i - 4], rx, ldsb + wm * 4096);
    };
    const unsigned w2vo = (unsigned)((((lane >> 2) & 7) * DM + G.nt2 * 64 + hh * 32 + (lane & 3) * 8) * 2);
    unsigned w2q[3];
#pragma unroll
    for (int q = 0; q < 3; ++q) w2q[q] = w2vo + (unsigned)((wm * 448 + ((q + G.rq) & 3) * 112) * DM * 2);
    auto dma_w2 = [&](auto PI) {
        constexpr int r = PI.value >> 1, t = PI.value & 1;
        dma16<(r % NB) * 8192 + t * 1024, ((r % 7) * 16 + t * 8) * DM * 2>(w2q[r / 7], rw2, ldsb + wm * 2048);
    };
    const unsigned wovo = w2vo + wm * (128 * DM * 2);
    auto dma_wo = [&](auto PI) { dma16<L2_A + PI.value * 1024, PI.value * 8 * DM * 2>(wovo, rwo, ldsb + wm * 16384); };
    unsigned cvo[WN == 1 ? NTB : 1];
    if constexpr (WN == 1) {
#pragma unroll
        for (int i = 0; i < NTB; ++i) {
            const int st = 2 * i + hh, k8 = 4 * wm + (st >= NTB ? 1 : 0), c = st >= NTB ? st - NTB : st;
            cvo[i] = (unsigned)(((k8 * 8 + ((lane >> 2) & 7)) * LDW + colb_of(4 + c) + (lane & 3) * 8) * 2);
        }
    }
    auto dma_chunk = [&](auto CI, auto II) {
        constexpr int c = CI.value, i = II.value;
        dma16<chunk_base(QKV, c) + i * 1024, (c * 128 + (i / NTB) * 16) * LDW * 2>(cvo[i % NTB], rw, ldsb + wm * (CP * 1024));
    };
    const int srow = mt * 128 + G.i16 * 8 + 2 * wm;
    const unsigned tag = (unsigned)__builtin_amdgcn_readfirstlane((int)epoch) + 1u;
    const unsigned trl = (4 * hh + q4) * 64 + blk * 32 + p4 * 8;
    if constexpr (WN == 0) {
        LAS char* p = lds + L_UNIT + wm * 4096 + trl;
        const unsigned one = 0x3F803F80u;
        float su[2];
#pragma unroll
        for (int u = 0; u < 2; ++u) {
            float s = 0.f;
#pragma unroll
            for (int i = 0; i < 4; ++i) { u32x2 w = __builtin_bit_cast(u32x2, tr_read(p + u * 512 + i * 1024)); s = dot2(w[1], one, dot2(w[0], one, s)); }
            su[u] = s;
        }
        auto sw = __builtin_amdgcn_permlane32_swap(f2u(su[0]), f2u(su[1]), false, false);
        *(LAS float*)(lds + L_CSP + ((hh * 4 + wm) * 32 + r32) * 4) = u2f(sw[0]) + u2f(sw[1]);
        static_for<0, 8>([&](auto I) { dma_pair(IC(1), I); });
    } else {
        float mean[2], vv[2];
#pragma unroll
        for (int r = 0; r < 2; ++r) {
            float e[8];
#pragma unroll
            for (int i = 0; i < 4; ++i) { e[2 * i] = u2f(rowv[r][i] << 16); e[2 * i + 1] = u2f(rowv[r][i] & 0xFFFF0000u); }
            mean[r] = wave_sum(((e[0] + e[1]) + (e[2] + e[3])) + ((e[4] + e[5]) + (e[6] + e[7]))) * (1.f / DM);
            float q = 0.f;
#pragma unroll
            for (int i = 0; i < 8; ++i) { float d = e[i] - mean[r]; q = __builtin_fmaf(d, d, q); }
            vv[r] = wave_sum(q) * (1.f / DM) + 1e-5f;
        }
        u32x4 g = {f2u(lane ? mean[1] : mean[0]), tag, f2u(lane ? vv[1] : vv[0]), tag};
        if (lane < 2) __builtin_amdgcn_raw_buffer_store_b128(g, rs, WS_RST + (srow + lane) * 16, 0, SC1);
    }

    LAS char* wb[2]; LAS char* xb[2][4];
#pragma unroll
    for (int s = 0; s < 2; ++s) {
        wb[s] = lds + s * 65536 + hh * 2048 + WN * 512 + q4 * 64 + blk * 32 + p4 * 8;
        PIN(wb[s]);
        const int row = 32 * wm + r32;
#pragma unroll
        for (int k = 0; k < 4; ++k) { xb[s][k] = lds + s * 65536 + XOFF + row * 128 + (((2 * k + hh) ^ ((row >> 1) & 7)) * 16); PIN(xb[s][k]); }
    }
    const int csbase = QKV ? nt * 192 : 1536 + nt * 224;

    f32x16 acc[2];
#pragma unroll
    for (int t = 0; t < 2; ++t)
#pragma unroll
        for (int i = 0; i < 16; ++i) acc[t][i] = 0.f;
    bf16x8 xf[32], wf[3][2];
    auto read_x = [&](int g) { const int sl = ((g >> 2) + 1) % 3; xf[g] = *(LAS bf16x8*)(xb[sl >> 1][g & 3] + (sl & 1) * SLOT); };
    auto read_w = [&](int g, int t) {
        const int sl = ((g >> 2) + 1) % 3;
        LAS char* p = wb[sl >> 1] + (sl & 1) * SLOT + (g & 3) * 4096 + t * 1024;
        wf[g % 3][t] = cat44(tr_read(p), tr_read(p + 256));
    };
    u32x4 gst, gcs; u32x2 gv2;
    const int mytile = wm < NG ? (wm < 2 ? 2 * wm + WN : (QKV ? 4 + WN : 2 + WN + wm)) : WN;
    const unsigned gvo_st = (unsigned)(mt * 128 + 32 * wm + r32) * 16;
    const unsigned gvo_v2 = (unsigned)(G.mt2 * 64 + wave * 8 + (lane >> 3)) * 16;
    const unsigned gvo_cs = (unsigned)(csbase + mytile * 32 + r32) * 32 + hh * 16;
    auto gather = [&]() {
        ld16c<WS_RST>(gst, gvo_st, rs);
        ld8c<WS_RST + 8>(gv2, gvo_v2, rs);
        ld16c<WS_CSG>(gcs, gvo_cs, rs);
    };
    float vpe = 1.f, r_t = 1.f;
    bf16x8 fixT, fixF[NG];
    auto gather_check_and_fix = [&]() {
        for (unsigned spin = 0;; ++spin) {
            const unsigned bad = (gst[1] ^ tag) | (gst[3] ^ tag) | (gv2[1] ^ tag) | (gcs[1] ^ tag) | (gcs[3] ^ tag);
            if (__all(bad == 0u) || spin > SPIN_CAP) break;
            __builtin_amdgcn_s_sleep(1);
            gather();
            gather_wait<0>(gst, gv2, gcs);
        }
        vpe = u2f(gst[2]);
        vp2 = u2f(gv2[0]);
        r_t = __builtin_amdgcn_rsqf(vpe);
        unsigned s0, s1, s2;
        split3(hh ? vpe * r_t : -u2f(gst[0]), s0, s1, s2);
        u32x4 u;
        u[0] = hi2(s0, s1);
        u[1] = hh ? (s2 >> 16) : u[0];
        u[2] = hh ? 0u : hi2(s2, s0);
        u[3] = 0u;
        fixT = asbf(u);
        if (QKV) *(LAS float*)(lds + L_RR + (wave * 32 + r32) * 4) = r_t;
        unsigned c0, c1, c2;
        split3(half_sum(u2f(gcs[0]) + u2f(gcs[2])), c0, c1, c2);
        unsigned bb = 0;
        if (!QKV) bb = *(LAS unsigned short*)(lds + L_AUX + (ptile(mytile) * 32 + r32) * 2);
        u32x4 w;
        w[0] = hh ? (bb | (bb << 16)) : hi2(c0, c0);
        w[1] = hh ? bb : hi2(c1, c1);
        w[2] = hh ? 0u : hi2(c0, c2);
        w[3] = 0u;
        *(LAS u32x4*)(lds + L_FIX + wave * 1024 + lane * 16) = w;
    };

    wait_vm<WN == 0 ? 8 : 1>();
    wait_lgkm0();
    barrier();
#pragma unroll
    for (int g = 0; g < 2; ++g) { read_x(g); read_w(g, 0); read_w(g, 1); }
    if constexpr (WN == 1) {
        const int idx = 16 * wm + (lane & 15);
        LAS float* p = (LAS float*)(lds + L_CSP) + (idx >> 5) * 128 + (idx & 31);
        u32x2 gq = {f2u((p[0] + p[32]) + (p[64] + p[96])), tag};
        const int uc = 2 * (mt >> 2) + (wm >> 1);
        if (lane < 16 && uc < NU / 4) __builtin_amdgcn_raw_buffer_store_b64(gq, rs, WS_CSG + (csbase + uc * 32 + (idx & 31)) * 32 + (mt & 3) * 8, 0, SC1);
        if (wave == 7 && lane == 0) __builtin_amdgcn_raw_buffer_store_b32(tag, rs, WS_MARK + G.bid * 4, 0, SC1);
        if (wave == 4) {
            if (QKV) {
                const srd_t rp = mk_srd(a.wphi, NH * 256 * 2);
                dma16<L_AUX, 0>((unsigned)(nt * 2048 + lane * 16), rp, ldsb);
                dma16<L_AUX + 1024, 1024>((unsigned)(nt * 2048 + lane * 16), rp, ldsb);
            } else {
                const srd_t rb = mk_srd(a.b1, FF * 2);
                dma16<L_AUX, 0>((unsigned)(nt * 448 + lane * 16), rb, ldsb);
            }
        }
    }
    static_for<0, 32>([&](auto GI) {
        constexpr int g = GI.value, j = g >> 2;
        SCHED0();
        if constexpr ((g & 3) == 2) {
            constexpr int JG = WN == 0 ? 4 : 5;
            if constexpr (j == JG + 1) { PIN(acc[0]); PIN(acc[1]); }
            if constexpr (WN == 0) {
                constexpr int n = j <= 6 ? 8 : 63;
                if constexpr (j == JG + 1) gather_wait<n>(gst, gv2, gcs);
                else wait_vm<n>();
            } else if constexpr (j == 4) wait_vm<1>();
            else if constexpr (j == JG + 1) gather_wait<CP>(gst, gv2, gcs);
            else if constexpr (j == 7) wait_vm<CP>();
            wait_lgkm0();
            barrier();
            if constexpr (WN == 1 && j == 1) {
                const int gw = G.bid * 4 + wm;
                const char* p = lane < 14 ? (const char*)a.w2 + (size_t)(gw * 14 + lane) * 128 : (const char*)a.wo + (size_t)(gw * 4 + (lane & 3)) * 128;
                touch_ptr<L_DUMMY>(p, ldsb);
            }
            if constexpr (WN == 1 && j == 3) {
                PIN(acc[0]); PIN(acc[1]);
                const int loc = G.bid >> 3;
                const unsigned c2 = (3u - (unsigned)((size_t)a.w2 >> 7)) & 7u, co = (3u - (unsigned)((size_t)a.wo >> 7)) & 7u;
                const char* p = lane < 14 ? (const char*)a.w2 + (size_t)((loc * 4 + wm) * 14 + lane) * 1024 + c2 * 128
                                          : (const char*)a.wo + (size_t)((loc * 4 + wm) * 4 + (lane & 3)) * 1024 + co * 128;
                touch_ptr<L_DUMMY>(p, ldsb);
            }
            if constexpr (j == JG) gather();
            if constexpr (WN == 1 && j == 7) static_for<0, CP>([&](auto I) { dma_chunk(IC(3), I); });
            if constexpr (j == JG + 1) gather_check_and_fix();
            if constexpr (j == 7) {
#pragma unroll
                for (int t = 0; t < NG; ++t) fixF[t] = *(LAS bf16x8*)(lds + L_FIX + (WN * 4 + t) * 1024 + lane * 16);
            }
            SCHED0();
        }
        constexpr int jb = (g - 2) >> 2, q = (g - 2) & 3;
        static_for<0, 2>([&](auto TI) {
            constexpr int t = TI.value;
            if (QKV && t == 1) acc[t] = MFMA32(xf[g], wf[g % 3][t], acc[t]);
            else acc[t] = MFMA32(wf[g % 3][t], xf[g], acc[t]);
            if constexpr (g + 2 < 32) {
                if (t == 0) read_x(g + 2);
                read_w(g + 2, t);
            }
            constexpr int i = q * 2 + t;
            if constexpr (WN == 0) {
                if constexpr (g < 2) { dma_pair(IC(2), IC(4 * g + 2 * t)); dma_pair(IC(2), IC(4 * g + 2 * t + 1)); }
                else if constexpr (jb <= 4) dma_pair(IC(jb + 3), IC(i));
                else if constexpr (jb == 5) dma_w2(IC(i));
                else if constexpr (jb == 6 && i < 2) dma_w2(IC(8 + i));
            } else if constexpr (g >= 2 && jb >= 4 && jb <= 6 && i < CP) dma_chunk(IC(jb - 4), IC(i));
            SCHED0();
        });
    });
    const int tt = mt * 4 + wm;
    f32x16 zero16;
#pragma unroll
    for (int i = 0; i < 16; ++i) zero16[i] = 0.f;
    auto store_h = [&](f32x16& c, int tile) {
#pragma unroll
        for (int i = 0; i < 16; ++i) { float v = fmx(c[i], 0.f); c[i] = v * v; }
        const unsigned off = WS_H + (unsigned)((tt * 112 + nt * 14 + ptile(tile) * 2) * 2048 + lane * 16 + (lane >= 24 ? 128 : 0));
        __builtin_amdgcn_raw_buffer_store_b128(pack8u<0>(c), rs, off, 0, SC1);
        __builtin_amdgcn_raw_buffer_store_b128(pack8u<1>(c), rs, off + 2048, 0, SC1);
    };
    bf16x8 wfr[2];
    const float kscale = r_t * LOG2E;
    auto feature_map2 = [&](f32x16 (&p)[2], u32x4 (&F)[2][2]) {
        float m[2][2], sm[2][2];
#pragma unroll
        for (int X = 0; X < 2; ++X)
#pragma unroll
            for (int h = 0; h < 2; ++h) {
                const int o = 8 * h;
                m[X][h] = fmx(fmx(fmx(fmx(p[X][o], p[X][o + 1]), p[X][o + 2]), fmx(fmx(p[X][o + 3], p[X][o + 4]), p[X][o + 5])), fmx(p[X][o + 6], p[X][o + 7]));
            }
#pragma unroll
        for (int X = 0; X < 2; ++X) {
            auto s = __builtin_amdgcn_permlane32_swap(f2u(m[X][0]), f2u(m[X][1]), false, false);
            float c = -kscale * fmx(u2f(s[0]), u2f(s[1]));
            auto s2 = __builtin_amdgcn_permlane32_swap(f2u(c), f2u(c), false, false);
            m[X][0] = u2f(s2[0]); m[X][1] = u2f(s2[1]);
        }
#pragma unroll
        for (int i = 0; i < 16; ++i)
#pragma unroll
            for (int X = 0; X < 2; ++X) p[X][i] = __builtin_amdgcn_exp2f(__builtin_fmaf(p[X][i], kscale, m[X][i >> 3]));
#pragma unroll
        for (int X = 0; X < 2; ++X)
#pragma unroll
            for (int h = 0; h < 2; ++h) {
                const int o = 8 * h;
                sm[X][h] = ((p[X][o] + p[X][o + 1]) + (p[X][o + 2] + p[X][o + 3])) + ((p[X][o + 4] + p[X][o + 5]) + (p[X][o + 6] + p[X][o + 7]));
            }
#pragma unroll
        for (int X = 0; X < 2; ++X) {
            auto s = __builtin_amdgcn_permlane32_swap(f2u(sm[X][0]), f2u(sm[X][1]), false, false);
            float c = __builtin_amdgcn_rcpf(u2f(s[0]) + u2f(s[1]));
            auto s2 = __builtin_amdgcn_permlane32_swap(f2u(c), f2u(c), false, false);
            sm[X][0] = u2f(s2[0]); sm[X][1] = u2f(s2[1]);
        }
#pragma unroll
        for (int X = 0; X < 2; ++X) {
#pragma unroll
            for (int i = 0; i < 16; ++i) p[X][i] *= sm[X][i >> 3];
            F[X][0] = pack8u<0>(p[X]); F[X][1] = pack8u<1>(p[X]);
        }
    };
    if constexpr (!QKV) {
        acc[0] = MFMA32(fixF[0], fixT, acc[0]);
        acc[1] = MFMA32(fixF[1], fixT, acc[1]);
        store_h(acc[0], WN);
        store_h(acc[1], 2 + WN);
    } else {
        acc[0] = MFMA32(fixF[0], fixT, acc[0]);
        acc[1] = MFMA32(fixT, fixF[1], acc[1]);
        bf16x8 idf[2];
#pragma unroll
        for (int s = 0; s < 2; ++s) {
            u32x4 u;
#pragma unroll
            for (int i = 0; i < 4; ++i) {
                int f0 = 16 * s + 8 * (i >> 1) + 4 * hh + 2 * (i & 1);
                u[i] = (r32 == f0 ? 0x3F80u : 0u) | (r32 == f0 + 1 ? 0x3F800000u : 0u);
            }
            idf[s] = asbf(u);
        }
#pragma unroll
        for (int X = 0; X < 2; ++X) {
            LAS char* p = lds + L_AUX + (2 * WN + X) * 512 + (4 * hh + q4) * 32 + p4 * 8;
            u32x4 u = __builtin_bit_cast(u32x4, cat44(tr_read(p), tr_read(p + 256)));
            const unsigned sg = blk ? 0x80008000u : 0u;
#pragma unroll
            for (int i = 0; i < 4; ++i) u[i] ^= sg;
            wfr[X] = asbf(u);
        }
#pragma unroll
        for (int g = 0; g < 4; ++g) {
            f32x4 rr = *(LAS f32x4*)(lds + L_RR + (wave * 32 + 8 * g + 4 * hh) * 4);
#pragma unroll
            for (int i = 0; i < 4; ++i) acc[1][4 * g + i] *= rr[i];
        }
        const u32x4 v0 = pack8u<0>(acc[1]), v1 = pack8u<1>(acc[1]);
        const bf16x8 kb0 = pack8<0>(acc[0]), kb1 = pack8<1>(acc[0]);
        {
            f32x16 p[2], d[2], kv[2];
            u32x4 F[2][2];
            p[0] = MFMA32(wfr[0], kb0, zero16);
            p[1] = MFMA32(wfr[1], kb1, zero16);
            feature_map2(p, F);
#pragma unroll
            for (int X = 0; X < 2; ++X) d[X] = MFMA32(asbf(F[X][0]), idf[0], zero16);
#pragma unroll
            for (int X = 0; X < 2; ++X) d[X] = MFMA32(asbf(F[X][1]), idf[1], d[X]);
            u32x4 b[2][2];
#pragma unroll
            for (int X = 0; X < 2; ++X)
#pragma unroll
                for (int i = 0; i < 4; ++i) { b[X][0][i] = (blk == X) ? v0[i] : 0x3F803F80u; b[X][1][i] = (blk == X) ? v1[i] : 0x3F803F80u; }
#pragma unroll
            for (int X = 0; X < 2; ++X) kv[X] = MFMA32(pack8<0>(d[X]), asbf(b[X][0]), zero16);
#pragma unroll
            for (int X = 0; X < 2; ++X) kv[X] = MFMA32(pack8<1>(d[X]), asbf(b[X][1]), kv[X]);
#pragma unroll
            for (int X = 0; X < 2; ++X) {
                const int row = X == 0 ? r32 : (r32 == 15 ? 16 : r32 - 16);
                const bool use = X == 0 ? (r32 <= 16) : (r32 >= 15);
                LAS char* dst = lds + (use ? L_RED + (((WN * 2 + X) * 4 + wm) * 17 + row) * 144 : L_RED + 39168 + (r32 & 7) * 144) + hh * 16;
#pragma unroll
                for (int g = 0; g < 4; ++g) {
                    f32x4 v = {kv[X][4 * g], kv[X][4 * g + 1], kv[X][4 * g + 2], kv[X][4 * g + 3]};
                    *(LAS f32x4*)(dst + g * 32) = v;
                }
            }
        }
        wait_lgkm0();
        barrier();
        if constexpr (WN == 1) {
#pragma unroll
            for (int it = 0; it < 5; ++it) {
                const int o = it * 64 + lane;
                if (it < 4 || lane < 16) {
                    LAS char* src = lds + L_RED + (wm * 4 * 17 + (o >> 4)) * 144 + (o & 15) * 8;
                    f32x2 v = (*(LAS f32x2*)(src) + *(LAS f32x2*)(src + 2448)) + (*(LAS f32x2*)(src + 2 * 2448) + *(LAS f32x2*)(src + 3 * 2448));
                    u32x4 g0 = {f2u(v[0]), tag, f2u(v[1]), tag};
                    __builtin_amdgcn_raw_buffer_store_b128(g0, rs, WS_KVP + (unsigned)((mt * 32 + nt * 4 + wm) * 272 + o) * 16, 0, SC1);
                }
            }
        }
    }
    f32x16 bc[NB2];
#pragma unroll
    for (int t = 0; t < NB2; ++t) bc[t] = MFMA32(fixF[2 + t], fixT, zero16);
    {
        constexpr int CB0 = QKV ? 81920 : 40960;
        LAS char* q0 = lds + hh * (NTB * 512) + WN * 512 + q4 * 64 + blk * 32 + p4 * 8 + CB0;
        LAS char* q1 = q0 + (98304 - CB0);
        PIN(q0); PIN(q1);
        bf16x8 qf[4][NB2];
        auto read_q = [&](int g, int t) {
            const int cb = chunk_base(QKV, g >> 3);
            LAS char* p = (cb >= 98304 ? q1 + (cb - 98304) : q0 + (cb - CB0)) + (g & 7) * (NTB * 1024) + t * 512;
            qf[g & 3][t] = cat44(tr_read(p), tr_read(p + 256));
        };
#pragma unroll
        for (int g = 0; g < 3; ++g)
#pragma unroll
            for (int t = 0; t < NB2; ++t) read_q(g, t);
        static_for<0, 32>([&](auto GI) {
            constexpr int g = GI.value;
            SCHED0();
            if constexpr (g == 8 || g == 16 || g == 24) {
                if constexpr (g == 8 && WN == 1) wait_vm<QKV ? 5 : 4>();
                wait_lgkm0(); barrier();
            }
            static_for<0, NB2>([&](auto TI) {
                constexpr int t = TI.value;
                bc[t] = MFMA32(qf[g & 3][t], xf[g], bc[t]);
                if constexpr (g + 3 < 32) read_q(g + 3, t);
                if constexpr (WN == 0) {
                    if constexpr (QKV && g >= 8 && g < 18) dma_w2(IC(10 + g - 8));
                    if constexpr (!QKV && g >= 24 && g < 30) dma_w2(IC(10 + g - 24));
                } else if constexpr (!QKV && g >= 16 && g < 24) dma_wo(IC(2 * (g - 16) + t));
                else if constexpr (QKV && g >= 9 && g < 25) { if (wm >= 2) dma_wo(IC(g - 9)); }
                SCHED0();
            });
        });
    }
    auto w2_to_regs = [&]() {
        LAS char* rdA = lds + wm * 2048 + trl;
#pragma unroll
        for (int s = 0; s < NR; ++s) {
            LAS char* p = rdA + s * 8192;
            ph[2 * s] = __builtin_bit_cast(u32x4, cat44(tr_read(p), tr_read(p + 1024)));
            ph[2 * s + 1] = __builtin_bit_cast(u32x4, cat44(tr_read(p + 512), tr_read(p + 1536)));
        }
        wait_lgkm0();
    };
    if constexpr (!QKV) {
#pragma unroll
        for (int t = 0; t < NB2; ++t) store_h(bc[t], 4 + WN + t);
        if constexpr (WN == 0) {
            wait_vm<2>();
            w2_to_regs();
            static_for<2 * NB, 2 * NB + 8>([&](auto P) { dma_w2(P); });
            wait_vm<8>();
            barrier();
        } else {
            if (wm >= 2) {
                const int sl = (mt * 8 + nt) * 2 + (wm & 1);
                const unsigned vo = (unsigned)(((sl >> 3) * 136 + (sl & 7) * 17 + (lane < 34 ? (lane >> 1) : 0)) * 32 + (lane & 1) * 16);
                static_for<0, 16>([&](auto M) { ld16c<WS_KVP + M.value * 32 * 136 * 32>(ph[M.value], vo, rs); });
                wait_vm<16>();
                barrier();
                wait16(ph);
                ph[0][0] = kv_reduce_publish(a.ws, ph, sl, lane, tag) ? 1u : 0u;
            } else {
                if (wave == 4 && lane < 16) *(LAS unsigned*)(lds + L2_CNT + lane * 4) = 0u;
                wait_vm<0>();
                wait_lgkm0();
                barrier();
                if (wave == 5 && lane == 0)
                    __hip_atomic_store((GAS unsigned*)(a.ws + WS_FLAG + (mt * 16 + 8 + nt) * 256), tag, RLX_AGENT);
            }
        }
    } else {
        const bf16x8 qb0 = pack8<0>(bc[0]), qb1 = pack8<1>(bc[0]);
        if constexpr (WN == 0) {
            wait_vm<0>(); w2_to_regs();
            static_for<2 * NB, 2 * NB + 8>([&](auto P) { dma_w2(P); });
        }
        {
            f32x16 p[2];
            u32x4 F[2][2];
            p[0] = MFMA32(wfr[0], qb0, zero16);
            p[1] = MFMA32(wfr[1], qb1, zero16);
            feature_map2(p, F);
#pragma unroll
            for (int X = 0; X < 2; ++X) {
                LAS char* dst = lds + L_PHI + ((2 * WN + X) * 4 + wm) * 2048 + lane * 16;
                *(LAS u32x4*)(dst) = F[X][0];
                *(LAS u32x4*)(dst + 1024) = F[X][1];
            }
        }
        wait_lgkm0();
        barrier();
        if constexpr (WN == 1) {
            if (wave == 4 && lane < 16) *(LAS unsigned*)(lds + L2_CNT + lane * 4) = 0u;
#pragma unroll
            for (int i = 0; i < 8; ++i) ph[i] = *(LAS u32x4*)(lds + L_PHI + wm * 8192 + i * 1024 + lane * 16);
            wait_lgkm0();
        }
        barrier();
    }
    return tag;
}

constexpr int HP = 6;
constexpr int w_at(int i) { return i >= NR && i + NB <= 27 ? i + NB : -1; }
constexpr int p2_pos(bool isw, int idx) {
    int pos = 0;
    for (int s = 0; s < HP; ++s) { pos += 2; if (!isw && idx == s) return pos; }
    for (int i = 1; i < 28; ++i) {
        if (i - 1 + HP <= 27) { pos += 2; if (!isw && idx == i - 1 + HP) return pos; }
        if (w_at(i - 1) >= 0) { pos += 2; if (isw && idx == w_at(i - 1)) return pos; }
    }
    return 0;
}
constexpr int p2_issued(int step) {
    int pos = 2 * HP;
    for (int i = 1; i < step; ++i) { if (i - 1 + HP <= 27) pos += 2; if (w_at(i - 1) >= 0) pos += 2; }
    return pos;
}
constexpr int p2_wait(int i) {
    int need = p2_pos(false, i);
    if (i + 1 >= NR + NB && i + 1 <= 27) { int w = p2_pos(true, i + 1); if (w > need) need = w; }
    return p2_issued(i) - need;
}
template <int SLEEP> DI void poll_words(srd_t rs, unsigned voff, unsigned tag) {
    for (unsigned spin = 0; spin <= SPIN_CAP; ++spin) {
        unsigned v;
        ld4c<0>(v, voff, rs);
        asm volatile("s_waitcnt vmcnt(0) ; PINW %0" : "+v"(v) :: "memory");
        if (__all(v == tag)) break;
        __builtin_amdgcn_s_sleep(SLEEP);
    }
}
DI void lds_wait_word(LAS char* p) {
    for (unsigned spin = 0; spin <= 16 * SPIN_CAP; ++spin) {
        if (__hip_atomic_load((LAS unsigned*)p, __ATOMIC_RELAXED, __HIP_MEMORY_SCOPE_WORKGROUP) != 0u) break;
        __builtin_amdgcn_s_sleep(1);
    }
    CFENCE();
}
DI void lds_set_word(LAS char* p) { __hip_atomic_store((LAS unsigned*)p, 1u, __ATOMIC_RELAXED, __HIP_MEMORY_SCOPE_WORKGROUP); }

struct Fin { f32x16 acc[2][2]; u32x4 xr, br; };
template <int PART>
DI void phase2(const Args& a, LAS char* lds, const Geo& G, unsigned tag, u32x4 (&ph)[16], float vp2, Fin& F) {
    const int lane = G.lane, wave = G.wave, mt = G.mt2, nt = G.nt2;
    const unsigned ldsb = (unsigned)(size_t)lds;
    const int hh = lane >> 5, r32 = lane & 31, blk = (lane >> 4) & 1, q4 = (lane & 15) >> 2, p4 = lane & 3;
    const int kq = wave & 3, hf = wave >> 2;
    const srd_t rs = mk_srd(a.ws, WS_END);
    const srd_t rw2 = mk_srd(a.w2, FF * DM * 2);
    const unsigned wvo = (unsigned)((((lane >> 2) & 7) * DM + nt * 64 + hh * 32 + (lane & 3) * 8) * 2);
    const unsigned trl = (4 * hh + q4) * 64 + blk * 32 + p4 * 8;
    const unsigned l16 = lane * 16;
    const unsigned l16s = l16 + (lane >= 24 ? 128 : 0);
    unsigned w2q[4];
#pragma unroll
    for (int q = 0; q < 4; ++q) w2q[q] = wvo + (unsigned)((kq * 448 + ((q + G.rq) & 3) * 112) * DM * 2);
    const int tl = wave * 8 + (lane >> 3), cg = lane & 7;
    const unsigned ofo = (unsigned)((mt * 64 + tl) * DM + nt * 64 + cg * 8) * 2;
    u32x4 &xr = F.xr, &br = F.br;
    auto final_loads = [&]() {
        ld16(xr, ofo, mk_srd(a.x, SEQ * DM * 2));
        ld16(br, (unsigned)(nt * 64 + cg * 8) * 2, mk_srd(a.b2, DM * 2));
    };
    f32x16 (&acc)[2][2] = F.acc;
    if constexpr (PART < 2) {
#pragma unroll
        for (int i = 0; i < 2; ++i)
#pragma unroll
            for (int j = 0; j < 2; ++j)
#pragma unroll
                for (int e = 0; e < 16; ++e) acc[i][j][e] = 0.f;
    }
    LAS char* upper = lds + L2_A + kq * 16384;
    if constexpr (PART == 0) {
        const unsigned lb = ldsb + kq * 2048;
        unsigned hq[4];
#pragma unroll
        for (int q = 0; q < 4; ++q) hq[q] = (unsigned)(((2 * mt) * 112 + 28 * kq + ((q + G.rq) & 3) * 7) * 2048) + l16s;
        auto issue_w = [&](auto SI) {
            constexpr int s = SI.value;
            dma16<(s % NB) * 8192, (s % 7) * 16 * DM * 2>(w2q[s / 7], rw2, lb);
            dma16<(s % NB) * 8192 + 1024, ((s % 7) * 16 + 8) * DM * 2>(w2q[s / 7], rw2, lb);
        };
        u32x4 hb[HP][2];
        if (!G.qkv) { lds_wait_word(lds + L2_CNT + 24); lds_wait_word(lds + L2_CNT + 28); issue_w(IC(8)); issue_w(IC(9)); }
        issue_w(IC(10));
        static_for<NB + 4, NR + NB>([&](auto SI) { issue_w(SI); });
        final_loads();
        LAS char* rdA = lds + kq * 2048 + trl;
        LAS char* rdB = rdA + 6 * 8192;
        PIN(rdB);
        bf16x8 wf[2][2];
        auto rd = [&](int s, bf16x8 (&d)[2]) {
            LAS char* p = (s % NB) < 6 ? rdA + (s % NB) * 8192 : rdB + (s % NB - 6) * 8192;
            d[0] = cat44(tr_read(p), tr_read(p + 1024));
            d[1] = cat44(tr_read(p + 512), tr_read(p + 1536));
        };
        lds_wait_word(lds + L2_CNT + 16);
        static_for<0, HP>([&](auto SI) {
            constexpr int s = SI.value;
            ld16c<WS_H + (s % 7) * 2048>(hb[s % HP][0], hq[s / 7], rs);
            ld16c<WS_H + (112 + s % 7) * 2048>(hb[s % HP][1], hq[s / 7], rs);
        });
        static_for<0, 28>([&](auto SI) {
            constexpr int s = SI.value;
            SCHED0();
            asm volatile("s_waitcnt vmcnt(%2) ; PINW %0 %1" : "+v"(hb[s % HP][0]), "+v"(hb[s % HP][1]) : "i"(p2_wait(s)) : "memory");
            static_for<0, 4>([&](auto MI) {
                constexpr int m = MI.value, i = m >> 1, j = m & 1;
                acc[i][j] = MFMA32(s < NR ? asbf(ph[s < NR ? 2 * s + i : 0]) : wf[s & 1][i], asbf(hb[s % HP][j]), acc[i][j]);
                SCHED0();
                if constexpr (m == 0 && s + 1 >= NR && s + 1 < 28) rd(s + 1, wf[(s + 1) & 1]);
                constexpr int hs = s - 1 + HP;
                constexpr int ws = s >= 1 ? w_at(s - 1) : -1, wz = ws >= 0 ? ws : 0;
                if constexpr (s >= 1 && hs <= 27 && m < 2) ld16c<WS_H + (m * 112 + hs % 7) * 2048>(hb[hs % HP][m], hq[hs / 7], rs);
                if constexpr (ws >= 0 && m >= 2) dma16<(wz % NB) * 8192 + (m - 2) * 1024, ((wz % 7) * 16 + (m - 2) * 8) * DM * 2>(w2q[wz / 7], rw2, lb);
                SCHED0();
            });
        });
        SCHED0();
        wait_lgkm0();
#pragma unroll
        for (int p = 0; p < 16; ++p) {
            const f32x16& c = acc[p >> 3][(p >> 2) & 1];
            f32x4 v = {c[4 * (p & 3)], c[4 * (p & 3) + 1], c[4 * (p & 3) + 2], c[4 * (p & 3) + 3]};
            *(LAS f32x4*)(lds + (p >> 1) * 8192 + kq * 2048 + (p & 1) * 1024 + (l16 ^ ((((p >> 3) * 4 + (p & 3)) * 2) * 16))) = v;
        }
        asm volatile("s_waitcnt vmcnt(0) ; PINW %0 %1" : "+v"(xr), "+v"(br) :: "memory");
    } else if constexpr (PART == 1) {
        const srd_t rwo = mk_srd(a.wo, DM * DM * 2);
        __builtin_amdgcn_s_setprio(3);
        bf16x8 wv[8][2];
        auto wo_on = [&]() {
#pragma unroll
            for (int h = 0; h < 8; ++h) {
                LAS char* p = upper + h * 2048 + trl;
                wv[h][0] = cat44(tr_read(p), tr_read(p + 1024));
                wv[h][1] = cat44(tr_read(p + 512), tr_read(p + 1536));
            }
        };
        auto poll_h = [&]() {
            poll_words<1>(rs, WS_FLAG + (unsigned)((mt >> 1) * 16 + 8 + (lane & 7)) * 256, tag);
            if (lane == 0) lds_set_word(lds + L2_CNT + 16);
        };
        if (!G.qkv) {
            if (kq == 0) poll_h();
            if (kq == 1 && G.bid == 255) {
                for (unsigned spin = 0; spin <= SPIN_CAP; ++spin) {
                    u32x4 mk;
                    ld16c<WS_MARK>(mk, l16, rs);
                    asm volatile("s_waitcnt vmcnt(0) ; PINW %0" : "+v"(mk) :: "memory");
                    if (__all(mk[0] == tag && mk[1] == tag && mk[2] == tag && mk[3] == tag)) break;
                    __builtin_amdgcn_s_sleep(1);
                }
                if (lane == 0) __hip_atomic_store((GAS unsigned*)(a.ws + WS_CTRL), tag, RLX_AGENT);
            }
            if (kq >= 2 && __builtin_amdgcn_readfirstlane((int)ph[0][0]) == 0) {
                const int sl = (G.mt * 8 + G.nt) * 2 + (kq & 1);
                const unsigned vo = (unsigned)(((sl >> 3) * 136 + (sl & 7) * 17 + (lane < 34 ? (lane >> 1) : 0)) * 32 + (lane & 1) * 16);
                for (unsigned spin = 0; spin <= SPIN_CAP; ++spin) {
                    static_for<0, 16>([&](auto M) { ld16c<WS_KVP + M.value * 32 * 136 * 32>(ph[M.value], vo, rs); });
                    wait16(ph);
                    if (kv_reduce_publish(a.ws, ph, sl, lane, tag)) break;
                    __builtin_amdgcn_s_sleep(2);
                }
            }
            if (kq >= 2 && lane == 0) lds_set_word(lds + L2_CNT + 16 + kq * 4);
        } else {
            const unsigned kvo = (unsigned)((4 * G.nt + kq) * 2176 + hh * 272 + (r32 < 16 ? r32 : 16) * 16);
            const unsigned hfo = WS_FLAG + (unsigned)((mt >> 1) * 16 + 8 + (lane & 7)) * 256;
            const unsigned wovo = wvo + kq * (128 * DM * 2);
            u32x4 raw[4]; unsigned fl;
            bool hdone = kq != 2;
            auto request = [&]() { static_for<0, 4>([&](auto I) { ld16c<WS_KVR + I.value * 544>(raw[I.value], kvo, rs); }); ld4c<0>(fl, hfo, rs); };
            if (kq < 2) static_for<0, 16>([&](auto P) { dma16<L2_A + P.value * 1024, P.value * 8 * DM * 2>(wovo, rwo, ldsb + kq * 16384); });
            request();
            asm volatile("s_waitcnt vmcnt(0) ; PINW %0 %1 %2 %3 %4" : "+v"(raw[0]), "+v"(raw[1]), "+v"(raw[2]), "+v"(raw[3]), "+v"(fl) :: "memory");
            for (unsigned spin = 0; spin <= SPIN_CAP; ++spin) {
                if (!hdone && __all(fl == tag)) { hdone = true; if (lane == 0) lds_set_word(lds + L2_CNT + 16); }
                unsigned bad = 0;
#pragma unroll
                for (int i = 0; i < 4; ++i) bad |= (raw[i][1] ^ tag) | (raw[i][3] ^ tag);
                if (__all(bad == 0u)) break;
                __builtin_amdgcn_s_sleep(1);
                request();
                asm volatile("s_waitcnt vmcnt(0) ; PINW %0 %1 %2 %3 %4" : "+v"(raw[0]), "+v"(raw[1]), "+v"(raw[2]), "+v"(raw[3]), "+v"(fl) :: "memory");
            }
            f32x16 zero16;
#pragma unroll
            for (int i = 0; i < 16; ++i) zero16[i] = 0.f;
            const u32x4 k0 = {raw[0][0], raw[0][2], raw[1][0], raw[1][2]}, k1 = {raw[2][0], raw[2][2], raw[3][0], raw[3][2]};
#pragma unroll
            for (int w = 0; w < 4; ++w) {
                f32x16 nd = MFMA32(asbf(k0), asbf(ph[2 * w]), zero16);
                nd = MFMA32(asbf(k1), asbf(ph[2 * w + 1]), nd);
                const float inv = __builtin_amdgcn_rcpf(nd[8] + 1e-6f);
#pragma unroll
                for (int e = 0; e < 8; ++e) nd[e] *= inv;
                __builtin_amdgcn_raw_buffer_store_b128(pack8u<0>(nd), rs, WS_O + (unsigned)(((G.mt * 4 + w) * 32 + G.nt * 4 + kq) * 2048) + l16s, 0, SC1);
            }
            wait_vm<0>();
            unsigned old = 0;
            if (lane == 0) old = __hip_atomic_fetch_add((LAS unsigned*)(lds + L2_CNT), 1u, __ATOMIC_RELAXED, __HIP_MEMORY_SCOPE_WORKGROUP);
            if (lane == 0 && old == 3u) __hip_atomic_store((GAS unsigned*)(a.ws + WS_FLAG + (G.mt * 16 + G.nt) * 256), tag, RLX_AGENT);
            if (!hdone) poll_h();
            __builtin_amdgcn_s_setprio(0);
        }
        __builtin_amdgcn_s_setprio(0);
        final_loads();
        wait_vm<2>();
        wo_on();
#pragma unroll
        for (int h = 0; h < 8; ++h) { PIN(wv[h][0]); PIN(wv[h][1]); }
        poll_words<4>(rs, WS_FLAG + (unsigned)((mt >> 1) * 16 + 2 * kq + (lane & 1)) * 256, tag);
        __builtin_amdgcn_s_setprio(3);
        const unsigned ovo = (unsigned)((2 * mt) * 32 + 8 * kq) * 2048 + l16s;
        static_for<0, 16>([&](auto I) { constexpr int i = I.value; ld16c<WS_O + (i & 1) * 65536 + (i >> 1) * 2048>(ph[i], ovo, rs); });
        static_for<0, 8>([&](auto HI) {
            constexpr int h = HI.value;
            SCHED0();
            asm volatile("s_waitcnt vmcnt(%2) ; PINW %0 %1" : "+v"(ph[2 * h]), "+v"(ph[2 * h + 1]) : "i"(14 - 2 * h) : "memory");
#pragma unroll
            for (int i = 0; i < 2; ++i)
#pragma unroll
                for (int j = 0; j < 2; ++j) acc[i][j] = MFMA32(wv[h][i], asbf(ph[2 * h + j]), acc[i][j]);
        });
        SCHED0();
        wait_lgkm0();
#pragma unroll
        for (int p = 0; p < 16; ++p) {
            const f32x16& c = acc[p >> 3][(p >> 2) & 1];
            f32x4 v = {c[4 * (p & 3)], c[4 * (p & 3) + 1], c[4 * (p & 3) + 2], c[4 * (p & 3) + 3]};
            *(LAS f32x4*)(upper + p * 1024 + (l16 ^ ((((p >> 3) * 4 + (p & 3)) * 2) * 16))) = v;
        }
        asm volatile("s_waitcnt vmcnt(0) ; PINW %0 %1" : "+v"(xr), "+v"(br) :: "memory");
    } else {
    wait_lgkm0();
    barrier();
    const float r2 = __builtin_amdgcn_rcpf(vp2);
    const int p = (cg >> 2) * 8 + (tl >> 5) * 4 + (cg & 3);
    const int sl = ((tl & 31) ^ (cg * 2)) * 16;
    LAS char* pa = lds + L2_A + p * 1024 + sl;
    LAS char* pf = lds + (p >> 1) * 8192 + (p & 1) * 1024 + sl;
    u32x4 ov;
#pragma unroll
    for (int h = 0; h < 2; ++h) {
        f32x4 ss = (*(LAS f32x4*)(pa + h * 512) + *(LAS f32x4*)(pa + h * 512 + 16384)) + (*(LAS f32x4*)(pa + h * 512 + 2 * 16384) + *(LAS f32x4*)(pa + h * 512 + 3 * 16384));
        f32x4 so = (*(LAS f32x4*)(pf + h * 512) + *(LAS f32x4*)(pf + h * 512 + 2048)) + (*(LAS f32x4*)(pf + h * 512 + 4096) + *(LAS f32x4*)(pf + h * 512 + 6144));
        float res[4];
#pragma unroll
        for (int i = 0; i < 4; ++i) {
            const unsigned xu = xr[2 * h + (i >> 1)], bu = br[2 * h + (i >> 1)];
            const float xv = u2f((i & 1) ? (xu & 0xFFFF0000u) : (xu << 16));
            const float bv = u2f((i & 1) ? (bu & 0xFFFF0000u) : (bu << 16));
            const float o = so[i] * r2 + bv;
            const float gt = o * __builtin_amdgcn_rcpf(1.f + __builtin_amdgcn_exp2f(-o * LOG2E));
            res[i] = xv + ss[i] * gt;
        }
        ov[2 * h] = pk2(res[0], res[1]); ov[2 * h + 1] = pk2(res[2], res[3]);
    }
    *(GAS u32x4*)((char*)a.out + ofo) = ov;
    }
}

__global__ void __launch_bounds__(512, 2) fused_kernel(Args a) {
    extern __shared__ __attribute__((aligned(16))) char smem[];
    LAS char* lds = (LAS char*)smem;
    Geo G;
    G.bid = blockIdx.x;
    G.wave = __builtin_amdgcn_readfirstlane(threadIdx.x >> 6);
    G.lane = threadIdx.x & 63;
    const int xcd = G.bid & 7, loc = G.bid >> 3;
    const int sel = loc >> 2;
    G.mt = (xcd >> 1) * 4 + (loc & 3);
    G.nt = (xcd & 1) * 4 + (sel & 3);
    G.i16 = (xcd & 1) * 8 + sel;
    G.mt2 = G.mt * 2 + (xcd & 1);
    G.nt2 = sel ^ 4;
    G.qkv = sel < 4;
    G.rq = (sel & 3);
    if (G.wave == 3) {
        unsigned long long pc;
        asm volatile("s_getpc_b64 %0" : "=s"(pc));
        const unsigned line = (unsigned)(loc + G.lane * 32);
        touch_ptr<L_DUMMY>((const char*)(pc & ~127ull) + (size_t)(line < CODE_LINES ? line : 0) * 128, (unsigned)(size_t)lds);
    }
    if (G.wave == 2) {
        const unsigned cx = (3u - (unsigned)((size_t)a.x >> 7)) & 7u, cq = (3u - (unsigned)((size_t)a.wqkv >> 7)) & 7u;
        const char* px = (const char*)a.x + (size_t)(G.mt * 128 + sel * 16 + (G.lane & 15)) * 1024 + cx * 128;
        const char* pq = (const char*)a.wqkv + (size_t)(loc * 16 + (G.lane & 15)) * 3072 + ((G.lane >> 4) % 3) * 1024 + cq * 128;
        touch_ptr<L_DUMMY>((G.lane < 16 || (int)(cq >> 2) != (xcd & 1)) ? px : pq, (unsigned)(size_t)lds);
    }
    unsigned wvo0[4], xvo0[4], epoch;
    u32x4 rowv[2] = {u32x4{0u, 0u, 0u, 0u}, u32x4{0u, 0u, 0u, 0u}};
    {
        const int lane = G.lane, wm = G.wave & 3, hh = lane >> 5;
        const unsigned ldsb = (unsigned)(size_t)lds;
        const int LDW = G.qkv ? 1536 : 1792;
        const srd_t rx = mk_srd(a.x, SEQ * DM * 2);
        const srd_t rw = mk_srd(G.qkv ? a.wqkv : a.w1, DM * LDW * 2);
        ld4c<WS_CTRL>(epoch, 0u, mk_srd(a.ws, WS_END));
        if (G.wave >= 4) {
            const unsigned ro = (unsigned)((G.mt * 128 + G.i16 * 8 + 2 * wm) * DM * 2 + lane * 16);
            ld16(rowv[0], ro, rx); ld16(rowv[1], ro + 1024, rx);
        } else {
            const int c = 2 * (G.mt >> 2) + hh, pc = c + (G.nt & 1);
            const int colb = G.qkv ? ((((c >> 1) + 1) % 3) * 512 + 64 * G.nt + 32 * hh) : (224 * G.nt + 32 * (pc >= 7 ? pc - 7 : pc));
            const unsigned uo = c < (G.qkv ? 6 : 7) ? (unsigned)((((G.mt & 3) * 128 + 32 * wm + ((lane >> 2) & 7)) * LDW + colb + (lane & 3) * 8) * 2) : 0x7F000000u;
#pragma unroll
            for (int i = 0; i < 4; ++i) dma16r(uo + (unsigned)(i * 8 * LDW * 2), rw, ldsb + L_UNIT + wm * 4096 + i * 1024);
        }
#pragma unroll
        for (int i = 0; i < 4; ++i) {
            const int row = 8 * (4 * wm + i) + (lane >> 3);
            xvo0[i] = (unsigned)(((G.mt * 128 + row) * DM + ((lane & 7) ^ ((row >> 1) & 7)) * 8) * 2);
            const int k8 = 2 * wm + (i >> 1), c = 2 * (i & 1) + hh;
            const int colb = G.qkv ? (((c >> 1) + 1) * 512 + 64 * G.nt + 32 * (c & 1)) : (224 * G.nt + 32 * (c + (G.nt & 1)));
            wvo0[i] = (unsigned)(((k8 * 8 + ((lane >> 2) & 7)) * LDW + colb + (lane & 3) * 8) * 2);
        }
        if (G.wave >= 4) {
            static_for<0, 4>([&](auto I) { dma16<SLOT + XOFF + I.value * 1024, 0>(xvo0[I.value], rx, ldsb + wm * 4096); });
        } else {
            static_for<0, 4>([&](auto I) { dma16<SLOT + I.value * 1024, 0>(wvo0[I.value], rw, ldsb + wm * 4096); });
        }
        asm volatile("s_waitcnt vmcnt(4) ; PINW %0 %1 %2" : "+v"(epoch), "+v"(rowv[0]), "+v"(rowv[1]) :: "memory");
    }
    unsigned tag;
    float vp2 = 1.f;
    Fin F;
    if (G.wave < 4) {
        u32x4 ph[16];
        if (sel < 4) tag = phase1<true, 0>(a, lds, G, wvo0, xvo0, epoch, rowv, ph, vp2);
        else tag = phase1<false, 0>(a, lds, G, wvo0, xvo0, epoch, rowv, ph, vp2);
        phase2<0>(a, lds, G, tag, ph, vp2, F);
    } else {
        u32x4 ph[16];
#pragma unroll
        for (int i = 0; i < 16; ++i) ph[i] = u32x4{0u, 0u, 0u, 0u};
        if (sel < 4) tag = phase1<true, 1>(a, lds, G, wvo0, xvo0, epoch, rowv, ph, vp2);
        else tag = phase1<false, 1>(a, lds, G, wvo0, xvo0, epoch, rowv, ph, vp2);
        phase2<1>(a, lds, G, tag, ph, vp2, F);
    }
    {
        u32x4 ph[16];
        phase2<2>(a, lds, G, tag, ph, vp2, F);
    }
}

extern "C" void kernel_launch(void* const* d_in, const int* in_sizes, int n_in, void* d_out, int out_size, void* d_ws,
                              size_t ws_size, hipStream_t stream) {
    Args a;
    a.x = (const __bf16*)d_in[0]; a.wqkv = (const __bf16*)d_in[1]; a.wphi = (const __bf16*)d_in[2]; a.wo = (const __bf16*)d_in[3];
    a.w1 = (const __bf16*)d_in[5]; a.b1 = (const __bf16*)d_in[6]; a.w2 = (const __bf16*)d_in[7]; a.b2 = (const __bf16*)d_in[8];
    a.ws = (char*)d_ws; a.out = (__bf16*)d_out;
    (void)hipFuncSetAttribute((const void*)fused_kernel, hipFuncAttributeMaxDynamicSharedMemorySize, L_TOTAL);
    fused_kernel<<<256, 512, L_TOTAL, stream>>>(a);
}
```
